```python
import torch

def _softmax(x, axis=-1):
    return torch.softmax(x, dim=axis)
def _gelu(x):
    return torch.nn.functional.gelu(x, approximate="tanh")
def _silu(x):
    return torch.nn.functional.silu(x)
def _sigmoid(x):
    return torch.sigmoid(x)
def _layernorm(x, axis=-1, eps=1e-5):
    m = x.mean(dim=axis, keepdim=True)
    v = x.var(dim=axis, keepdim=True, unbiased=False)
    return (x - m) / torch.sqrt(v + eps)
def _scatter_add(tgt, idx, src, axis=0):
    return tgt.clone().index_add_(axis, idx, src)
def _segment_sum(src, seg, n):
    out = torch.zeros((n,) + src.shape[1:], dtype=src.dtype)
    return out.index_add_(0, seg, src)
def _pad(x, pw):
    flat = tuple(v for pair in reversed(pw) for v in pair)
    return torch.nn.functional.pad(x, flat)

kernel_name = 'standalone__mha_block_cached__near_ridge'

def setup_inputs():

    _w = (lambda a: (lambda t: t.to(torch.bfloat16) if t.is_floating_point() else t)(torch.as_tensor(a)))
    B, Sq, D = 32, 160, 576
    H, Sk, Dh = 32, 512, 128
    return {
        "x": _w((lambda *s: torch.randn(*s, dtype=torch.bfloat16) * 0.8)(B, Sq, D)),
        "k_cache": _w((lambda *s: torch.randn(*s, dtype=torch.bfloat16) * 0.8)(B, H, Sk, Dh)),
        "v_cache": _w((lambda *s: torch.randn(*s, dtype=torch.bfloat16) * 0.05)(B, H, Sk, Dh)),
        "wq": _w((lambda *s: torch.randn(*s, dtype=torch.bfloat16) * 0.8)(D, H * Dh) * (D ** -0.5)),
        "wo": _w((lambda *s: torch.randn(*s, dtype=torch.bfloat16) * 0.05)(H * Dh, D) * ((H * Dh) ** -0.5)),
    }

def reference(x, k_cache, v_cache, wq, wo):
    B, Sq, D = x.shape
    H, Dh = 32, 128
    q = (lambda x, *d: x.permute(*d))((x @ wq).reshape(B, Sq, H, Dh), 0, 2, 1, 3)
    scores = torch.einsum("bhqd,bhkd->bhqk", q, k_cache) * (Dh ** -0.5)
    p = _softmax(scores, axis=-1)
    o = torch.einsum("bhqk,bhkd->bhqd", p, v_cache)
    return (lambda x, *d: x.permute(*d))(o, 0, 2, 1, 3).reshape(B, Sq, H * Dh) @ wo
```

```cpp
#include <hip/hip_runtime.h>
#include <hip/hip_bf16.h>
#include <stdint.h>

typedef __bf16 bf16x8 __attribute__((ext_vector_type(8)));
typedef __bf16 bf16x4 __attribute__((ext_vector_type(4)));
typedef __bf16 bf16x2 __attribute__((ext_vector_type(2)));
typedef float f32x16 __attribute__((ext_vector_type(16)));
typedef float f32x2 __attribute__((ext_vector_type(2)));
typedef unsigned u32x4 __attribute__((ext_vector_type(4)));
typedef unsigned u32x2 __attribute__((ext_vector_type(2)));
typedef __attribute__((address_space(3))) bf16x4* lds_bf16x4_p;

constexpr int B_ = 32, SQ = 160, DM = 576, H_ = 32, SK = 512, DH = 128;
#define DI __device__ __forceinline__

DI unsigned cvtpk(float lo, float hi) {
  f32x2 v = {lo, hi};
  bf16x2 b = __builtin_convertvector(v, bf16x2);
  return __builtin_bit_cast(unsigned, b);
}
DI unsigned offb(unsigned row, unsigned ch) { return 256u * row + 16u * (ch ^ (((row & 3) << 2) | ((row >> 2) & 3))); }
DI bf16x4 trread(const char* lds, unsigned off) {
  return __builtin_amdgcn_ds_read_tr16_b64_v4bf16((lds_bf16x4_p)(lds + off));
}
DI bf16x8 cat(bf16x4 a, bf16x4 b) { return __builtin_shufflevector(a, b, 0, 1, 2, 3, 4, 5, 6, 7); }

__global__ __launch_bounds__(320, 2) void k1_attn(const uint16_t* __restrict__ x, const uint16_t* __restrict__ kc,
                                                  const uint16_t* __restrict__ vc, const uint16_t* __restrict__ wq,
                                                  uint16_t* __restrict__ o) {
  __shared__ __attribute__((aligned(16))) char lds[65536];
  const int tid = threadIdx.x, wid = tid >> 6, lane = tid & 63, r32 = lane & 31, hi = lane >> 5;
  const int h = blockIdx.x & 31, b = blockIdx.x >> 5;
  const int blk = (lane >> 4) & 1, q4 = (lane & 15) >> 2, p4 = lane & 3;
  f32x16 acc[4] = {};
  const uint16_t* xrow = x + ((size_t)b * SQ + wid * 32 + r32) * DM + hi * 8;
  for (int k0 = 0; k0 < DM; k0 += 64) {
    __syncthreads();
    for (int c = tid; c < 1024; c += 320) {
      int row = c >> 4, ch = c & 15;
      u32x4 v = *(const u32x4*)(wq + (size_t)(k0 + row) * (H_ * DH) + h * DH + ch * 8);
      *(u32x4*)(lds + offb(row, ch)) = v;
    }
    __syncthreads();
#pragma unroll
    for (int ks = 0; ks < 4; ++ks) {
      bf16x8 xb = *(const bf16x8*)(xrow + k0 + ks * 16);
#pragma unroll
      for (int c = 0; c < 4; ++c) {
        bf16x4 lo = trread(lds, offb(16 * ks + 8 * hi + q4, 4 * c + 2 * blk + (p4 & 1)) + 8 * (p4 >> 1));
        bf16x4 up = trread(lds, offb(16 * ks + 8 * hi + 4 + q4, 4 * c + 2 * blk + (p4 & 1)) + 8 * (p4 >> 1));
        acc[c] = __builtin_amdgcn_mfma_f32_32x32x16_bf16(cat(lo, up), xb, acc[c], 0, 0, 0);
      }
    }
  }
  bf16x8 qf[8];
#pragma unroll
  for (int c = 0; c < 4; ++c)
#pragma unroll
    for (int s = 0; s < 2; ++s) {
      u32x4 w = {cvtpk(acc[c][8 * s + 0], acc[c][8 * s + 1]), cvtpk(acc[c][8 * s + 2], acc[c][8 * s + 3]),
                 cvtpk(acc[c][8 * s + 4], acc[c][8 * s + 5]), cvtpk(acc[c][8 * s + 6], acc[c][8 * s + 7])};
      qf[2 * c + s] = __builtin_bit_cast(bf16x8, w);
    }
  const float C = 0.088388347648318440f * 1.4426950408889634f;
  const uint16_t* kh = kc + ((size_t)(b * H_ + h) * SK) * DH;
  const uint16_t* vh = vc + ((size_t)(b * H_ + h) * SK) * DH;
  char* kl = lds;
  char* vl = lds + 16384;
  f32x16 oacc[4] = {};
  float m = -1e30f, l = 0.f;
  for (int t = 0; t < SK / 64; ++t) {
    __syncthreads();
    for (int c = tid; c < 2048; c += 320) {
      int which = c >> 10, cc = c & 1023, row = cc >> 4, ch = cc & 15;
      const uint16_t* src = (which ? vh : kh) + (size_t)(t * 64 + row) * DH + ch * 8;
      u32x4 v = *(const u32x4*)src;
      *(u32x4*)(lds + which * 16384 + offb(row, ch)) = v;
    }
    __syncthreads();
    f32x16 s0 = {}, s1 = {};
#pragma unroll
    for (int s = 0; s < 8; ++s) {
      bf16x8 a0 = *(const bf16x8*)(kl + offb(r32, 2 * s + hi));
      bf16x8 a1 = *(const bf16x8*)(kl + offb(32 + r32, 2 * s + hi));
      s0 = __builtin_amdgcn_mfma_f32_32x32x16_bf16(a0, qf[s], s0, 0, 0, 0);
      s1 = __builtin_amdgcn_mfma_f32_32x32x16_bf16(a1, qf[s], s1, 0, 0, 0);
    }
    float pm = s0[0];
#pragma unroll
    for (int r = 1; r < 16; ++r) pm = fmaxf(pm, s0[r]);
#pragma unroll
    for (int r = 0; r < 16; ++r) pm = fmaxf(pm, s1[r]);
    pm = fmaxf(pm, __shfl_xor(pm, 32));
    float mn = fmaxf(m, pm);
    float alpha = __builtin_amdgcn_exp2f((m - mn) * C);
    m = mn;
    float mc = -mn * C;
    float ps = 0.f;
#pragma unroll
    for (int r = 0; r < 16; ++r) { s0[r] = __builtin_amdgcn_exp2f(fmaf(s0[r], C, mc)); ps += s0[r]; }
#pragma unroll
    for (int r = 0; r < 16; ++r) { s1[r] = __builtin_amdgcn_exp2f(fmaf(s1[r], C, mc)); ps += s1[r]; }
    l = l * alpha + ps;
#pragma unroll
    for (int c = 0; c < 4; ++c)
#pragma unroll
      for (int r = 0; r < 16; ++r) oacc[c][r] *= alpha;
    bf16x8 pf[4];
#pragma unroll
    for (int s = 0; s < 2; ++s) {
      u32x4 w0 = {cvtpk(s0[8 * s + 0], s0[8 * s + 1]), cvtpk(s0[8 * s + 2], s0[8 * s + 3]),
                  cvtpk(s0[8 * s + 4], s0[8 * s + 5]), cvtpk(s0[8 * s + 6], s0[8 * s + 7])};
      u32x4 w1 = {cvtpk(s1[8 * s + 0], s1[8 * s + 1]), cvtpk(s1[8 * s + 2], s1[8 * s + 3]),
                  cvtpk(s1[8 * s + 4], s1[8 * s + 5]), cvtpk(s1[8 * s + 6], s1[8 * s + 7])};
      pf[s] = __builtin_bit_cast(bf16x8, w0);
      pf[2 + s] = __builtin_bit_cast(bf16x8, w1);
    }
#pragma unroll
    for (int ks = 0; ks < 4; ++ks)
#pragma unroll
      for (int c = 0; c < 4; ++c) {
        bf16x4 lo = trread(vl, offb(16 * ks + 4 * hi + q4, 4 * c + 2 * blk + (p4 >> 1)) + 8 * (p4 & 1));
        bf16x4 up = trread(vl, offb(16 * ks + 8 + 4 * hi + q4, 4 * c + 2 * blk + (p4 >> 1)) + 8 * (p4 & 1));
        oacc[c] = __builtin_amdgcn_mfma_f32_32x32x16_bf16(cat(lo, up), pf[ks], oacc[c], 0, 0, 0);
      }
  }
  l += __shfl_xor(l, 32);
  float il = 1.f / l;
  uint16_t* orow = o + ((size_t)b * SQ + wid * 32 + r32) * (H_ * DH) + h * DH;
#pragma unroll
  for (int c = 0; c < 4; ++c)
#pragma unroll
    for (int g = 0; g < 4; ++g) {
      u32x2 w = {cvtpk(oacc[c][4 * g] * il, oacc[c][4 * g + 1] * il), cvtpk(oacc[c][4 * g + 2] * il, oacc[c][4 * g + 3] * il)};
      *(u32x2*)(orow + 32 * c + 8 * g + 4 * hi) = w;
    }
}

DI unsigned offc(unsigned row, unsigned ch) { return 128u * row + 16u * (ch ^ (row & 7)); }
__global__ __launch_bounds__(256, 2) void k2_out(const uint16_t* __restrict__ o, const uint16_t* __restrict__ wo,
                                                 uint16_t* __restrict__ out) {
  __shared__ __attribute__((aligned(16))) char lds[16384];
  const int tid = threadIdx.x, wid = tid >> 6, lane = tid & 63, r32 = lane & 31, hi = lane >> 5;
  const int blk = (lane >> 4) & 1, q4 = (lane & 15) >> 2, p4 = lane & 3;
  const int n0 = (blockIdx.x % 9) * 64, m0 = (blockIdx.x / 9) * 128;
  f32x16 acc[2] = {};
  const uint16_t* orow = o + (size_t)(m0 + wid * 32 + r32) * 4096 + hi * 8;
  for (int k0 = 0; k0 < 4096; k0 += 128) {
    __syncthreads();
    for (int c = tid; c < 1024; c += 256) {
      int row = c >> 3, ch = c & 7;
      u32x4 v = *(const u32x4*)(wo + (size_t)(k0 + row) * DM + n0 + ch * 8);
      *(u32x4*)(lds + offc(row, ch)) = v;
    }
    __syncthreads();
#pragma unroll
    for (int ks = 0; ks < 8; ++ks) {
      bf16x8 ob = *(const bf16x8*)(orow + k0 + ks * 16);
#pragma unroll
      for (int c = 0; c < 2; ++c) {
        bf16x4 lo = trread(lds, offc(16 * ks + 8 * hi + q4, 4 * c + 2 * blk + (p4 >> 1)) + 8 * (p4 & 1));
        bf16x4 up = trread(lds, offc(16 * ks + 8 * hi + 4 + q4, 4 * c + 2 * blk + (p4 >> 1)) + 8 * (p4 & 1));
        acc[c] = __builtin_amdgcn_mfma_f32_32x32x16_bf16(cat(lo, up), ob, acc[c], 0, 0, 0);
      }
    }
  }
  uint16_t* dst = out + (size_t)(m0 + wid * 32 + r32) * DM + n0;
#pragma unroll
  for (int c = 0; c < 2; ++c)
#pragma unroll
    for (int g = 0; g < 4; ++g) {
      u32x2 w = {cvtpk(acc[c][4 * g], acc[c][4 * g + 1]), cvtpk(acc[c][4 * g + 2], acc[c][4 * g + 3])};
      *(u32x2*)(dst + 32 * c + 8 * g + 4 * hi) = w;
    }
}

extern "C" void kernel_launch(void* const* d_in, const int* in_sizes, int n_in, void* d_out, int out_size, void* d_ws,
                              size_t ws_size, hipStream_t stream) {
  const uint16_t* x = (const uint16_t*)d_in[0];
  const uint16_t* kc = (const uint16_t*)d_in[1];
  const uint16_t* vc = (const uint16_t*)d_in[2];
  const uint16_t* wq = (const uint16_t*)d_in[3];
  const uint16_t* wo = (const uint16_t*)d_in[4];
  uint16_t* o = (uint16_t*)d_ws;
  k1_attn<<<B_ * H_, 320, 0, stream>>>(x, kc, vc, wq, o);
  k2_out<<<40 * 9, 256, 0, stream>>>(o, wo, (uint16_t*)d_out);
}
```

```cpp
#include <hip/hip_runtime.h>
#include <stdint.h>

__global__ __launch_bounds__(256, 1) void mha_block_fused(const void* x, const void* kc, const void* vc, const void* wq, const void* wo, void* out, void* ws) {
  __shared__ __attribute__((aligned(16))) char lds[163840];
  asm volatile(R"ASM(
.macro WL n
s_waitcnt lgkmcnt(\n)
.endm
.macro M0 s,i
s_add_u32 m0,s\s,\i
.endm
.macro DM v,s,o,i
buffer_load_dwordx4 v\v,s[\s:\s+3],s\o offen offset:\i lds
.endm
.macro TR d,v,o
ds_read_b64_tr_b16 a[\d:\d+1],v\v offset:\o
.endm
.macro RA d,v,o
ds_read_b128 a[\d:\d+3],v\v offset:\o
.endm
.macro Mvaaz d,a,b
v_mfma_f32_16x16x32_bf16 v[\d:\d+3],a[\a:\a+3],a[\b:\b+3],0
.endm
.macro Mvaa d,a,b
v_mfma_f32_16x16x32_bf16 v[\d:\d+3],a[\a:\a+3],a[\b:\b+3],v[\d:\d+3]
.endm
.macro DN v,s,o,i
buffer_load_dwordx4 v\v,s[\s:\s+3],s\o offen offset:\i nt lds
.endm
.macro CV d,a,b
v_cvt_pk_bf16_f32 v\d,v\a,v\b
.endm
.macro Mvavz d,a,b
v_mfma_f32_16x16x32_bf16 v[\d:\d+3],a[\a:\a+3],v[\b:\b+3],0
.endm
.macro Mvav d,a,b
v_mfma_f32_16x16x32_bf16 v[\d:\d+3],a[\a:\a+3],v[\b:\b+3],v[\d:\d+3]
.endm
.macro Mvavc d,a,b,c
v_mfma_f32_16x16x32_bf16 v[\d:\d+3],a[\a:\a+3],v[\b:\b+3],v[\c:\c+3]
.endm
.macro EX n
v_exp_f32 v\n,v\n
.endm
.macro Maavz d,a,b
v_mfma_f32_16x16x32_bf16 a[\d:\d+3],a[\a:\a+3],v[\b:\b+3],0
.endm
.macro Maav d,a,b
v_mfma_f32_16x16x32_bf16 a[\d:\d+3],a[\a:\a+3],v[\b:\b+3],a[\d:\d+3]
.endm
.macro AR d,a
v_accvgpr_read_b32 v\d,a\a
.endm
.macro Maaa d,a,b
v_mfma_f32_16x16x32_bf16 a[\d:\d+3],a[\a:\a+3],a[\b:\b+3],a[\d:\d+3]
.endm
.macro Mvvaz d,a,b
v_mfma_f32_16x16x32_bf16 v[\d:\d+3],v[\a:\a+3],a[\b:\b+3],0
.endm
.macro Mvva d,a,b
v_mfma_f32_16x16x32_bf16 v[\d:\d+3],v[\a:\a+3],a[\b:\b+3],v[\d:\d+3]
.endm
.macro Mava d,a,b
v_mfma_f32_16x16x32_bf16 a[\d:\d+3],v[\a:\a+3],a[\b:\b+3],a[\d:\d+3]
.endm
.macro Mavv d,a,b
v_mfma_f32_16x16x32_bf16 a[\d:\d+3],v[\a:\a+3],v[\b:\b+3],a[\d:\d+3]
.endm
.macro Maaaz d,a,b
v_mfma_f32_16x16x32_bf16 a[\d:\d+3],a[\a:\a+3],a[\b:\b+3],0
.endm
s_load_dwordx8 s[8:15],%0,0
s_load_dwordx4 s[16:19],%0,32
s_load_dwordx2 s[20:21],%0,48
s_mov_b32 s22,%1
L_begin:
s_getpc_b64 s[60:61]
v_and_b32 v1,63,%2
s_and_b32 s61,s61,0xffff
s_mov_b32 s62,L_end-L_begin-8
s_mov_b32 s63,0x20000
s_bitcmp1_b32 s22,4
s_cselect_b32 %3,LB_body-L_begin,0
s_cselect_b32 %1,512,0
v_lshlrev_b32 v8,6,%2
v_add_u32 v8,%3,v8
buffer_load_dword v8,v8,s[60:63],0 offen
v_lshrrev_b32 v27,6,%2
s_nop 0
v_readfirstlane_b32 s64,v27
s_nop 0
s_and_b32 s65,s64,1
s_lshr_b32 s66,s64,1
s_and_b32 s90,s22,3
s_bfe_u32 s68,s22,0x10003
s_lshl_b32 s68,s68,2
s_add_u32 s68,s68,s90
s_lshr_b32 s90,s22,4
s_lshl_b32 s90,s90,1
s_bfe_u32 s67,s22,0x10002
s_add_u32 s67,s67,s90
s_mov_b32 s85,0
s_branch L_go
L_restart:
s_load_dwordx8 s[8:15],%0,0
s_load_dwordx4 s[16:19],%0,32
s_load_dwordx2 s[20:21],%0,48
L_go:
s_mov_b32 s27,0x20000
s_mov_b32 s31,0x20000
s_mov_b32 s35,0x20000
s_mov_b32 s39,0x20000
s_mov_b32 s43,0x20000
s_mov_b32 s51,0x20000
s_mov_b32 s55,0x20000
s_mov_b32 s59,0x20000
s_lshl_b32 s90,s68,2
s_add_u32 s90,s90,s66
s_lshl_b32 s91,s90,8
s_mul_i32 s92,s67,184320
s_mul_i32 s77,s66,8656
s_mul_i32 s93,s65,4360
s_add_u32 s77,s77,s93
s_lshl_b32 s69,s64,10
v_lshrrev_b32 v197,4,v1
v_lshrrev_b32 v206,5,v1
v_and_b32 v200,1,v197
v_and_b32 v201,15,v1
v_lshlrev_b32 v199,4,v206
v_lshl_add_u32 v199,v200,2,v199
s_lshl_b32 s93,s65,3
v_add_u32 v199,s93,v199
v_lshlrev_b32 v199,13,v199
v_lshl_add_u32 v20,v201,4,v199
v_lshrrev_b32 v196,3,v1
v_and_b32 v198,6,v196
v_xor_b32 v198,v198,v1
v_and_b32 v198,7,v198
s_lshl_b32 s93,s64,3
v_add_u32 v210,s93,v196
v_mul_u32_u24 v210,0x480,v210
v_lshl_add_u32 v21,v198,4,v210
WL 0
s_add_u32 s28,s14,s91
s_addc_u32 s29,s15,0
s_and_b32 s29,s29,0xffff
s_sub_u32 s30,4718592,s91
s_add_u32 s24,s8,s92
s_addc_u32 s25,s9,0
s_and_b32 s25,s25,0xffff
s_mov_b32 s26,184320
s_add_u32 s79,%1,0
M0 77,89728
s_nop 0
DM 20,28,79,0
s_add_u32 s79,%1,8192
M0 77,90816
s_nop 0
DM 20,28,79,0
s_add_u32 s79,%1,16384
M0 77,91904
s_nop 0
DM 20,28,79,0
s_add_u32 s79,%1,24576
M0 77,92992
s_nop 0
DM 20,28,79,0
s_mov_b32 s79,0
M0 69,0
s_nop 0
DM 21,24,79,0
s_mov_b32 s79,36864
M0 69,4096
s_nop 0
DM 21,24,79,0
s_mov_b32 s79,73728
M0 69,8192
s_nop 0
DM 21,24,79,0
s_mov_b32 s79,110592
M0 69,12288
s_nop 0
DM 21,24,79,0
s_mov_b32 s79,147456
M0 69,16384
s_nop 0
DM 21,24,79,0
s_add_u32 s79,%1,262144
M0 77,107040
s_nop 0
DM 20,28,79,0
s_add_u32 s79,%1,270336
M0 77,108128
s_nop 0
DM 20,28,79,0
s_add_u32 s79,%1,278528
M0 77,109216
s_nop 0
DM 20,28,79,0
s_add_u32 s79,%1,286720
M0 77,110304
s_nop 0
DM 20,28,79,0
s_mov_b32 s79,128
M0 69,141664
s_nop 0
DM 21,24,79,0
s_mov_b32 s79,36992
M0 69,145760
s_nop 0
DM 21,24,79,0
s_mov_b32 s79,73856
M0 69,149856
s_nop 0
DM 21,24,79,0
s_mov_b32 s79,110720
M0 69,153952
s_nop 0
DM 21,24,79,0
s_mov_b32 s79,147584
M0 69,158048
s_nop 0
DM 21,24,79,0
s_add_u32 s79,%1,524288
M0 77,124352
s_nop 0
DM 20,28,79,0
s_add_u32 s79,%1,532480
M0 77,125440
s_nop 0
DM 20,28,79,0
s_add_u32 s79,%1,540672
M0 77,126528
s_nop 0
DM 20,28,79,0
s_add_u32 s79,%1,548864
M0 77,127616
s_nop 0
DM 20,28,79,0
s_lshl_b32 s91,s67,3
s_add_u32 s91,s91,s68
s_lshl_b32 s91,s91,19
s_add_u32 s32,s10,s91
s_addc_u32 s33,s11,0
s_and_b32 s33,s33,0xffff
s_add_u32 s36,s12,s91
s_addc_u32 s37,s13,0
s_and_b32 s37,s37,0xffff
s_mov_b32 s34,524288
s_mov_b32 s38,524288
s_mul_i32 s78,s64,1056
v_lshlrev_b32 v2,4,v1
s_lshl_b32 s93,s64,10
v_add_u32 v19,s93,v2
v_add_u32 v33,4096,v19
s_mul_i32 s80,s67,8192
s_mul_i32 s93,s68,16384
s_add_u32 s80,s80,s93
s_add_u32 s52,s18,s92
s_addc_u32 s53,s19,0
s_and_b32 s53,s53,0xffff
s_mov_b32 s54,184320
s_mul_i32 s91,s68,589824
s_add_u32 s40,s16,s91
s_addc_u32 s41,s17,0
s_and_b32 s41,s41,0xffff
s_mov_b32 s42,589824
s_mul_i32 s91,s67,1310720
s_add_u32 s91,s91,1048576
s_add_u32 s48,s20,s91
s_addc_u32 s49,s21,0
s_and_b32 s49,s49,0xffff
s_mov_b32 s50,1310720
s_lshl_b32 s91,s67,9
s_add_u32 s56,s20,s91
s_addc_u32 s57,s21,0
s_and_b32 s57,s57,0xffff
s_mov_b32 s58,512
buffer_load_dwordx2 v[28:29],off,s[56:59],0 offset:256 sc1
s_lshl_b32 s71,s64,11
s_add_u32 s71,s71,%3
s_mul_i32 s74,s68,163840
s_mul_i32 s91,s64,5120
s_add_u32 s74,s74,s91
s_mul_i32 s82,s64,4224
s_mov_b32 s84,0x3e0293ee
s_mov_b32 s75,0x5d800000
s_mov_b64 s[86:87],0
s_lshl_b32 s93,s64,3
v_add_u32 v210,s93,v196
v_mul_u32_u24 v210,0x1200,v210
v_and_b32 v202,7,v1
v_lshl_add_u32 v7,v202,4,v210
v_mul_u32_u24 v209,0x420,v202
v_bfe_u32 v203,v1,3,1
v_lshl_add_u32 v209,v203,8,v209
v_lshl_add_u32 v209,v197,4,v209
s_mul_i32 s90,s66,8448
s_add_u32 s91,s90,20480
s_add_u32 s92,s90,89728
v_add_u32 v10,s91,v209
v_add_u32 v11,s92,v209
v_bfe_u32 v204,v1,2,3
v_mul_u32_u24 v205,0x420,v204
v_lshl_add_u32 v205,v206,8,v205
v_and_b32 v207,3,v1
v_lshl_add_u32 v205,v207,3,v205
v_add_u32 v12,s91,v205
v_add_u32 v13,s92,v205
v_mul_u32_u24 v205,0x440,v204
v_lshl_add_u32 v205,v200,3,v205
v_lshl_add_u32 v205,v206,9,v205
v_lshl_add_u32 v205,v207,4,v205
s_mul_i32 s90,s66,8656
s_add_u32 s92,s90,89728
v_add_u32 v14,s92,v205
v_and_b32 v208,6,v1
v_xor_b32 v208,v208,v197
v_lshlrev_b32 v208,4,v208
v_lshl_add_u32 v208,v201,7,v208
v_xor_b32 v209,64,v208
s_mul_i32 s90,s65,10240
s_add_u32 s91,s90,141664
v_add_u32 v15,s90,v208
v_add_u32 v16,s90,v209
v_add_u32 v17,s91,v208
v_add_u32 v18,s91,v209
v_bfe_u32 v205,v1,2,2
v_mul_u32_u24 v205,0x420,v205
v_lshl_add_u32 v205,v197,7,v205
v_lshl_add_u32 v205,v207,3,v205
s_mul_i32 s90,s66,17376
s_add_u32 s91,s90,20480
s_add_u32 s92,s90,89728
v_add_u32 v3,s91,v205
v_add_u32 v4,s92,v205
s_mul_i32 s90,s66,17248
s_sub_u32 s91,37792,s90
s_sub_u32 s92,107040,s90
v_add_u32 v5,s91,v205
v_add_u32 v6,s92,v205
v_mov_b32 v209,0x3f803f80
v_accvgpr_write_b32 a212,v209
v_accvgpr_write_b32 a213,v209
v_accvgpr_write_b32 a214,v209
v_accvgpr_write_b32 a215,v209
v_mov_b32 v22,0xff800000
v_mov_b32 v23,0xff800000
v_mov_b32 v24,0xff800000
v_mov_b32 v25,0xff800000
v_mov_b32 v26,0xff800000
s_cmp_eq_u32 %3,0
s_cbranch_scc1 LC_body
s_getpc_b64 s[90:91]
L_jp:
s_add_u32 s90,s90,LB_body-L_jp
s_addc_u32 s91,s91,0
s_setpc_b64 s[90:91]
LC_body:
s_waitcnt vmcnt(14) lgkmcnt(0)
s_barrier
TR 180,14,0
TR 182,14,256
RA 0,15,0
RA 4,15,2048
RA 8,15,4096
RA 12,15,6144
RA 16,15,8192
TR 184,14,8
TR 186,14,264
TR 188,14,64
TR 190,14,320
TR 192,14,72
TR 194,14,328
TR 196,14,128
TR 198,14,384
WL 12
Mvaaz 36,180,0
s_nop 0
TR 200,14,136
TR 202,14,392
WL 13
Mvaaz 52,180,4
WL 12
Mvaaz 68,180,8
WL 11
Mvaaz 84,180,12
WL 10
Mvaaz 100,180,16
WL 8
Mvaaz 116,184,0
s_nop 0
TR 204,14,192
TR 206,14,448
Mvaaz 120,184,4
Mvaaz 124,184,8
Mvaaz 128,184,12
Mvaaz 132,184,16
WL 6
Mvaaz 40,188,0
s_nop 0
TR 208,14,200
TR 210,14,456
Mvaaz 56,188,4
Mvaaz 72,188,8
Mvaaz 88,188,12
Mvaaz 104,188,16
Mvaaz 136,192,0
Mvaaz 140,192,4
Mvaaz 144,192,8
Mvaaz 148,192,12
Mvaaz 152,192,16
WL 4
Mvaaz 44,196,0
s_waitcnt vmcnt(10) lgkmcnt(0)
s_barrier
Mvaaz 60,196,4
s_nop 0
TR 180,14,17312
TR 182,14,17568
Mvaaz 76,196,8
RA 20,16,0
Mvaaz 92,196,12
RA 24,16,2048
Mvaaz 108,196,16
RA 28,16,4096
Mvaaz 156,200,0
s_mov_b32 s79,786432
RA 32,16,6144
Mvaaz 160,200,4
M0 77,89728
RA 36,16,8192
Mvaaz 164,200,8
s_nop 0
TR 184,14,17320
TR 186,14,17576
Mvaaz 168,200,12
s_nop 0
TR 188,14,17376
TR 190,14,17632
Mvaaz 172,200,16
DM 20,28,79,0
Mvaaz 48,204,0
s_mov_b32 s79,794624
TR 192,14,17384
TR 194,14,17640
Mvaaz 64,204,4
M0 77,90816
Mvaaz 80,204,8
DM 20,28,79,0
Mvaaz 96,204,12
s_mov_b32 s79,802816
Mvaaz 112,204,16
M0 77,91904
Mvaaz 176,208,0
s_nop 0
TR 196,14,17440
TR 198,14,17696
Mvaaz 180,208,4
DM 20,28,79,0
Mvaaz 184,208,8
s_mov_b32 s79,811008
Mvaaz 188,208,12
M0 77,92992
Mvaaz 192,208,16
WL 6
DM 20,28,79,0
Mvaa 36,180,20
s_nop 0
TR 200,14,17448
TR 202,14,17704
Mvaa 52,180,24
Mvaa 68,180,28
Mvaa 84,180,32
Mvaa 100,180,36
Mvaa 116,184,20
s_nop 0
TR 204,14,17504
TR 206,14,17760
Mvaa 120,184,24
Mvaa 124,184,28
Mvaa 128,184,32
Mvaa 132,184,36
WL 6
Mvaa 40,188,20
s_nop 0
TR 208,14,17512
TR 210,14,17768
Mvaa 56,188,24
Mvaa 72,188,28
Mvaa 88,188,32
Mvaa 104,188,36
Mvaa 136,192,20
Mvaa 140,192,24
Mvaa 144,192,28
Mvaa 148,192,32
Mvaa 152,192,36
WL 4
Mvaa 44,196,20
s_waitcnt vmcnt(5) lgkmcnt(0)
s_barrier
Mvaa 60,196,24
s_add_u32 s98,s71,16384
TR 180,14,34624
TR 182,14,34880
Mvaa 76,196,28
RA 0,17,0
v_lshl_add_u32 v8,v2,1,s98
Mvaa 92,196,32
RA 4,17,2048
Mvaa 108,196,36
RA 8,17,4096
Mvaa 156,200,20
s_mov_b32 s79,256
RA 12,17,6144
Mvaa 160,200,24
M0 69,0
RA 16,17,8192
Mvaa 164,200,28
s_nop 0
TR 184,14,34632
TR 186,14,34888
Mvaa 168,200,32
s_nop 0
TR 188,14,34688
TR 190,14,34944
Mvaa 172,200,36
DM 21,24,79,0
Mvaa 48,204,20
s_mov_b32 s79,37120
TR 192,14,34696
TR 194,14,34952
Mvaa 64,204,24
M0 69,4096
buffer_load_dword v8,v8,s[60:63],0 offen
Mvaa 80,204,28
DM 21,24,79,0
Mvaa 96,204,32
s_mov_b32 s79,73984
Mvaa 112,204,36
M0 69,8192
Mvaa 176,208,20
s_nop 0
TR 196,14,34752
TR 198,14,35008
Mvaa 180,208,24
DM 21,24,79,0
Mvaa 184,208,28
s_mov_b32 s79,110848
Mvaa 188,208,32
M0 69,12288
Mvaa 192,208,36
WL 6
DM 21,24,79,0
Mvaa 36,180,0
s_mov_b32 s79,147712
TR 200,14,34760
TR 202,14,35016
Mvaa 52,180,4
M0 69,16384
Mvaa 68,180,8
DM 21,24,79,0
Mvaa 84,180,12
s_mov_b32 s79,1048576
Mvaa 100,180,16
M0 77,107040
Mvaa 116,184,0
s_nop 0
TR 204,14,34816
TR 206,14,35072
Mvaa 120,184,4
DM 20,28,79,0
Mvaa 124,184,8
s_mov_b32 s79,1056768
Mvaa 128,184,12
M0 77,108128
Mvaa 132,184,16
WL 6
DM 20,28,79,0
Mvaa 40,188,0
s_mov_b32 s79,1064960
TR 208,14,34824
TR 210,14,35080
Mvaa 56,188,4
M0 77,109216
Mvaa 72,188,8
DM 20,28,79,0
Mvaa 88,188,12
s_mov_b32 s79,1073152
Mvaa 104,188,16
M0 77,110304
Mvaa 136,192,0
DM 20,28,79,0
Mvaa 140,192,4
Mvaa 144,192,8
Mvaa 148,192,12
Mvaa 152,192,16
WL 4
Mvaa 44,196,0
s_waitcnt vmcnt(10) lgkmcnt(0)
s_barrier
Mvaa 60,196,4
s_nop 0
TR 180,14,0
TR 182,14,256
Mvaa 76,196,8
RA 20,18,0
Mvaa 92,196,12
s_add_u32 s4,s80,0
RA 24,18,2048
Mvaa 108,196,16
s_and_b32 s4,s4,0x1e000
RA 28,18,4096
Mvaa 156,200,0
s_xor_b32 s5,s4,0x30000
RA 32,18,6144
Mvaa 160,200,4
M0 78,20480
RA 36,18,8192
Mvaa 164,200,8
s_nop 0
TR 184,14,8
TR 186,14,264
Mvaa 168,200,12
s_nop 0
TR 188,14,64
TR 190,14,320
Mvaa 172,200,16
DN 19,32,4,0
Mvaa 48,204,0
s_mov_b32 s79,1310720
TR 192,14,72
TR 194,14,328
Mvaa 64,204,4
M0 77,124352
Mvaa 80,204,8
DM 20,28,79,0
Mvaa 96,204,12
s_mov_b32 s79,1318912
Mvaa 112,204,16
M0 77,125440
Mvaa 176,208,0
s_nop 0
TR 196,14,128
TR 198,14,384
Mvaa 180,208,4
DM 20,28,79,0
Mvaa 184,208,8
s_mov_b32 s79,1327104
Mvaa 188,208,12
M0 77,126528
Mvaa 192,208,16
WL 6
DM 20,28,79,0
Mvaa 36,180,20
s_mov_b32 s79,1335296
TR 200,14,136
TR 202,14,392
Mvaa 52,180,24
M0 77,127616
Mvaa 68,180,28
DM 20,28,79,0
Mvaa 84,180,32
Mvaa 100,180,36
Mvaa 116,184,20
s_nop 0
TR 204,14,192
TR 206,14,448
Mvaa 120,184,24
Mvaa 124,184,28
Mvaa 128,184,32
M0 78,24704
Mvaa 132,184,36
WL 6
DN 33,32,4,0
Mvaa 40,188,20
s_nop 0
TR 208,14,200
TR 210,14,456
Mvaa 56,188,24
Mvaa 72,188,28
Mvaa 88,188,32
Mvaa 104,188,36
Mvaa 136,192,20
Mvaa 140,192,24
Mvaa 144,192,28
Mvaa 148,192,32
Mvaa 152,192,36
WL 4
Mvaa 44,196,20
s_waitcnt vmcnt(6) lgkmcnt(0)
s_barrier
Mvaa 60,196,24
s_nop 0
TR 180,14,17312
TR 182,14,17568
Mvaa 76,196,28
RA 0,15,0
Mvaa 92,196,32
RA 4,15,2048
Mvaa 108,196,36
RA 8,15,4096
Mvaa 156,200,20
s_mov_b32 s79,384
RA 12,15,6144
Mvaa 160,200,24
M0 69,141664
RA 16,15,8192
Mvaa 164,200,28
s_nop 0
TR 184,14,17320
TR 186,14,17576
Mvaa 168,200,32
s_nop 0
TR 188,14,17376
TR 190,14,17632
Mvaa 172,200,36
DM 21,24,79,0
Mvaa 48,204,20
s_mov_b32 s79,37248
TR 192,14,17384
TR 194,14,17640
Mvaa 64,204,24
M0 69,145760
Mvaa 80,204,28
DM 21,24,79,0
Mvaa 96,204,32
s_mov_b32 s79,74112
Mvaa 112,204,36
M0 69,149856
Mvaa 176,208,20
s_nop 0
TR 196,14,17440
TR 198,14,17696
Mvaa 180,208,24
DM 21,24,79,0
Mvaa 184,208,28
s_mov_b32 s79,110976
Mvaa 188,208,32
M0 69,153952
Mvaa 192,208,36
WL 6
DM 21,24,79,0
v_readfirstlane_b32 s90,v28
v_readfirstlane_b32 s91,v29
s_nop 0
s_add_u32 s90,s90,0x7f4a7c15
s_addc_u32 s91,s91,0x9e3779b9
s_and_b32 s90,s90,0xffffffe0
s_cmp_eq_u32 s85,0
s_cselect_b64 s[88:89],s[90:91],s[88:89]
Mvaa 36,180,0
s_mov_b32 s79,147840
TR 200,14,17448
TR 202,14,17704
Mvaa 52,180,4
M0 69,158048
Mvaa 68,180,8
DM 21,24,79,0
Mvaa 84,180,12
s_mov_b32 s79,1572864
Mvaa 100,180,16
M0 77,89728
Mvaa 116,184,0
s_nop 0
TR 204,14,17504
TR 206,14,17760
Mvaa 120,184,4
DM 20,28,79,0
Mvaa 124,184,8
s_mov_b32 s79,1581056
Mvaa 128,184,12
M0 77,90816
Mvaa 132,184,16
WL 6
DM 20,28,79,0
Mvaa 40,188,0
s_mov_b32 s79,1589248
TR 208,14,17512
TR 210,14,17768
Mvaa 56,188,4
M0 77,91904
Mvaa 72,188,8
DM 20,28,79,0
Mvaa 88,188,12
s_mov_b32 s79,1597440
Mvaa 104,188,16
M0 77,92992
Mvaa 136,192,0
DM 20,28,79,0
Mvaa 140,192,4
M0 78,28928
Mvaa 144,192,8
Mvaa 148,192,12
DN 19,32,5,0
Mvaa 152,192,16
WL 4
Mvaa 44,196,0
s_waitcnt vmcnt(11) lgkmcnt(0)
s_barrier
Mvaa 60,196,4
s_nop 0
TR 180,14,34624
TR 182,14,34880
Mvaa 76,196,8
RA 20,16,0
Mvaa 92,196,12
RA 24,16,2048
Mvaa 108,196,16
RA 28,16,4096
Mvaa 156,200,0
s_mov_b32 s79,1835008
RA 32,16,6144
Mvaa 160,200,4
M0 77,107040
RA 36,16,8192
Mvaa 164,200,8
s_nop 0
TR 184,14,34632
TR 186,14,34888
Mvaa 168,200,12
s_nop 0
TR 188,14,34688
TR 190,14,34944
Mvaa 172,200,16
DM 20,28,79,0
Mvaa 48,204,0
s_mov_b32 s79,1843200
TR 192,14,34696
TR 194,14,34952
Mvaa 64,204,4
M0 77,108128
Mvaa 80,204,8
DM 20,28,79,0
Mvaa 96,204,12
s_mov_b32 s79,1851392
Mvaa 112,204,16
M0 77,109216
Mvaa 176,208,0
s_nop 0
TR 196,14,34752
TR 198,14,35008
Mvaa 180,208,4
DM 20,28,79,0
Mvaa 184,208,8
s_mov_b32 s79,1859584
Mvaa 188,208,12
M0 77,110304
Mvaa 192,208,16
WL 6
DM 20,28,79,0
Mvaa 36,180,20
s_nop 0
TR 200,14,34760
TR 202,14,35016
Mvaa 52,180,24
Mvaa 68,180,28
Mvaa 84,180,32
Mvaa 100,180,36
Mvaa 116,184,20
s_nop 0
TR 204,14,34816
TR 206,14,35072
Mvaa 120,184,24
Mvaa 124,184,28
Mvaa 128,184,32
M0 78,33152
Mvaa 132,184,36
WL 6
DN 33,32,5,0
Mvaa 40,188,20
s_nop 0
TR 208,14,34824
TR 210,14,35080
Mvaa 56,188,24
Mvaa 72,188,28
Mvaa 88,188,32
Mvaa 104,188,36
Mvaa 136,192,20
Mvaa 140,192,24
Mvaa 144,192,28
Mvaa 148,192,32
Mvaa 152,192,36
WL 4
Mvaa 44,196,20
s_waitcnt vmcnt(6) lgkmcnt(0)
s_barrier
Mvaa 60,196,24
s_nop 0
TR 180,14,0
TR 182,14,256
Mvaa 76,196,28
RA 0,17,0
Mvaa 92,196,32
RA 4,17,2048
Mvaa 108,196,36
RA 8,17,4096
Mvaa 156,200,20
s_mov_b32 s79,512
RA 12,17,6144
Mvaa 160,200,24
M0 69,0
RA 16,17,8192
Mvaa 164,200,28
s_nop 0
TR 184,14,8
TR 186,14,264
Mvaa 168,200,32
s_nop 0
TR 188,14,64
TR 190,14,320
Mvaa 172,200,36
DM 21,24,79,0
Mvaa 48,204,20
s_mov_b32 s79,37376
TR 192,14,72
TR 194,14,328
Mvaa 64,204,24
M0 69,4096
Mvaa 80,204,28
DM 21,24,79,0
Mvaa 96,204,32
s_mov_b32 s79,74240
Mvaa 112,204,36
M0 69,8192
Mvaa 176,208,20
s_nop 0
TR 196,14,128
TR 198,14,384
Mvaa 180,208,24
DM 21,24,79,0
Mvaa 184,208,28
s_mov_b32 s79,111104
Mvaa 188,208,32
M0 69,12288
Mvaa 192,208,36
WL 6
DM 21,24,79,0
Mvaa 36,180,0
s_mov_b32 s79,147968
TR 200,14,136
TR 202,14,392
Mvaa 52,180,4
M0 69,16384
Mvaa 68,180,8
DM 21,24,79,0
Mvaa 84,180,12
s_mov_b32 s79,2097152
Mvaa 100,180,16
M0 77,124352
Mvaa 116,184,0
s_nop 0
TR 204,14,192
TR 206,14,448
Mvaa 120,184,4
DM 20,28,79,0
Mvaa 124,184,8
s_mov_b32 s79,2105344
Mvaa 128,184,12
M0 77,125440
Mvaa 132,184,16
WL 6
DM 20,28,79,0
Mvaa 40,188,0
s_mov_b32 s79,2113536
TR 208,14,200
TR 210,14,456
Mvaa 56,188,4
M0 77,126528
Mvaa 72,188,8
DM 20,28,79,0
Mvaa 88,188,12
s_mov_b32 s79,2121728
Mvaa 104,188,16
M0 77,127616
Mvaa 136,192,0
DM 20,28,79,0
Mvaa 140,192,4
Mvaa 144,192,8
Mvaa 148,192,12
Mvaa 152,192,16
WL 4
Mvaa 44,196,0
s_waitcnt vmcnt(10) lgkmcnt(0)
s_barrier
Mvaa 60,196,4
s_nop 0
TR 180,14,17312
TR 182,14,17568
Mvaa 76,196,8
RA 20,18,0
Mvaa 92,196,12
s_add_u32 s6,s80,8192
RA 24,18,2048
Mvaa 108,196,16
s_and_b32 s6,s6,0x1e000
RA 28,18,4096
Mvaa 156,200,0
s_xor_b32 s7,s6,0x30000
RA 32,18,6144
Mvaa 160,200,4
M0 78,37792
RA 36,18,8192
Mvaa 164,200,8
s_nop 0
TR 184,14,17320
TR 186,14,17576
Mvaa 168,200,12
s_nop 0
TR 188,14,17376
TR 190,14,17632
Mvaa 172,200,16
DN 19,32,6,0
Mvaa 48,204,0
s_mov_b32 s79,2359296
TR 192,14,17384
TR 194,14,17640
Mvaa 64,204,4
M0 77,89728
Mvaa 80,204,8
DM 20,28,79,0
Mvaa 96,204,12
s_mov_b32 s79,2367488
Mvaa 112,204,16
M0 77,90816
Mvaa 176,208,0
s_nop 0
TR 196,14,17440
TR 198,14,17696
Mvaa 180,208,4
DM 20,28,79,0
Mvaa 184,208,8
s_mov_b32 s79,2375680
Mvaa 188,208,12
M0 77,91904
Mvaa 192,208,16
WL 6
DM 20,28,79,0
Mvaa 36,180,20
s_mov_b32 s79,2383872
TR 200,14,17448
TR 202,14,17704
Mvaa 52,180,24
M0 77,92992
Mvaa 68,180,28
DM 20,28,79,0
Mvaa 84,180,32
Mvaa 100,180,36
Mvaa 116,184,20
s_nop 0
TR 204,14,17504
TR 206,14,17760
Mvaa 120,184,24
Mvaa 124,184,28
Mvaa 128,184,32
M0 78,42016
Mvaa 132,184,36
WL 6
DN 33,32,6,0
Mvaa 40,188,20
s_nop 0
TR 208,14,17512
TR 210,14,17768
Mvaa 56,188,24
Mvaa 72,188,28
Mvaa 88,188,32
Mvaa 104,188,36
Mvaa 136,192,20
Mvaa 140,192,24
Mvaa 144,192,28
Mvaa 148,192,32
Mvaa 152,192,36
WL 4
Mvaa 44,196,20
s_waitcnt vmcnt(6) lgkmcnt(0)
s_barrier
Mvaa 60,196,24
s_nop 0
TR 180,14,34624
TR 182,14,34880
Mvaa 76,196,28
RA 0,15,0
Mvaa 92,196,32
RA 4,15,2048
Mvaa 108,196,36
RA 8,15,4096
Mvaa 156,200,20
s_mov_b32 s79,640
RA 12,15,6144
Mvaa 160,200,24
M0 69,141664
RA 16,15,8192
Mvaa 164,200,28
s_nop 0
TR 184,14,34632
TR 186,14,34888
Mvaa 168,200,32
s_nop 0
TR 188,14,34688
TR 190,14,34944
Mvaa 172,200,36
DM 21,24,79,0
Mvaa 48,204,20
s_mov_b32 s79,37504
TR 192,14,34696
TR 194,14,34952
Mvaa 64,204,24
M0 69,145760
Mvaa 80,204,28
DM 21,24,79,0
Mvaa 96,204,32
s_mov_b32 s79,74368
Mvaa 112,204,36
M0 69,149856
Mvaa 176,208,20
s_nop 0
TR 196,14,34752
TR 198,14,35008
Mvaa 180,208,24
DM 21,24,79,0
Mvaa 184,208,28
s_mov_b32 s79,111232
Mvaa 188,208,32
M0 69,153952
Mvaa 192,208,36
WL 6
DM 21,24,79,0
Mvaa 36,180,0
s_mov_b32 s79,148096
TR 200,14,34760
TR 202,14,35016
Mvaa 52,180,4
M0 69,158048
Mvaa 68,180,8
DM 21,24,79,0
Mvaa 84,180,12
s_mov_b32 s79,2621440
Mvaa 100,180,16
M0 77,107040
Mvaa 116,184,0
s_nop 0
TR 204,14,34816
TR 206,14,35072
Mvaa 120,184,4
DM 20,28,79,0
Mvaa 124,184,8
s_mov_b32 s79,2629632
Mvaa 128,184,12
M0 77,108128
Mvaa 132,184,16
WL 6
DM 20,28,79,0
Mvaa 40,188,0
s_mov_b32 s79,2637824
TR 208,14,34824
TR 210,14,35080
Mvaa 56,188,4
M0 77,109216
Mvaa 72,188,8
DM 20,28,79,0
Mvaa 88,188,12
s_mov_b32 s79,2646016
Mvaa 104,188,16
M0 77,110304
Mvaa 136,192,0
DM 20,28,79,0
Mvaa 140,192,4
M0 78,46240
Mvaa 144,192,8
Mvaa 148,192,12
DN 19,32,7,0
Mvaa 152,192,16
WL 4
Mvaa 44,196,0
s_waitcnt vmcnt(11) lgkmcnt(0)
s_barrier
Mvaa 60,196,4
s_nop 0
TR 180,14,0
TR 182,14,256
Mvaa 76,196,8
RA 20,16,0
Mvaa 92,196,12
RA 24,16,2048
Mvaa 108,196,16
RA 28,16,4096
Mvaa 156,200,0
s_mov_b32 s79,2883584
RA 32,16,6144
Mvaa 160,200,4
M0 77,124352
RA 36,16,8192
Mvaa 164,200,8
s_nop 0
TR 184,14,8
TR 186,14,264
Mvaa 168,200,12
s_nop 0
TR 188,14,64
TR 190,14,320
Mvaa 172,200,16
DM 20,28,79,0
Mvaa 48,204,0
s_mov_b32 s79,2891776
TR 192,14,72
TR 194,14,328
Mvaa 64,204,4
M0 77,125440
Mvaa 80,204,8
DM 20,28,79,0
Mvaa 96,204,12
s_mov_b32 s79,2899968
Mvaa 112,204,16
M0 77,126528
Mvaa 176,208,0
s_nop 0
TR 196,14,128
TR 198,14,384
Mvaa 180,208,4
DM 20,28,79,0
Mvaa 184,208,8
s_mov_b32 s79,2908160
Mvaa 188,208,12
M0 77,127616
Mvaa 192,208,16
WL 6
DM 20,28,79,0
Mvaa 36,180,20
s_nop 0
TR 200,14,136
TR 202,14,392
Mvaa 52,180,24
Mvaa 68,180,28
Mvaa 84,180,32
Mvaa 100,180,36
Mvaa 116,184,20
s_nop 0
TR 204,14,192
TR 206,14,448
Mvaa 120,184,24
Mvaa 124,184,28
Mvaa 128,184,32
M0 78,50464
Mvaa 132,184,36
WL 6
DN 33,32,7,0
Mvaa 40,188,20
s_nop 0
TR 208,14,200
TR 210,14,456
Mvaa 56,188,24
Mvaa 72,188,28
Mvaa 88,188,32
Mvaa 104,188,36
Mvaa 136,192,20
Mvaa 140,192,24
Mvaa 144,192,28
Mvaa 148,192,32
Mvaa 152,192,36
WL 4
Mvaa 44,196,20
s_waitcnt vmcnt(6) lgkmcnt(0)
s_barrier
Mvaa 60,196,24
s_nop 0
TR 180,14,17312
TR 182,14,17568
Mvaa 76,196,28
RA 0,17,0
Mvaa 92,196,32
RA 4,17,2048
Mvaa 108,196,36
RA 8,17,4096
Mvaa 156,200,20
s_mov_b32 s79,768
RA 12,17,6144
Mvaa 160,200,24
M0 69,0
RA 16,17,8192
Mvaa 164,200,28
s_nop 0
TR 184,14,17320
TR 186,14,17576
Mvaa 168,200,32
s_nop 0
TR 188,14,17376
TR 190,14,17632
Mvaa 172,200,36
DM 21,24,79,0
Mvaa 48,204,20
s_mov_b32 s79,37632
TR 192,14,17384
TR 194,14,17640
Mvaa 64,204,24
M0 69,4096
Mvaa 80,204,28
DM 21,24,79,0
Mvaa 96,204,32
s_mov_b32 s79,74496
Mvaa 112,204,36
M0 69,8192
Mvaa 176,208,20
s_nop 0
TR 196,14,17440
TR 198,14,17696
Mvaa 180,208,24
DM 21,24,79,0
Mvaa 184,208,28
s_mov_b32 s79,111360
Mvaa 188,208,32
M0 69,12288
Mvaa 192,208,36
WL 6
DM 21,24,79,0
Mvaa 36,180,0
s_mov_b32 s79,148224
TR 200,14,17448
TR 202,14,17704
Mvaa 52,180,4
M0 69,16384
s_add_u32 s98,s71,24576
v_lshl_add_u32 v8,v2,1,s98
Mvaa 68,180,8
DM 21,24,79,0
Mvaa 84,180,12
s_mov_b32 s79,3145728
Mvaa 100,180,16
M0 77,89728
Mvaa 116,184,0
s_nop 0
TR 204,14,17504
TR 206,14,17760
Mvaa 120,184,4
DM 20,28,79,0
Mvaa 124,184,8
s_mov_b32 s79,3153920
Mvaa 128,184,12
M0 77,90816
Mvaa 132,184,16
WL 6
DM 20,28,79,0
Mvaa 40,188,0
s_mov_b32 s79,3162112
TR 208,14,17512
TR 210,14,17768
Mvaa 56,188,4
M0 77,91904
Mvaa 72,188,8
DM 20,28,79,0
Mvaa 88,188,12
s_mov_b32 s79,3170304
Mvaa 104,188,16
M0 77,92992
Mvaa 136,192,0
DM 20,28,79,0
Mvaa 140,192,4
M0 78,55104
Mvaa 144,192,8
Mvaa 148,192,12
DN 19,36,4,0
Mvaa 152,192,16
WL 4
buffer_load_dword v8,v8,s[60:63],0 offen
Mvaa 44,196,0
s_waitcnt vmcnt(12) lgkmcnt(0)
s_barrier
Mvaa 60,196,4
s_nop 0
TR 180,14,34624
TR 182,14,34880
Mvaa 76,196,8
RA 20,18,0
Mvaa 92,196,12
RA 24,18,2048
Mvaa 108,196,16
RA 28,18,4096
Mvaa 156,200,0
s_mov_b32 s79,3407872
RA 32,18,6144
Mvaa 160,200,4
M0 77,107040
RA 36,18,8192
Mvaa 164,200,8
s_nop 0
TR 184,14,34632
TR 186,14,34888
Mvaa 168,200,12
s_nop 0
TR 188,14,34688
TR 190,14,34944
Mvaa 172,200,16
DM 20,28,79,0
Mvaa 48,204,0
s_mov_b32 s79,3416064
TR 192,14,34696
TR 194,14,34952
Mvaa 64,204,4
M0 77,108128
Mvaa 80,204,8
DM 20,28,79,0
Mvaa 96,204,12
s_mov_b32 s79,3424256
Mvaa 112,204,16
M0 77,109216
Mvaa 176,208,0
s_nop 0
TR 196,14,34752
TR 198,14,35008
Mvaa 180,208,4
DM 20,28,79,0
Mvaa 184,208,8
s_mov_b32 s79,3432448
Mvaa 188,208,12
M0 77,110304
Mvaa 192,208,16
WL 6
DM 20,28,79,0
Mvaa 36,180,20
s_nop 0
TR 200,14,34760
TR 202,14,35016
Mvaa 52,180,24
Mvaa 68,180,28
Mvaa 84,180,32
Mvaa 100,180,36
Mvaa 116,184,20
s_nop 0
TR 204,14,34816
TR 206,14,35072
Mvaa 120,184,24
Mvaa 124,184,28
Mvaa 128,184,32
M0 78,59328
Mvaa 132,184,36
WL 6
DN 33,36,4,0
Mvaa 40,188,20
s_nop 0
TR 208,14,34824
TR 210,14,35080
Mvaa 56,188,24
Mvaa 72,188,28
Mvaa 88,188,32
Mvaa 104,188,36
Mvaa 136,192,20
Mvaa 140,192,24
Mvaa 144,192,28
Mvaa 148,192,32
Mvaa 152,192,36
WL 4
Mvaa 44,196,20
s_waitcnt vmcnt(7) lgkmcnt(0)
s_barrier
Mvaa 60,196,24
s_nop 0
TR 180,14,0
TR 182,14,256
Mvaa 76,196,28
RA 0,15,0
Mvaa 92,196,32
RA 4,15,2048
Mvaa 108,196,36
RA 8,15,4096
Mvaa 156,200,20
s_mov_b32 s79,896
RA 12,15,6144
Mvaa 160,200,24
M0 69,141664
RA 16,15,8192
Mvaa 164,200,28
s_nop 0
TR 184,14,8
TR 186,14,264
Mvaa 168,200,32
s_nop 0
TR 188,14,64
TR 190,14,320
Mvaa 172,200,36
DM 21,24,79,0
Mvaa 48,204,20
s_mov_b32 s79,37760
TR 192,14,72
TR 194,14,328
Mvaa 64,204,24
M0 69,145760
Mvaa 80,204,28
DM 21,24,79,0
Mvaa 96,204,32
s_mov_b32 s79,74624
Mvaa 112,204,36
M0 69,149856
Mvaa 176,208,20
s_nop 0
TR 196,14,128
TR 198,14,384
Mvaa 180,208,24
DM 21,24,79,0
Mvaa 184,208,28
s_mov_b32 s79,111488
Mvaa 188,208,32
M0 69,153952
Mvaa 192,208,36
WL 6
DM 21,24,79,0
Mvaa 36,180,0
s_mov_b32 s79,148352
TR 200,14,136
TR 202,14,392
Mvaa 52,180,4
M0 69,158048
Mvaa 68,180,8
DM 21,24,79,0
Mvaa 84,180,12
s_mov_b32 s79,3670016
Mvaa 100,180,16
M0 77,124352
Mvaa 116,184,0
s_nop 0
TR 204,14,192
TR 206,14,448
Mvaa 120,184,4
DM 20,28,79,0
Mvaa 124,184,8
s_mov_b32 s79,3678208
Mvaa 128,184,12
M0 77,125440
Mvaa 132,184,16
WL 6
DM 20,28,79,0
Mvaa 40,188,0
s_mov_b32 s79,3686400
TR 208,14,200
TR 210,14,456
Mvaa 56,188,4
M0 77,126528
Mvaa 72,188,8
DM 20,28,79,0
Mvaa 88,188,12
s_mov_b32 s79,3694592
Mvaa 104,188,16
M0 77,127616
Mvaa 136,192,0
DM 20,28,79,0
Mvaa 140,192,4
M0 78,63552
Mvaa 144,192,8
Mvaa 148,192,12
DN 19,36,5,0
Mvaa 152,192,16
WL 4
Mvaa 44,196,0
s_waitcnt vmcnt(11) lgkmcnt(0)
s_barrier
Mvaa 60,196,4
s_nop 0
TR 180,14,17312
TR 182,14,17568
Mvaa 76,196,8
RA 20,16,0
Mvaa 92,196,12
RA 24,16,2048
Mvaa 108,196,16
RA 28,16,4096
Mvaa 156,200,0
s_mov_b32 s79,3932160
RA 32,16,6144
Mvaa 160,200,4
M0 77,89728
RA 36,16,8192
Mvaa 164,200,8
s_nop 0
TR 184,14,17320
TR 186,14,17576
Mvaa 168,200,12
s_nop 0
TR 188,14,17376
TR 190,14,17632
Mvaa 172,200,16
DM 20,28,79,0
Mvaa 48,204,0
s_mov_b32 s79,3940352
TR 192,14,17384
TR 194,14,17640
Mvaa 64,204,4
M0 77,90816
Mvaa 80,204,8
DM 20,28,79,0
Mvaa 96,204,12
s_mov_b32 s79,3948544
Mvaa 112,204,16
M0 77,91904
Mvaa 176,208,0
s_nop 0
TR 196,14,17440
TR 198,14,17696
Mvaa 180,208,4
DM 20,28,79,0
Mvaa 184,208,8
s_mov_b32 s79,3956736
Mvaa 188,208,12
M0 77,92992
Mvaa 192,208,16
WL 6
DM 20,28,79,0
Mvaa 36,180,20
s_nop 0
TR 200,14,17448
TR 202,14,17704
Mvaa 52,180,24
Mvaa 68,180,28
Mvaa 84,180,32
Mvaa 100,180,36
Mvaa 116,184,20
s_nop 0
TR 204,14,17504
TR 206,14,17760
Mvaa 120,184,24
Mvaa 124,184,28
Mvaa 128,184,32
M0 78,67776
Mvaa 132,184,36
WL 6
DN 33,36,5,0
Mvaa 40,188,20
s_nop 0
TR 208,14,17512
TR 210,14,17768
Mvaa 56,188,24
Mvaa 72,188,28
Mvaa 88,188,32
Mvaa 104,188,36
Mvaa 136,192,20
Mvaa 140,192,24
Mvaa 144,192,28
Mvaa 148,192,32
Mvaa 152,192,36
WL 4
Mvaa 44,196,20
s_waitcnt vmcnt(6) lgkmcnt(0)
s_barrier
Mvaa 60,196,24
s_nop 0
TR 180,14,34624
TR 182,14,34880
Mvaa 76,196,28
RA 0,17,0
Mvaa 92,196,32
RA 4,17,2048
Mvaa 108,196,36
RA 8,17,4096
Mvaa 156,200,20
s_mov_b32 s79,1024
RA 12,17,6144
Mvaa 160,200,24
M0 69,0
RA 16,17,8192
Mvaa 164,200,28
s_nop 0
TR 184,14,34632
TR 186,14,34888
Mvaa 168,200,32
s_nop 0
TR 188,14,34688
TR 190,14,34944
Mvaa 172,200,36
DM 21,24,79,0
Mvaa 48,204,20
s_mov_b32 s79,37888
TR 192,14,34696
TR 194,14,34952
Mvaa 64,204,24
M0 69,4096
Mvaa 80,204,28
DM 21,24,79,0
Mvaa 96,204,32
s_mov_b32 s79,74752
Mvaa 112,204,36
M0 69,8192
Mvaa 176,208,20
s_nop 0
TR 196,14,34752
TR 198,14,35008
Mvaa 180,208,24
DM 21,24,79,0
Mvaa 184,208,28
s_mov_b32 s79,111616
Mvaa 188,208,32
M0 69,12288
Mvaa 192,208,36
WL 6
DM 21,24,79,0
Mvaa 36,180,0
s_mov_b32 s79,148480
TR 200,14,34760
TR 202,14,35016
Mvaa 52,180,4
M0 69,16384
Mvaa 68,180,8
DM 21,24,79,0
Mvaa 84,180,12
s_mov_b32 s79,4194304
Mvaa 100,180,16
M0 77,107040
Mvaa 116,184,0
s_nop 0
TR 204,14,34816
TR 206,14,35072
Mvaa 120,184,4
DM 20,28,79,0
Mvaa 124,184,8
s_mov_b32 s79,4202496
Mvaa 128,184,12
M0 77,108128
Mvaa 132,184,16
WL 6
DM 20,28,79,0
Mvaa 40,188,0
s_mov_b32 s79,4210688
TR 208,14,34824
TR 210,14,35080
Mvaa 56,188,4
M0 77,109216
Mvaa 72,188,8
DM 20,28,79,0
Mvaa 88,188,12
s_mov_b32 s79,4218880
Mvaa 104,188,16
M0 77,110304
Mvaa 136,192,0
DM 20,28,79,0
Mvaa 140,192,4
Mvaa 144,192,8
Mvaa 148,192,12
Mvaa 152,192,16
WL 4
Mvaa 44,196,0
s_waitcnt vmcnt(10) lgkmcnt(0)
s_barrier
Mvaa 60,196,4
s_nop 0
TR 180,14,0
TR 182,14,256
Mvaa 76,196,8
RA 20,18,0
Mvaa 92,196,12
s_add_u32 s4,s80,16384
RA 24,18,2048
Mvaa 108,196,16
s_and_b32 s4,s4,0x1e000
RA 28,18,4096
Mvaa 156,200,0
s_xor_b32 s5,s4,0x30000
RA 32,18,6144
Mvaa 160,200,4
M0 78,72416
RA 36,18,8192
Mvaa 164,200,8
s_nop 0
TR 184,14,8
TR 186,14,264
Mvaa 168,200,12
s_nop 0
TR 188,14,64
TR 190,14,320
Mvaa 172,200,16
DN 19,32,4,0
Mvaa 48,204,0
s_mov_b32 s79,4456448
TR 192,14,72
TR 194,14,328
Mvaa 64,204,4
M0 77,124352
Mvaa 80,204,8
DM 20,28,79,0
Mvaa 96,204,12
s_mov_b32 s79,4464640
Mvaa 112,204,16
M0 77,125440
Mvaa 176,208,0
s_nop 0
TR 196,14,128
TR 198,14,384
Mvaa 180,208,4
DM 20,28,79,0
Mvaa 184,208,8
s_mov_b32 s79,4472832
Mvaa 188,208,12
M0 77,126528
Mvaa 192,208,16
WL 6
DM 20,28,79,0
Mvaa 36,180,20
s_mov_b32 s79,4481024
TR 200,14,136
TR 202,14,392
Mvaa 52,180,24
M0 77,127616
Mvaa 68,180,28
DM 20,28,79,0
Mvaa 84,180,32
Mvaa 100,180,36
Mvaa 116,184,20
s_nop 0
TR 204,14,192
TR 206,14,448
Mvaa 120,184,24
Mvaa 124,184,28
Mvaa 128,184,32
M0 78,76640
Mvaa 132,184,36
WL 6
DN 33,32,4,0
Mvaa 40,188,20
s_nop 0
TR 208,14,200
TR 210,14,456
Mvaa 56,188,24
Mvaa 72,188,28
Mvaa 88,188,32
Mvaa 104,188,36
Mvaa 136,192,20
Mvaa 140,192,24
Mvaa 144,192,28
Mvaa 148,192,32
Mvaa 152,192,36
WL 4
Mvaa 44,196,20
s_waitcnt vmcnt(6) lgkmcnt(0)
s_barrier
Mvaa 60,196,24
s_nop 0
TR 180,14,17312
TR 182,14,17568
Mvaa 76,196,28
RA 0,15,0
Mvaa 92,196,32
RA 4,15,2048
Mvaa 108,196,36
RA 8,15,4096
Mvaa 156,200,20
RA 12,15,6144
Mvaa 160,200,24
M0 78,89728
RA 16,15,8192
Mvaa 164,200,28
s_nop 0
TR 184,14,17320
TR 186,14,17576
Mvaa 168,200,32
s_nop 0
TR 188,14,17376
TR 190,14,17632
Mvaa 172,200,36
DN 19,36,6,0
Mvaa 48,204,20
M0 78,93952
TR 192,14,17384
TR 194,14,17640
Mvaa 64,204,24
Mvaa 80,204,28
DN 33,36,6,0
Mvaa 96,204,32
M0 78,98176
Mvaa 112,204,36
Mvaa 176,208,20
s_nop 0
TR 196,14,17440
TR 198,14,17696
Mvaa 180,208,24
DN 19,36,7,0
Mvaa 184,208,28
M0 78,102400
Mvaa 188,208,32
Mvaa 192,208,36
WL 6
DN 33,36,7,0
Mvaa 36,180,0
s_nop 0
TR 200,14,17448
TR 202,14,17704
Mvaa 52,180,4
Mvaa 68,180,8
Mvaa 84,180,12
Mvaa 100,180,16
Mvaa 116,184,0
s_nop 0
TR 204,14,17504
TR 206,14,17760
Mvaa 120,184,4
Mvaa 124,184,8
Mvaa 128,184,12
M0 78,80864
Mvaa 132,184,16
WL 6
DN 19,32,5,0
Mvaa 40,188,0
s_nop 0
TR 208,14,17512
TR 210,14,17768
Mvaa 56,188,4
Mvaa 72,188,8
Mvaa 88,188,12
Mvaa 104,188,16
Mvaa 136,192,0
Mvaa 140,192,4
Mvaa 144,192,8
Mvaa 148,192,12
Mvaa 152,192,16
WL 4
Mvaa 44,196,0
s_waitcnt vmcnt(6) lgkmcnt(0)
s_barrier
Mvaa 60,196,4
s_nop 0
TR 180,14,34624
TR 182,14,34880
Mvaa 76,196,8
RA 20,16,0
Mvaa 92,196,12
RA 24,16,2048
Mvaa 108,196,16
RA 28,16,4096
Mvaa 156,200,0
RA 32,16,6144
Mvaa 160,200,4
RA 36,16,8192
Mvaa 164,200,8
s_nop 0
TR 184,14,34632
TR 186,14,34888
Mvaa 168,200,12
s_nop 0
TR 188,14,34688
TR 190,14,34944
Mvaa 172,200,16
Mvaa 48,204,0
s_nop 0
TR 192,14,34696
TR 194,14,34952
Mvaa 64,204,4
Mvaa 80,204,8
Mvaa 96,204,12
Mvaa 112,204,16
Mvaa 176,208,0
s_nop 0
TR 196,14,34752
TR 198,14,35008
Mvaa 180,208,4
Mvaa 184,208,8
Mvaa 188,208,12
Mvaa 192,208,16
WL 6
Mvaa 36,180,20
s_nop 0
TR 200,14,34760
TR 202,14,35016
Mvaa 52,180,24
Mvaa 68,180,28
Mvaa 84,180,32
Mvaa 100,180,36
Mvaa 116,184,20
s_nop 0
TR 204,14,34816
TR 206,14,35072
Mvaa 120,184,24
Mvaa 124,184,28
Mvaa 128,184,32
M0 78,85088
v_mul_f32 v36,s84,v36
v_mul_f32 v37,s84,v37
Mvaa 132,184,36
WL 6
DN 33,32,5,0
Mvaa 40,188,20
s_add_u32 s6,s80,24576
TR 208,14,34824
TR 210,14,35080
Mvaa 56,188,24
s_and_b32 s6,s6,0x1e000
v_mul_f32 v38,s84,v38
v_mul_f32 v39,s84,v39
Mvaa 72,188,28
s_xor_b32 s7,s6,0x30000
CV 36,36,37
CV 37,38,39
Mvaa 88,188,32
M0 78,107040
v_mul_f32 v116,s84,v116
v_mul_f32 v117,s84,v117
Mvaa 104,188,36
DN 19,32,6,0
Mvaa 136,192,20
M0 78,111264
v_mul_f32 v118,s84,v118
v_mul_f32 v119,s84,v119
Mvaa 140,192,24
CV 38,116,117
CV 39,118,119
Mvaa 144,192,28
DN 33,32,6,0
Mvaa 148,192,32
v_mul_f32 v52,s84,v52
v_mul_f32 v53,s84,v53
Mvaa 152,192,36
WL 4
v_mul_f32 v54,s84,v54
v_mul_f32 v55,s84,v55
v_mul_f32 v56,s84,v56
Mvaa 44,196,20
WL 0
s_barrier
v_mul_f32 v57,s84,v57
Mvaa 60,196,24
M0 78,115488
RA 180,10,0
CV 52,52,53
v_mul_f32 v58,s84,v58
Mvaa 76,196,28
RA 184,10,64
CV 53,54,55
v_mul_f32 v59,s84,v59
Mvaa 92,196,32
DN 19,32,7,0
CV 56,56,57
Mvaa 108,196,36
M0 78,119712
v_mul_f32 v120,s84,v120
v_mul_f32 v121,s84,v121
CV 57,58,59
Mvaa 156,200,20
RA 188,10,128
v_mul_f32 v122,s84,v122
v_mul_f32 v140,s84,v140
Mvaa 160,200,24
DN 33,32,7,0
v_mul_f32 v141,s84,v141
Mvaa 164,200,28
M0 78,124352
v_mul_f32 v123,s84,v123
CV 54,120,121
v_mul_f32 v142,s84,v142
Mvaa 168,200,32
CV 55,122,123
v_mul_f32 v68,s84,v68
v_mul_f32 v143,s84,v143
Mvaa 172,200,36
DN 19,36,4,0
CV 58,140,141
Mvaa 48,204,20
M0 78,128576
RA 192,10,192
v_mul_f32 v69,s84,v69
CV 59,142,143
Mvaa 64,204,24
v_mul_f32 v70,s84,v70
v_mul_f32 v71,s84,v71
v_mul_f32 v104,s84,v104
Mvaa 80,204,28
DN 33,36,4,0
v_mul_f32 v105,s84,v105
Mvaa 96,204,32
M0 78,132800
CV 68,68,69
CV 69,70,71
v_mul_f32 v106,s84,v106
Mvaa 112,204,36
v_mul_f32 v124,s84,v124
v_mul_f32 v125,s84,v125
v_mul_f32 v107,s84,v107
Mvaa 176,208,20
RA 196,10,512
v_mul_f32 v126,s84,v126
CV 104,104,105
Mvaa 180,208,24
DN 19,36,5,0
CV 105,106,107
Mvaa 184,208,28
M0 78,137024
v_mul_f32 v127,s84,v127
CV 70,124,125
v_mul_f32 v152,s84,v152
Mvaa 188,208,32
CV 71,126,127
v_mul_f32 v84,s84,v84
v_mul_f32 v153,s84,v153
Mvaa 192,208,36
WL 3
DN 33,36,5,0
v_mul_f32 v154,s84,v154
Mvavz 116,180,36
s_add_u32 s4,s80,32768
RA 200,10,576
v_mul_f32 v85,s84,v85
v_mul_f32 v155,s84,v155
Mvavz 120,180,52
s_and_b32 s4,s4,0x1e000
v_mul_f32 v86,s84,v86
v_mul_f32 v87,s84,v87
CV 84,84,85
CV 85,86,87
v_mul_f32 v128,s84,v128
v_mul_f32 v129,s84,v129
v_mul_f32 v130,s84,v130
v_mul_f32 v131,s84,v131
CV 86,128,129
CV 87,130,131
Mvavz 124,180,68
s_xor_b32 s5,s4,0x30000
v_mul_f32 v100,s84,v100
v_mul_f32 v101,s84,v101
v_mul_f32 v102,s84,v102
v_mul_f32 v103,s84,v103
CV 100,100,101
CV 101,102,103
v_mul_f32 v132,s84,v132
v_mul_f32 v133,s84,v133
v_mul_f32 v134,s84,v134
v_mul_f32 v135,s84,v135
CV 102,132,133
CV 103,134,135
Mvavz 128,180,84
M0 78,141664
v_mul_f32 v40,s84,v40
v_mul_f32 v41,s84,v41
v_mul_f32 v42,s84,v42
v_mul_f32 v43,s84,v43
CV 40,40,41
CV 41,42,43
v_mul_f32 v136,s84,v136
v_mul_f32 v137,s84,v137
v_mul_f32 v138,s84,v138
v_mul_f32 v139,s84,v139
CV 42,136,137
CV 43,138,139
Mvavz 132,180,100
DN 19,32,4,0
CV 106,152,153
Mvav 116,184,40
M0 78,145888
RA 204,10,640
v_mul_f32 v72,s84,v72
v_mul_f32 v73,s84,v73
v_mul_f32 v74,s84,v74
v_mul_f32 v75,s84,v75
CV 72,72,73
CV 73,74,75
v_mul_f32 v144,s84,v144
v_mul_f32 v145,s84,v145
v_mul_f32 v146,s84,v146
v_mul_f32 v147,s84,v147
CV 74,144,145
CV 75,146,147
Mvav 120,184,56
v_mul_f32 v88,s84,v88
v_mul_f32 v89,s84,v89
v_mul_f32 v90,s84,v90
v_mul_f32 v91,s84,v91
CV 88,88,89
CV 89,90,91
v_mul_f32 v148,s84,v148
v_mul_f32 v149,s84,v149
v_mul_f32 v150,s84,v150
v_mul_f32 v151,s84,v151
CV 90,148,149
CV 91,150,151
Mvav 124,184,72
DN 33,32,4,0
CV 107,154,155
Mvav 128,184,88
M0 78,150112
v_mul_f32 v44,s84,v44
v_mul_f32 v45,s84,v45
v_mul_f32 v46,s84,v46
v_mul_f32 v47,s84,v47
CV 44,44,45
CV 45,46,47
v_mul_f32 v156,s84,v156
v_mul_f32 v157,s84,v157
v_mul_f32 v158,s84,v158
v_mul_f32 v159,s84,v159
CV 46,156,157
CV 47,158,159
Mvav 132,184,104
WL 3
v_mul_f32 v60,s84,v60
v_mul_f32 v61,s84,v61
v_mul_f32 v62,s84,v62
v_mul_f32 v63,s84,v63
CV 60,60,61
CV 61,62,63
v_mul_f32 v160,s84,v160
v_mul_f32 v161,s84,v161
v_mul_f32 v162,s84,v162
v_mul_f32 v163,s84,v163
CV 62,160,161
CV 63,162,163
Mvav 116,188,44
RA 208,10,704
v_mul_f32 v76,s84,v76
v_mul_f32 v77,s84,v77
v_mul_f32 v78,s84,v78
v_mul_f32 v79,s84,v79
CV 76,76,77
CV 77,78,79
v_mul_f32 v164,s84,v164
v_mul_f32 v165,s84,v165
v_mul_f32 v166,s84,v166
v_mul_f32 v167,s84,v167
CV 78,164,165
CV 79,166,167
Mvav 120,188,60
DN 19,32,5,0
v_mul_f32 v92,s84,v92
v_mul_f32 v93,s84,v93
v_mul_f32 v94,s84,v94
v_mul_f32 v95,s84,v95
CV 92,92,93
CV 93,94,95
v_mul_f32 v168,s84,v168
v_mul_f32 v169,s84,v169
v_mul_f32 v170,s84,v170
v_mul_f32 v171,s84,v171
CV 94,168,169
CV 95,170,171
Mvav 124,188,76
M0 78,154336
v_mul_f32 v108,s84,v108
v_mul_f32 v109,s84,v109
v_mul_f32 v110,s84,v110
v_mul_f32 v111,s84,v111
CV 108,108,109
CV 109,110,111
v_mul_f32 v172,s84,v172
v_mul_f32 v173,s84,v173
v_mul_f32 v174,s84,v174
v_mul_f32 v175,s84,v175
CV 110,172,173
CV 111,174,175
Mvav 128,188,92
v_mul_f32 v48,s84,v48
v_mul_f32 v49,s84,v49
v_mul_f32 v50,s84,v50
v_mul_f32 v51,s84,v51
CV 48,48,49
CV 49,50,51
v_mul_f32 v176,s84,v176
v_mul_f32 v177,s84,v177
v_mul_f32 v178,s84,v178
v_mul_f32 v179,s84,v179
CV 50,176,177
CV 51,178,179
Mvav 132,188,108
DN 33,32,5,0
v_mul_f32 v64,s84,v64
v_mul_f32 v65,s84,v65
v_mul_f32 v66,s84,v66
v_mul_f32 v67,s84,v67
CV 64,64,65
CV 65,66,67
v_mul_f32 v180,s84,v180
v_mul_f32 v181,s84,v181
v_mul_f32 v182,s84,v182
v_mul_f32 v183,s84,v183
CV 66,180,181
CV 67,182,183
Mvav 116,192,48
v_mul_f32 v80,s84,v80
v_mul_f32 v81,s84,v81
v_mul_f32 v82,s84,v82
v_mul_f32 v83,s84,v83
CV 80,80,81
CV 81,82,83
v_mul_f32 v184,s84,v184
v_mul_f32 v185,s84,v185
v_mul_f32 v186,s84,v186
v_mul_f32 v187,s84,v187
CV 82,184,185
CV 83,186,187
Mvav 120,192,64
v_mul_f32 v96,s84,v96
v_mul_f32 v97,s84,v97
v_mul_f32 v98,s84,v98
v_mul_f32 v99,s84,v99
CV 96,96,97
CV 97,98,99
v_mul_f32 v188,s84,v188
v_mul_f32 v189,s84,v189
v_mul_f32 v190,s84,v190
v_mul_f32 v191,s84,v191
CV 98,188,189
CV 99,190,191
Mvav 124,192,80
v_mul_f32 v112,s84,v112
v_mul_f32 v113,s84,v113
v_mul_f32 v114,s84,v114
v_mul_f32 v115,s84,v115
CV 112,112,113
CV 113,114,115
v_mul_f32 v192,s84,v192
v_mul_f32 v193,s84,v193
v_mul_f32 v194,s84,v194
v_mul_f32 v195,s84,v195
CV 114,192,193
CV 115,194,195
Mvav 128,192,96
s_nop 0
Mvav 132,192,112
WL 2
s_cmp_lg_u32 s85,0
s_cbranch_scc1 LC_safe0
LC_back0:
v_max3_f32 v196,v116,v117,v118
v_max3_f32 v200,v120,v121,v122
v_max3_f32 v204,v124,v125,v126
v_max3_f32 v208,v128,v129,v130
s_nop 0
v_max3_f32 v212,v132,v133,v134
v_max3_f32 v196,v196,v119,v22
v_max3_f32 v200,v200,v123,v23
v_max3_f32 v204,v204,v127,v24
v_max3_f32 v208,v208,v131,v25
v_max3_f32 v212,v212,v135,v26
v_mov_b32 v197,v196
v_mov_b32 v201,v200
v_mov_b32 v205,v204
v_mov_b32 v209,v208
v_mov_b32 v213,v212
v_permlane16_swap_b32 v196,v197
v_permlane16_swap_b32 v200,v201
v_permlane16_swap_b32 v204,v205
v_permlane16_swap_b32 v208,v209
v_permlane16_swap_b32 v212,v213
v_max_f32 v196,v196,v197
v_max_f32 v200,v200,v201
v_max_f32 v204,v204,v205
v_max_f32 v208,v208,v209
v_max_f32 v212,v212,v213
v_mov_b32 v197,v196
v_mov_b32 v201,v200
v_mov_b32 v205,v204
v_mov_b32 v209,v208
v_mov_b32 v213,v212
v_permlane32_swap_b32 v196,v197
v_permlane32_swap_b32 v200,v201
v_permlane32_swap_b32 v204,v205
v_permlane32_swap_b32 v208,v209
v_permlane32_swap_b32 v212,v213
v_max_f32 v196,v196,v197
v_max_f32 v200,v200,v201
v_max_f32 v204,v204,v205
v_max_f32 v208,v208,v209
v_max_f32 v212,v212,v213
v_sub_f32 v199,0,v196
v_sub_f32 v198,0,v196
v_sub_f32 v197,0,v196
v_sub_f32 v196,0,v196
v_sub_f32 v203,0,v200
v_sub_f32 v202,0,v200
v_sub_f32 v201,0,v200
v_sub_f32 v200,0,v200
v_sub_f32 v207,0,v204
v_sub_f32 v206,0,v204
v_sub_f32 v205,0,v204
v_sub_f32 v204,0,v204
v_sub_f32 v211,0,v208
v_sub_f32 v210,0,v208
v_sub_f32 v209,0,v208
v_sub_f32 v208,0,v208
v_sub_f32 v215,0,v212
v_sub_f32 v214,0,v212
v_sub_f32 v213,0,v212
v_sub_f32 v212,0,v212
Mvavc 136,196,36,196
s_memrealtime s[94:95]
WL 0
s_barrier
s_add_u32 s96,s94,90
Mvav 136,200,40
M0 78,20480
RA 180,10,17312
v_add_f32 v116,v116,v196
Mvavc 140,196,52,200
s_add_u32 s98,s71,32768
RA 184,10,17376
v_lshl_add_u32 v8,v2,1,s98
Mvav 140,200,56
DN 19,36,6,0
Mvavc 144,196,68,204
EX 116
Mvav 144,200,72
RA 188,10,17440
v_add_f32 v117,v117,v196
Mvavc 148,196,84,208
buffer_load_dword v8,v8,s[60:63],0 offen
Mvav 148,200,88
EX 117
Mvavc 152,196,100,212
v_add_f32 v118,v118,v196
v_add_f32 v120,v120,v200
CV 156,116,117
Mvav 152,200,104
EX 118
Mvav 136,204,44
M0 78,24704
RA 192,10,17504
v_add_f32 v119,v119,v196
Mvav 136,208,48
DN 33,36,6,0
Mvav 140,204,60
EX 119
Mvav 140,208,64
EX 120
CV 157,118,119
Mvav 144,204,76
v_add_f32 v124,v124,v204
v_add_f32 v128,v128,v208
Mvav 144,208,80
RA 196,10,17824
v_add_f32 v121,v121,v200
Mvav 148,204,92
EX 121
Mvav 148,208,96
v_add_f32 v122,v122,v200
v_add_f32 v132,v132,v212
CV 160,120,121
Mvav 152,204,108
M0 78,28928
EX 122
Mvav 152,208,112
WL 3
DN 19,36,7,0
Mvavc 116,180,36,196
RA 200,10,17888
v_add_f32 v123,v123,v200
EX 123
Mvav 116,184,40
EX 124
CV 161,122,123
Mvavc 120,180,52,200
v_add_f32 v125,v125,v204
EX 125
v_add_f32 v126,v126,v204
EX 126
v_add_f32 v127,v127,v204
EX 127
Mvav 120,184,56
EX 128
CV 164,124,125
CV 165,126,127
Mvavc 124,180,68,204
v_add_f32 v129,v129,v208
EX 129
v_add_f32 v130,v130,v208
EX 130
v_add_f32 v131,v131,v208
EX 131
Mvav 124,184,72
RA 204,10,17952
CV 168,128,129
CV 169,130,131
Mvavc 128,180,84,208
M0 78,33152
EX 132
v_add_f32 v133,v133,v212
EX 133
v_add_f32 v134,v134,v212
EX 134
v_add_f32 v135,v135,v212
EX 135
Mvav 128,184,88
DN 33,36,7,0
CV 172,132,133
CV 173,134,135
Mvavc 132,180,100,212
EX 136
Mvav 132,184,104
WL 3
EX 137
Mvav 116,188,44
RA 208,10,18016
CV 158,136,137
Mvav 116,192,48
EX 138
Mvav 120,188,60
EX 139
Mvav 120,192,64
EX 140
CV 159,138,139
Mvav 124,188,76
EX 141
Mvav 124,192,80
s_nop 0
TR 180,12,34624
TR 182,12,35136
CV 162,140,141
Mvav 128,188,92
EX 142
Mvav 128,192,96
EX 143
Mvav 132,188,108
EX 144
CV 163,142,143
Mvav 132,192,112
WL 4
EX 145
Mvavc 136,196,36,196
s_nop 0
TR 184,12,34656
TR 186,12,35168
CV 166,144,145
Mvav 136,200,40
EX 146
Mvavc 140,196,52,200
EX 147
Mvav 140,200,56
EX 148
CV 167,146,147
Mvavc 144,196,68,204
EX 149
EX 150
EX 151
Mvav 144,200,72
s_nop 0
TR 188,12,34688
TR 190,12,35200
CV 170,148,149
CV 171,150,151
Mvavc 148,196,84,208
EX 152
EX 153
EX 154
EX 155
Mvav 148,200,88
CV 174,152,153
CV 175,154,155
Mvavc 152,196,100,212
EX 116
Mvav 152,200,104
WL 6
EX 117
Mvav 136,204,44
s_nop 0
TR 192,12,34720
TR 194,12,35232
Mvav 136,208,48
EX 118
Mvav 140,204,60
EX 119
Mvav 140,208,64
EX 120
Mvav 144,204,76
EX 121
Mvav 144,208,80
s_nop 0
TR 196,12,34752
TR 198,12,35264
Mvav 148,204,92
EX 122
Mvav 148,208,96
EX 123
Mvav 152,204,108
EX 124
Mvav 152,208,112
WL 6
EX 125
Maavz 0,180,156
s_nop 0
TR 200,12,34784
TR 202,12,35296
Maavz 4,180,160
EX 126
Maavz 8,180,164
EX 127
Maavz 12,180,168
EX 128
Maavz 16,180,172
EX 129
Maavz 20,184,156
s_nop 0
TR 204,12,34816
TR 206,12,35328
Maavz 24,184,160
EX 130
Maavz 28,184,164
EX 131
Maavz 32,184,168
EX 132
Maavz 36,184,172
WL 6
EX 133
Maavz 40,188,156
s_nop 0
TR 208,12,34848
TR 210,12,35360
Maavz 44,188,160
EX 134
Maavz 48,188,164
EX 135
Maavz 52,188,168
EX 136
Maavz 56,188,172
EX 137
Maavz 60,192,156
EX 138
Maavz 64,192,160
EX 139
Maavz 68,192,164
EX 140
Maavz 72,192,168
EX 141
Maavz 76,192,172
WL 4
EX 142
Maavz 80,196,156
s_memrealtime s[94:95]
s_waitcnt vmcnt(17) lgkmcnt(0)
s_barrier
s_sub_u32 s95,s94,s96
s_cmp_lt_i32 s95,0
s_cbranch_scc1 LC_pc21
LC_pb21:
s_add_u32 s96,s94,90
Maavz 84,196,160
s_add_u32 s6,s80,40960
RA 180,10,51936
CV 176,116,117
Maavz 88,196,164
s_and_b32 s6,s6,0x1e000
RA 184,10,52000
CV 177,118,119
Maavz 92,196,168
s_xor_b32 s7,s6,0x30000
EX 143
Maavz 96,196,172
M0 78,37792
EX 144
Maavz 100,200,156
RA 188,10,52064
CV 180,120,121
Maavz 104,200,160
DN 19,32,6,0
Maavz 108,200,164
EX 145
Maavz 112,200,168
EX 146
Maavz 116,200,172
EX 147
Maavz 120,204,156
RA 192,10,52128
CV 181,122,123
Maavz 124,204,160
EX 148
Maavz 128,204,164
EX 149
Maavz 132,204,168
M0 78,42016
EX 150
Maavz 136,204,172
DN 33,32,6,0
Maavz 140,208,156
RA 196,10,52448
CV 184,124,125
Maavz 144,208,160
EX 151
Maavz 148,208,164
EX 152
Maavz 152,208,168
EX 153
Maavz 156,208,172
EX 154
Maavz 160,212,156
EX 155
Maavz 164,212,160
M0 78,46240
CV 185,126,127
CV 188,128,129
Maavz 168,212,164
DN 19,32,7,0
Maavz 172,212,168
CV 189,130,131
CV 192,132,133
Maavz 176,212,172
WL 3
CV 193,134,135
CV 178,136,137
Mvavc 116,180,36,196
RA 200,10,52512
CV 179,138,139
Mvav 116,184,40
CV 182,140,141
CV 183,142,143
Mvavc 120,180,52,200
CV 186,144,145
CV 187,146,147
Mvav 120,184,56
CV 190,148,149
CV 191,150,151
Mvavc 124,180,68,204
M0 78,50464
CV 195,154,155
Mvav 124,184,72
RA 204,10,52576
CV 194,152,153
Mvavc 128,180,84,208
DN 33,32,7,0
Mvav 128,184,88
Mvavc 132,180,100,212
Mvav 132,184,104
WL 3
Mvav 116,188,44
RA 208,10,52640
Mvav 116,192,48
Mvav 120,188,60
Mvav 120,192,64
M0 78,55104
Mvav 124,188,76
DN 19,36,4,0
Mvav 124,192,80
s_nop 0
TR 180,13,0
TR 182,13,512
Mvav 128,188,92
EX 116
Mvav 128,192,96
EX 117
Mvav 132,188,108
EX 118
Mvav 132,192,112
WL 4
EX 119
Mvavc 136,196,36,196
s_nop 0
TR 184,13,32
TR 186,13,544
Mvav 136,200,40
M0 78,59328
EX 120
Mvavc 140,196,52,200
DN 33,36,4,0
Mvav 140,200,56
EX 121
Mvavc 144,196,68,204
EX 122
Mvav 144,200,72
s_nop 0
TR 188,13,64
TR 190,13,576
Mvavc 148,196,84,208
EX 123
Mvav 148,200,88
EX 124
Mvavc 152,196,100,212
EX 125
Mvav 152,200,104
WL 6
EX 126
Mvav 136,204,44
M0 78,63552
TR 192,13,96
TR 194,13,608
Mvav 136,208,48
DN 19,36,5,0
Mvav 140,204,60
EX 127
Mvav 140,208,64
EX 128
Mvav 144,204,76
EX 129
Mvav 144,208,80
s_nop 0
TR 196,13,128
TR 198,13,640
Mvav 148,204,92
EX 130
Mvav 148,208,96
EX 131
Mvav 152,204,108
M0 78,67776
EX 132
Mvav 152,208,112
WL 6
DN 33,36,5,0
Maav 0,180,176
s_nop 0
TR 200,13,160
TR 202,13,672
Maav 4,180,180
EX 133
Maav 8,180,184
EX 134
Maav 12,180,188
EX 135
Maav 16,180,192
EX 136
Maav 20,184,176
s_nop 0
TR 204,13,192
TR 206,13,704
Maav 24,184,180
EX 137
Maav 28,184,184
EX 138
Maav 32,184,188
EX 139
Maav 36,184,192
WL 6
EX 140
Maav 40,188,176
s_nop 0
TR 208,13,224
TR 210,13,736
Maav 44,188,180
EX 141
Maav 48,188,184
EX 142
Maav 52,188,188
EX 143
Maav 56,188,192
EX 144
Maav 60,192,176
EX 145
Maav 64,192,180
EX 146
Maav 68,192,184
EX 147
Maav 72,192,188
EX 148
Maav 76,192,192
WL 4
EX 149
Maav 80,196,176
s_memrealtime s[94:95]
s_waitcnt vmcnt(17) lgkmcnt(0)
s_barrier
s_sub_u32 s95,s94,s96
s_cmp_lt_i32 s95,0
s_cbranch_scc1 LC_pc22
LC_pb22:
s_add_u32 s96,s94,90
Maav 84,196,180
s_add_u32 s4,s80,49152
RA 180,11,17312
CV 156,116,117
Maav 88,196,184
s_and_b32 s4,s4,0x1e000
RA 184,11,17376
CV 157,118,119
Maav 92,196,188
s_xor_b32 s5,s4,0x30000
EX 150
Maav 96,196,192
M0 78,72416
EX 151
Maav 100,200,176
RA 188,11,17440
CV 160,120,121
Maav 104,200,180
DN 19,32,4,0
Maav 108,200,184
EX 152
Maav 112,200,188
EX 153
Maav 116,200,192
EX 154
Maav 120,204,176
RA 192,11,17504
CV 161,122,123
Maav 124,204,180
EX 155
Maav 128,204,184
CV 164,124,125
CV 165,126,127
Maav 132,204,188
M0 78,76640
CV 168,128,129
CV 169,130,131
Maav 136,204,192
DN 33,32,4,0
Maav 140,208,176
RA 196,11,17824
CV 172,132,133
Maav 144,208,180
CV 173,134,135
CV 158,136,137
Maav 148,208,184
CV 159,138,139
CV 162,140,141
Maav 152,208,188
CV 163,142,143
CV 166,144,145
Maav 156,208,192
CV 167,146,147
CV 170,148,149
Maav 160,212,176
CV 171,150,151
CV 174,152,153
Maav 164,212,180
M0 78,80864
CV 175,154,155
Maav 168,212,184
DN 19,32,5,0
Maav 172,212,188
Maav 176,212,192
WL 3
Mvavc 116,180,36,196
RA 200,11,17888
Mvav 116,184,40
Mvavc 120,180,52,200
Mvav 120,184,56
Mvavc 124,180,68,204
M0 78,85088
Mvav 124,184,72
RA 204,11,17952
Mvavc 128,180,84,208
DN 33,32,5,0
Mvav 128,184,88
Mvavc 132,180,100,212
Mvav 132,184,104
WL 3
Mvav 116,188,44
RA 208,11,18016
Mvav 116,192,48
Mvav 120,188,60
Mvav 120,192,64
M0 78,89728
Mvav 124,188,76
DN 19,36,6,0
Mvav 124,192,80
s_nop 0
TR 180,13,34624
TR 182,13,35136
Mvav 128,188,92
EX 116
Mvav 128,192,96
EX 117
Mvav 132,188,108
EX 118
Mvav 132,192,112
WL 4
EX 119
Mvavc 136,196,36,196
s_nop 0
TR 184,13,34656
TR 186,13,35168
Mvav 136,200,40
M0 78,93952
EX 120
Mvavc 140,196,52,200
DN 33,36,6,0
Mvav 140,200,56
EX 121
Mvavc 144,196,68,204
EX 122
Mvav 144,200,72
s_nop 0
TR 188,13,34688
TR 190,13,35200
Mvavc 148,196,84,208
EX 123
Mvav 148,200,88
EX 124
Mvavc 152,196,100,212
EX 125
Mvav 152,200,104
WL 6
EX 126
Mvav 136,204,44
M0 78,98176
TR 192,13,34720
TR 194,13,35232
Mvav 136,208,48
DN 19,36,7,0
Mvav 140,204,60
EX 127
Mvav 140,208,64
EX 128
Mvav 144,204,76
EX 129
Mvav 144,208,80
s_nop 0
TR 196,13,34752
TR 198,13,35264
Mvav 148,204,92
EX 130
Mvav 148,208,96
EX 131
Mvav 152,204,108
M0 78,102400
EX 132
Mvav 152,208,112
WL 6
DN 33,36,7,0
Maav 0,180,156
s_nop 0
TR 200,13,34784
TR 202,13,35296
Maav 4,180,160
EX 133
Maav 8,180,164
EX 134
Maav 12,180,168
EX 135
Maav 16,180,172
EX 136
Maav 20,184,156
s_nop 0
TR 204,13,34816
TR 206,13,35328
Maav 24,184,160
EX 137
Maav 28,184,164
EX 138
Maav 32,184,168
EX 139
Maav 36,184,172
WL 6
EX 140
Maav 40,188,156
s_nop 0
TR 208,13,34848
TR 210,13,35360
Maav 44,188,160
EX 141
Maav 48,188,164
EX 142
Maav 52,188,168
EX 143
Maav 56,188,172
EX 144
Maav 60,192,156
EX 145
Maav 64,192,160
EX 146
Maav 68,192,164
EX 147
Maav 72,192,168
EX 148
Maav 76,192,172
WL 4
EX 149
Maav 80,196,156
s_memrealtime s[94:95]
s_waitcnt vmcnt(16) lgkmcnt(0)
s_barrier
s_sub_u32 s95,s94,s96
s_cmp_lt_i32 s95,0
s_cbranch_scc1 LC_pc23
LC_pb23:
s_add_u32 s96,s94,90
Maav 84,196,160
s_add_u32 s6,s80,57344
RA 180,11,51936
CV 176,116,117
Maav 88,196,164
s_and_b32 s6,s6,0x1e000
RA 184,11,52000
CV 177,118,119
Maav 92,196,168
s_xor_b32 s7,s6,0x30000
EX 150
Maav 96,196,172
M0 78,107040
EX 151
Maav 100,200,156
RA 188,11,52064
CV 180,120,121
Maav 104,200,160
DN 19,32,6,0
Maav 108,200,164
EX 152
Maav 112,200,168
EX 153
Maav 116,200,172
EX 154
Maav 120,204,156
RA 192,11,52128
CV 181,122,123
Maav 124,204,160
EX 155
Maav 128,204,164
CV 184,124,125
CV 185,126,127
Maav 132,204,168
M0 78,111264
CV 188,128,129
CV 189,130,131
Maav 136,204,172
DN 33,32,6,0
Maav 140,208,156
RA 196,11,52448
CV 192,132,133
Maav 144,208,160
CV 193,134,135
CV 178,136,137
Maav 148,208,164
CV 179,138,139
CV 182,140,141
Maav 152,208,168
CV 183,142,143
CV 186,144,145
Maav 156,208,172
CV 187,146,147
CV 190,148,149
Maav 160,212,156
CV 191,150,151
CV 194,152,153
Maav 164,212,160
M0 78,115488
CV 195,154,155
Maav 168,212,164
DN 19,32,7,0
Maav 172,212,168
Maav 176,212,172
WL 3
Mvavc 116,180,36,196
RA 200,11,52512
Mvav 116,184,40
Mvavc 120,180,52,200
Mvav 120,184,56
Mvavc 124,180,68,204
M0 78,119712
Mvav 124,184,72
RA 204,11,52576
Mvavc 128,180,84,208
DN 33,32,7,0
Mvav 128,184,88
Mvavc 132,180,100,212
Mvav 132,184,104
WL 3
Mvav 116,188,44
RA 208,11,52640
Mvav 116,192,48
Mvav 120,188,60
Mvav 120,192,64
M0 78,124352
Mvav 124,188,76
DN 19,36,4,0
Mvav 124,192,80
s_nop 0
TR 180,12,0
TR 182,12,512
Mvav 128,188,92
EX 116
Mvav 128,192,96
EX 117
Mvav 132,188,108
EX 118
Mvav 132,192,112
WL 4
EX 119
Mvavc 136,196,36,196
s_nop 0
TR 184,12,32
TR 186,12,544
Mvav 136,200,40
M0 78,128576
EX 120
Mvavc 140,196,52,200
DN 33,36,4,0
Mvav 140,200,56
EX 121
Mvavc 144,196,68,204
EX 122
Mvav 144,200,72
s_nop 0
TR 188,12,64
TR 190,12,576
Mvavc 148,196,84,208
EX 123
Mvav 148,200,88
EX 124
Mvavc 152,196,100,212
EX 125
Mvav 152,200,104
WL 6
EX 126
Mvav 136,204,44
M0 78,132800
TR 192,12,96
TR 194,12,608
Mvav 136,208,48
DN 19,36,5,0
Mvav 140,204,60
EX 127
Mvav 140,208,64
EX 128
Mvav 144,204,76
EX 129
Mvav 144,208,80
s_nop 0
TR 196,12,128
TR 198,12,640
Mvav 148,204,92
EX 130
Mvav 148,208,96
EX 131
Mvav 152,204,108
M0 78,137024
EX 132
Mvav 152,208,112
WL 6
DN 33,36,5,0
Maav 0,180,176
s_nop 0
TR 200,12,160
TR 202,12,672
Maav 4,180,180
EX 133
Maav 8,180,184
EX 134
Maav 12,180,188
EX 135
Maav 16,180,192
EX 136
Maav 20,184,176
s_nop 0
TR 204,12,192
TR 206,12,704
Maav 24,184,180
EX 137
Maav 28,184,184
EX 138
Maav 32,184,188
EX 139
Maav 36,184,192
WL 6
EX 140
Maav 40,188,176
s_nop 0
TR 208,12,224
TR 210,12,736
Maav 44,188,180
EX 141
Maav 48,188,184
EX 142
Maav 52,188,188
EX 143
Maav 56,188,192
EX 144
Maav 60,192,176
EX 145
Maav 64,192,180
EX 146
Maav 68,192,184
EX 147
Maav 72,192,188
EX 148
Maav 76,192,192
WL 4
EX 149
Maav 80,196,176
s_memrealtime s[94:95]
s_waitcnt vmcnt(16) lgkmcnt(0)
s_barrier
s_sub_u32 s95,s94,s96
s_cmp_lt_i32 s95,0
s_cbranch_scc1 LC_pc24
LC_pb24:
s_add_u32 s96,s94,90
Maav 84,196,180
s_add_u32 s4,s80,65536
RA 180,10,17312
CV 156,116,117
Maav 88,196,184
s_and_b32 s4,s4,0x1e000
RA 184,10,17376
CV 157,118,119
Maav 92,196,188
s_xor_b32 s5,s4,0x30000
EX 150
Maav 96,196,192
M0 78,141664
EX 151
Maav 100,200,176
RA 188,10,17440
CV 160,120,121
Maav 104,200,180
DN 19,32,4,0
Maav 108,200,184
s_add_u32 s98,s71,40960
EX 152
Maav 112,200,188
EX 153
Maav 116,200,192
EX 154
Maav 120,204,176
RA 192,10,17504
v_lshl_add_u32 v8,v2,1,s98
Maav 124,204,180
buffer_load_dword v8,v8,s[60:63],0 offen
Maav 128,204,184
EX 155
Maav 132,204,188
M0 78,145888
CV 161,122,123
CV 164,124,125
Maav 136,204,192
DN 33,32,4,0
Maav 140,208,176
RA 196,10,17824
CV 165,126,127
Maav 144,208,180
CV 168,128,129
CV 169,130,131
Maav 148,208,184
CV 172,132,133
CV 173,134,135
Maav 152,208,188
CV 158,136,137
CV 159,138,139
Maav 156,208,192
CV 162,140,141
CV 163,142,143
Maav 160,212,176
CV 166,144,145
CV 167,146,147
Maav 164,212,180
M0 78,150112
CV 170,148,149
CV 171,150,151
Maav 168,212,184
DN 19,32,5,0
Maav 172,212,188
CV 174,152,153
CV 175,154,155
Maav 176,212,192
WL 3
Mvavc 116,180,36,196
RA 200,10,17888
Mvav 116,184,40
Mvavc 120,180,52,200
Mvav 120,184,56
Mvavc 124,180,68,204
M0 78,154336
Mvav 124,184,72
RA 204,10,17952
Mvavc 128,180,84,208
DN 33,32,5,0
Mvav 128,184,88
Mvavc 132,180,100,212
Mvav 132,184,104
WL 3
Mvav 116,188,44
RA 208,10,18016
Mvav 116,192,48
Mvav 120,188,60
Mvav 120,192,64
M0 78,20480
Mvav 124,188,76
DN 19,36,6,0
Mvav 124,192,80
s_nop 0
TR 180,12,34624
TR 182,12,35136
Mvav 128,188,92
EX 116
Mvav 128,192,96
EX 117
Mvav 132,188,108
EX 118
Mvav 132,192,112
WL 4
EX 119
Mvavc 136,196,36,196
s_nop 0
TR 184,12,34656
TR 186,12,35168
Mvav 136,200,40
M0 78,24704
EX 120
Mvavc 140,196,52,200
DN 33,36,6,0
Mvav 140,200,56
EX 121
Mvavc 144,196,68,204
EX 122
Mvav 144,200,72
s_nop 0
TR 188,12,34688
TR 190,12,35200
Mvavc 148,196,84,208
EX 123
Mvav 148,200,88
EX 124
Mvavc 152,196,100,212
EX 125
Mvav 152,200,104
WL 6
EX 126
Mvav 136,204,44
M0 78,28928
TR 192,12,34720
TR 194,12,35232
Mvav 136,208,48
DN 19,36,7,0
Mvav 140,204,60
EX 127
Mvav 140,208,64
EX 128
Mvav 144,204,76
EX 129
Mvav 144,208,80
s_nop 0
TR 196,12,34752
TR 198,12,35264
Mvav 148,204,92
EX 130
Mvav 148,208,96
EX 131
Mvav 152,204,108
M0 78,33152
EX 132
Mvav 152,208,112
WL 6
DN 33,36,7,0
Maav 0,180,156
s_nop 0
TR 200,12,34784
TR 202,12,35296
Maav 4,180,160
EX 133
Maav 8,180,164
EX 134
Maav 12,180,168
EX 135
Maav 16,180,172
EX 136
Maav 20,184,156
s_nop 0
TR 204,12,34816
TR 206,12,35328
Maav 24,184,160
EX 137
Maav 28,184,164
EX 138
Maav 32,184,168
EX 139
Maav 36,184,172
WL 6
EX 140
Maav 40,188,156
s_nop 0
TR 208,12,34848
TR 210,12,35360
Maav 44,188,160
EX 141
Maav 48,188,164
EX 142
Maav 52,188,168
EX 143
Maav 56,188,172
EX 144
Maav 60,192,156
EX 145
Maav 64,192,160
EX 146
Maav 68,192,164
EX 147
Maav 72,192,168
EX 148
Maav 76,192,172
WL 4
EX 149
Maav 80,196,156
s_memrealtime s[94:95]
s_waitcnt vmcnt(17) lgkmcnt(0)
s_barrier
s_sub_u32 s95,s94,s96
s_cmp_lt_i32 s95,0
s_cbranch_scc1 LC_pc25
LC_pb25:
s_add_u32 s96,s94,90
Maav 84,196,160
s_add_u32 s6,s80,73728
RA 180,10,51936
CV 176,116,117
Maav 88,196,164
s_and_b32 s6,s6,0x1e000
RA 184,10,52000
CV 177,118,119
Maav 92,196,168
s_xor_b32 s7,s6,0x30000
EX 150
Maav 96,196,172
M0 78,37792
EX 151
Maav 100,200,156
RA 188,10,52064
CV 180,120,121
Maav 104,200,160
DN 19,32,6,0
Maav 108,200,164
EX 152
Maav 112,200,168
EX 153
Maav 116,200,172
EX 154
Maav 120,204,156
RA 192,10,52128
CV 181,122,123
Maav 124,204,160
EX 155
Maav 128,204,164
CV 184,124,125
CV 185,126,127
Maav 132,204,168
M0 78,42016
CV 188,128,129
CV 189,130,131
Maav 136,204,172
DN 33,32,6,0
Maav 140,208,156
RA 196,10,52448
CV 192,132,133
Maav 144,208,160
CV 193,134,135
CV 178,136,137
Maav 148,208,164
CV 179,138,139
CV 182,140,141
Maav 152,208,168
CV 183,142,143
CV 186,144,145
Maav 156,208,172
CV 187,146,147
CV 190,148,149
Maav 160,212,156
CV 191,150,151
CV 194,152,153
Maav 164,212,160
M0 78,46240
CV 195,154,155
Maav 168,212,164
DN 19,32,7,0
Maav 172,212,168
Maav 176,212,172
WL 3
Mvavc 116,180,36,196
RA 200,10,52512
Mvav 116,184,40
Mvavc 120,180,52,200
Mvav 120,184,56
Mvavc 124,180,68,204
M0 78,50464
Mvav 124,184,72
RA 204,10,52576
Mvavc 128,180,84,208
DN 33,32,7,0
Mvav 128,184,88
Mvavc 132,180,100,212
Mvav 132,184,104
WL 3
Mvav 116,188,44
RA 208,10,52640
Mvav 116,192,48
Mvav 120,188,60
Mvav 120,192,64
M0 78,55104
Mvav 124,188,76
DN 19,36,4,0
Mvav 124,192,80
s_nop 0
TR 180,13,0
TR 182,13,512
Mvav 128,188,92
EX 116
Mvav 128,192,96
EX 117
Mvav 132,188,108
EX 118
Mvav 132,192,112
WL 4
EX 119
Mvavc 136,196,36,196
s_nop 0
TR 184,13,32
TR 186,13,544
Mvav 136,200,40
M0 78,59328
EX 120
Mvavc 140,196,52,200
DN 33,36,4,0
Mvav 140,200,56
EX 121
Mvavc 144,196,68,204
EX 122
Mvav 144,200,72
s_nop 0
TR 188,13,64
TR 190,13,576
Mvavc 148,196,84,208
EX 123
Mvav 148,200,88
EX 124
Mvavc 152,196,100,212
EX 125
Mvav 152,200,104
WL 6
EX 126
Mvav 136,204,44
M0 78,63552
TR 192,13,96
TR 194,13,608
Mvav 136,208,48
DN 19,36,5,0
Mvav 140,204,60
EX 127
Mvav 140,208,64
EX 128
Mvav 144,204,76
EX 129
Mvav 144,208,80
s_nop 0
TR 196,13,128
TR 198,13,640
Mvav 148,204,92
EX 130
Mvav 148,208,96
EX 131
Mvav 152,204,108
M0 78,67776
EX 132
Mvav 152,208,112
WL 6
DN 33,36,5,0
Maav 0,180,176
s_nop 0
TR 200,13,160
TR 202,13,672
Maav 4,180,180
EX 133
Maav 8,180,184
EX 134
Maav 12,180,188
EX 135
Maav 16,180,192
EX 136
Maav 20,184,176
s_nop 0
TR 204,13,192
TR 206,13,704
Maav 24,184,180
EX 137
Maav 28,184,184
EX 138
Maav 32,184,188
EX 139
Maav 36,184,192
WL 6
EX 140
Maav 40,188,176
s_nop 0
TR 208,13,224
TR 210,13,736
Maav 44,188,180
EX 141
Maav 48,188,184
EX 142
Maav 52,188,188
EX 143
Maav 56,188,192
EX 144
Maav 60,192,176
EX 145
Maav 64,192,180
EX 146
Maav 68,192,184
EX 147
Maav 72,192,188
EX 148
Maav 76,192,192
WL 4
EX 149
Maav 80,196,176
s_memrealtime s[94:95]
s_waitcnt vmcnt(17) lgkmcnt(0)
s_barrier
s_sub_u32 s95,s94,s96
s_cmp_lt_i32 s95,0
s_cbranch_scc1 LC_pc26
LC_pb26:
s_add_u32 s96,s94,90
Maav 84,196,180
s_add_u32 s4,s80,81920
RA 180,11,17312
CV 156,116,117
Maav 88,196,184
s_and_b32 s4,s4,0x1e000
RA 184,11,17376
CV 157,118,119
Maav 92,196,188
s_xor_b32 s5,s4,0x30000
EX 150
Maav 96,196,192
M0 78,72416
EX 151
Maav 100,200,176
RA 188,11,17440
CV 160,120,121
Maav 104,200,180
DN 19,32,4,0
Maav 108,200,184
EX 152
Maav 112,200,188
EX 153
Maav 116,200,192
EX 154
Maav 120,204,176
RA 192,11,17504
CV 161,122,123
Maav 124,204,180
EX 155
Maav 128,204,184
CV 164,124,125
CV 165,126,127
Maav 132,204,188
M0 78,76640
CV 168,128,129
CV 169,130,131
Maav 136,204,192
DN 33,32,4,0
Maav 140,208,176
RA 196,11,17824
CV 172,132,133
Maav 144,208,180
CV 173,134,135
CV 158,136,137
Maav 148,208,184
CV 159,138,139
CV 162,140,141
Maav 152,208,188
CV 163,142,143
CV 166,144,145
Maav 156,208,192
CV 167,146,147
CV 170,148,149
Maav 160,212,176
CV 171,150,151
CV 174,152,153
Maav 164,212,180
M0 78,80864
CV 175,154,155
Maav 168,212,184
DN 19,32,5,0
Maav 172,212,188
Maav 176,212,192
WL 3
Mvavc 116,180,36,196
RA 200,11,17888
Mvav 116,184,40
Mvavc 120,180,52,200
Mvav 120,184,56
Mvavc 124,180,68,204
M0 78,85088
Mvav 124,184,72
RA 204,11,17952
Mvavc 128,180,84,208
DN 33,32,5,0
Mvav 128,184,88
Mvavc 132,180,100,212
Mvav 132,184,104
WL 3
Mvav 116,188,44
RA 208,11,18016
Mvav 116,192,48
Mvav 120,188,60
Mvav 120,192,64
M0 78,89728
Mvav 124,188,76
DN 19,36,6,0
Mvav 124,192,80
s_nop 0
TR 180,13,34624
TR 182,13,35136
Mvav 128,188,92
EX 116
Mvav 128,192,96
EX 117
Mvav 132,188,108
EX 118
Mvav 132,192,112
WL 4
EX 119
Mvavc 136,196,36,196
s_nop 0
TR 184,13,34656
TR 186,13,35168
Mvav 136,200,40
M0 78,93952
EX 120
Mvavc 140,196,52,200
DN 33,36,6,0
Mvav 140,200,56
EX 121
Mvavc 144,196,68,204
EX 122
Mvav 144,200,72
s_nop 0
TR 188,13,34688
TR 190,13,35200
Mvavc 148,196,84,208
EX 123
Mvav 148,200,88
EX 124
Mvavc 152,196,100,212
EX 125
Mvav 152,200,104
WL 6
EX 126
Mvav 136,204,44
M0 78,98176
TR 192,13,34720
TR 194,13,35232
Mvav 136,208,48
DN 19,36,7,0
Mvav 140,204,60
EX 127
Mvav 140,208,64
EX 128
Mvav 144,204,76
EX 129
Mvav 144,208,80
s_nop 0
TR 196,13,34752
TR 198,13,35264
Mvav 148,204,92
EX 130
Mvav 148,208,96
EX 131
Mvav 152,204,108
M0 78,102400
EX 132
Mvav 152,208,112
WL 6
DN 33,36,7,0
Maav 0,180,156
s_nop 0
TR 200,13,34784
TR 202,13,35296
Maav 4,180,160
EX 133
Maav 8,180,164
EX 134
Maav 12,180,168
EX 135
Maav 16,180,172
EX 136
Maav 20,184,156
s_nop 0
TR 204,13,34816
TR 206,13,35328
Maav 24,184,160
EX 137
Maav 28,184,164
EX 138
Maav 32,184,168
EX 139
Maav 36,184,172
WL 6
EX 140
Maav 40,188,156
s_nop 0
TR 208,13,34848
TR 210,13,35360
Maav 44,188,160
EX 141
Maav 48,188,164
EX 142
Maav 52,188,168
EX 143
Maav 56,188,172
EX 144
Maav 60,192,156
EX 145
Maav 64,192,160
EX 146
Maav 68,192,164
EX 147
Maav 72,192,168
EX 148
Maav 76,192,172
WL 4
EX 149
Maav 80,196,156
s_memrealtime s[94:95]
s_waitcnt vmcnt(16) lgkmcnt(0)
s_barrier
s_sub_u32 s95,s94,s96
s_cmp_lt_i32 s95,0
s_cbranch_scc1 LC_pc27
LC_pb27:
s_add_u32 s96,s94,90
Maav 84,196,160
s_add_u32 s6,s80,90112
RA 180,11,51936
CV 176,116,117
Maav 88,196,164
s_and_b32 s6,s6,0x1e000
RA 184,11,52000
CV 177,118,119
Maav 92,196,168
s_xor_b32 s7,s6,0x30000
EX 150
Maav 96,196,172
M0 78,107040
EX 151
Maav 100,200,156
RA 188,11,52064
CV 180,120,121
Maav 104,200,160
DN 19,32,6,0
Maav 108,200,164
EX 152
Maav 112,200,168
EX 153
Maav 116,200,172
EX 154
Maav 120,204,156
RA 192,11,52128
CV 181,122,123
Maav 124,204,160
EX 155
Maav 128,204,164
CV 184,124,125
CV 185,126,127
Maav 132,204,168
M0 78,111264
CV 188,128,129
CV 189,130,131
Maav 136,204,172
DN 33,32,6,0
Maav 140,208,156
RA 196,11,52448
CV 192,132,133
Maav 144,208,160
CV 193,134,135
CV 178,136,137
Maav 148,208,164
CV 179,138,139
CV 182,140,141
Maav 152,208,168
CV 183,142,143
CV 186,144,145
Maav 156,208,172
CV 187,146,147
CV 190,148,149
Maav 160,212,156
CV 191,150,151
CV 194,152,153
Maav 164,212,160
M0 78,115488
CV 195,154,155
Maav 168,212,164
DN 19,32,7,0
Maav 172,212,168
Maav 176,212,172
WL 3
Mvavc 116,180,36,196
RA 200,11,52512
Mvav 116,184,40
Mvavc 120,180,52,200
Mvav 120,184,56
Mvavc 124,180,68,204
M0 78,119712
Mvav 124,184,72
RA 204,11,52576
Mvavc 128,180,84,208
DN 33,32,7,0
Mvav 128,184,88
Mvavc 132,180,100,212
Mvav 132,184,104
WL 3
Mvav 116,188,44
RA 208,11,52640
Mvav 116,192,48
Mvav 120,188,60
Mvav 120,192,64
M0 78,124352
Mvav 124,188,76
DN 19,36,4,0
Mvav 124,192,80
s_nop 0
TR 180,12,0
TR 182,12,512
Mvav 128,188,92
EX 116
Mvav 128,192,96
EX 117
Mvav 132,188,108
EX 118
Mvav 132,192,112
WL 4
EX 119
Mvavc 136,196,36,196
s_nop 0
TR 184,12,32
TR 186,12,544
Mvav 136,200,40
M0 78,128576
EX 120
Mvavc 140,196,52,200
DN 33,36,4,0
Mvav 140,200,56
EX 121
Mvavc 144,196,68,204
EX 122
Mvav 144,200,72
s_nop 0
TR 188,12,64
TR 190,12,576
Mvavc 148,196,84,208
EX 123
Mvav 148,200,88
EX 124
Mvavc 152,196,100,212
EX 125
Mvav 152,200,104
WL 6
EX 126
Mvav 136,204,44
M0 78,132800
TR 192,12,96
TR 194,12,608
Mvav 136,208,48
DN 19,36,5,0
Mvav 140,204,60
EX 127
Mvav 140,208,64
EX 128
Mvav 144,204,76
EX 129
Mvav 144,208,80
s_nop 0
TR 196,12,128
TR 198,12,640
Mvav 148,204,92
EX 130
Mvav 148,208,96
EX 131
Mvav 152,204,108
M0 78,137024
EX 132
Mvav 152,208,112
WL 6
DN 33,36,5,0
Maav 0,180,176
s_nop 0
TR 200,12,160
TR 202,12,672
Maav 4,180,180
EX 133
Maav 8,180,184
EX 134
Maav 12,180,188
EX 135
Maav 16,180,192
EX 136
Maav 20,184,176
s_nop 0
TR 204,12,192
TR 206,12,704
Maav 24,184,180
EX 137
Maav 28,184,184
EX 138
Maav 32,184,188
EX 139
Maav 36,184,192
WL 6
EX 140
Maav 40,188,176
s_nop 0
TR 208,12,224
TR 210,12,736
Maav 44,188,180
EX 141
Maav 48,188,184
EX 142
Maav 52,188,188
EX 143
Maav 56,188,192
EX 144
Maav 60,192,176
EX 145
Maav 64,192,180
EX 146
Maav 68,192,184
EX 147
Maav 72,192,188
EX 148
Maav 76,192,192
WL 4
EX 149
Maav 80,196,176
s_memrealtime s[94:95]
s_waitcnt vmcnt(16) lgkmcnt(0)
s_barrier
s_sub_u32 s95,s94,s96
s_cmp_lt_i32 s95,0
s_cbranch_scc1 LC_pc28
LC_pb28:
s_add_u32 s96,s94,90
Maav 84,196,180
s_add_u32 s4,s80,98304
RA 180,10,17312
CV 156,116,117
Maav 88,196,184
s_and_b32 s4,s4,0x1e000
RA 184,10,17376
CV 157,118,119
Maav 92,196,188
s_xor_b32 s5,s4,0x30000
EX 150
Maav 96,196,192
M0 78,141664
EX 151
Maav 100,200,176
RA 188,10,17440
CV 160,120,121
Maav 104,200,180
DN 19,32,4,0
Maav 108,200,184
s_add_u32 s98,s71,49152
EX 152
Maav 112,200,188
EX 153
Maav 116,200,192
EX 154
Maav 120,204,176
RA 192,10,17504
v_lshl_add_u32 v8,v2,1,s98
Maav 124,204,180
buffer_load_dword v8,v8,s[60:63],0 offen
Maav 128,204,184
EX 155
Maav 132,204,188
M0 78,145888
CV 161,122,123
CV 164,124,125
Maav 136,204,192
DN 33,32,4,0
Maav 140,208,176
RA 196,10,17824
CV 165,126,127
Maav 144,208,180
CV 168,128,129
CV 169,130,131
Maav 148,208,184
CV 172,132,133
CV 173,134,135
Maav 152,208,188
CV 158,136,137
CV 159,138,139
Maav 156,208,192
CV 162,140,141
CV 163,142,143
Maav 160,212,176
CV 166,144,145
CV 167,146,147
Maav 164,212,180
M0 78,150112
CV 170,148,149
CV 171,150,151
Maav 168,212,184
DN 19,32,5,0
Maav 172,212,188
CV 174,152,153
CV 175,154,155
Maav 176,212,192
WL 3
Mvavc 116,180,36,196
RA 200,10,17888
Mvav 116,184,40
Mvavc 120,180,52,200
Mvav 120,184,56
Mvavc 124,180,68,204
M0 78,154336
Mvav 124,184,72
RA 204,10,17952
Mvavc 128,180,84,208
DN 33,32,5,0
Mvav 128,184,88
Mvavc 132,180,100,212
Mvav 132,184,104
WL 3
Mvav 116,188,44
RA 208,10,18016
Mvav 116,192,48
Mvav 120,188,60
Mvav 120,192,64
M0 78,20480
Mvav 124,188,76
DN 19,36,6,0
Mvav 124,192,80
s_nop 0
TR 180,12,34624
TR 182,12,35136
Mvav 128,188,92
EX 116
Mvav 128,192,96
EX 117
Mvav 132,188,108
EX 118
Mvav 132,192,112
WL 4
EX 119
Mvavc 136,196,36,196
s_nop 0
TR 184,12,34656
TR 186,12,35168
Mvav 136,200,40
M0 78,24704
EX 120
Mvavc 140,196,52,200
DN 33,36,6,0
Mvav 140,200,56
EX 121
Mvavc 144,196,68,204
EX 122
Mvav 144,200,72
s_nop 0
TR 188,12,34688
TR 190,12,35200
Mvavc 148,196,84,208
EX 123
Mvav 148,200,88
EX 124
Mvavc 152,196,100,212
EX 125
Mvav 152,200,104
WL 6
EX 126
Mvav 136,204,44
M0 78,28928
TR 192,12,34720
TR 194,12,35232
Mvav 136,208,48
DN 19,36,7,0
Mvav 140,204,60
EX 127
Mvav 140,208,64
EX 128
Mvav 144,204,76
EX 129
Mvav 144,208,80
s_nop 0
TR 196,12,34752
TR 198,12,35264
Mvav 148,204,92
EX 130
Mvav 148,208,96
EX 131
Mvav 152,204,108
M0 78,33152
EX 132
Mvav 152,208,112
WL 6
DN 33,36,7,0
Maav 0,180,156
s_nop 0
TR 200,12,34784
TR 202,12,35296
Maav 4,180,160
EX 133
Maav 8,180,164
EX 134
Maav 12,180,168
EX 135
Maav 16,180,172
EX 136
Maav 20,184,156
s_nop 0
TR 204,12,34816
TR 206,12,35328
Maav 24,184,160
EX 137
Maav 28,184,164
EX 138
Maav 32,184,168
EX 139
Maav 36,184,172
WL 6
EX 140
Maav 40,188,156
s_nop 0
TR 208,12,34848
TR 210,12,35360
Maav 44,188,160
EX 141
Maav 48,188,164
EX 142
Maav 52,188,168
EX 143
Maav 56,188,172
EX 144
Maav 60,192,156
EX 145
Maav 64,192,160
EX 146
Maav 68,192,164
EX 147
Maav 72,192,168
EX 148
Maav 76,192,172
WL 4
EX 149
Maav 80,196,156
s_memrealtime s[94:95]
s_waitcnt vmcnt(17) lgkmcnt(0)
s_barrier
s_sub_u32 s95,s94,s96
s_cmp_lt_i32 s95,0
s_cbranch_scc1 LC_pc29
LC_pb29:
s_add_u32 s96,s94,115
Maav 84,196,160
s_add_u32 s6,s80,106496
RA 180,10,51936
CV 176,116,117
Maav 88,196,164
s_and_b32 s6,s6,0x1e000
RA 184,10,52000
CV 177,118,119
Maav 92,196,168
s_xor_b32 s7,s6,0x30000
EX 150
Maav 96,196,172
M0 78,37792
EX 151
Maav 100,200,156
RA 188,10,52064
CV 180,120,121
Maav 104,200,160
DN 19,32,6,0
Maav 108,200,164
EX 152
Maav 112,200,168
EX 153
Maav 116,200,172
EX 154
Maav 120,204,156
RA 192,10,52128
CV 181,122,123
Maav 124,204,160
EX 155
Maav 128,204,164
CV 184,124,125
CV 185,126,127
Maav 132,204,168
M0 78,42016
CV 188,128,129
CV 189,130,131
Maav 136,204,172
DN 33,32,6,0
Maav 140,208,156
RA 196,10,52448
CV 192,132,133
Maav 144,208,160
CV 193,134,135
CV 178,136,137
Maav 148,208,164
CV 179,138,139
CV 182,140,141
Maav 152,208,168
CV 183,142,143
CV 186,144,145
Maav 156,208,172
CV 187,146,147
CV 190,148,149
Maav 160,212,156
CV 191,150,151
CV 194,152,153
Maav 164,212,160
M0 78,46240
CV 195,154,155
Maav 168,212,164
DN 19,32,7,0
Maav 172,212,168
Maav 176,212,172
WL 3
Mvavc 116,180,36,196
RA 200,10,52512
Mvav 116,184,40
Mvavc 120,180,52,200
Mvav 120,184,56
Mvavc 124,180,68,204
M0 78,50464
Mvav 124,184,72
RA 204,10,52576
Mvavc 128,180,84,208
DN 33,32,7,0
Mvav 128,184,88
Mvavc 132,180,100,212
Mvav 132,184,104
WL 3
Mvav 116,188,44
RA 208,10,52640
Mvav 116,192,48
Mvav 120,188,60
Mvav 120,192,64
M0 78,55104
Mvav 124,188,76
DN 19,36,4,0
Mvav 124,192,80
s_nop 0
TR 180,13,0
TR 182,13,512
Mvav 128,188,92
EX 116
Mvav 128,192,96
EX 117
Mvav 132,188,108
EX 118
Mvav 132,192,112
WL 4
EX 119
Mvavc 136,196,36,196
s_nop 0
TR 184,13,32
TR 186,13,544
Mvav 136,200,40
M0 78,59328
EX 120
Mvavc 140,196,52,200
DN 33,36,4,0
Mvav 140,200,56
EX 121
Mvavc 144,196,68,204
EX 122
Mvav 144,200,72
s_nop 0
TR 188,13,64
TR 190,13,576
Mvavc 148,196,84,208
EX 123
Mvav 148,200,88
EX 124
Mvavc 152,196,100,212
EX 125
Mvav 152,200,104
WL 6
EX 126
Mvav 136,204,44
M0 78,63552
TR 192,13,96
TR 194,13,608
Mvav 136,208,48
DN 19,36,5,0
Mvav 140,204,60
EX 127
Mvav 140,208,64
EX 128
Mvav 144,204,76
EX 129
Mvav 144,208,80
s_nop 0
TR 196,13,128
TR 198,13,640
Mvav 148,204,92
EX 130
Mvav 148,208,96
EX 131
Mvav 152,204,108
M0 78,67776
EX 132
Mvav 152,208,112
WL 6
DN 33,36,5,0
Maav 0,180,176
s_nop 0
TR 200,13,160
TR 202,13,672
Maav 4,180,180
EX 133
Maav 8,180,184
EX 134
Maav 12,180,188
EX 135
Maav 16,180,192
EX 136
Maav 20,184,176
s_nop 0
TR 204,13,192
TR 206,13,704
Maav 24,184,180
EX 137
Maav 28,184,184
EX 138
Maav 32,184,188
EX 139
Maav 36,184,192
WL 6
EX 140
Maav 40,188,176
s_nop 0
TR 208,13,224
TR 210,13,736
Maav 44,188,180
EX 141
Maav 48,188,184
EX 142
Maav 52,188,188
EX 143
Maav 56,188,192
EX 144
Maav 60,192,176
EX 145
Maav 64,192,180
EX 146
Maav 68,192,184
EX 147
Maav 72,192,188
EX 148
Maav 76,192,192
WL 4
EX 149
Maav 80,196,176
s_memrealtime s[94:95]
s_waitcnt vmcnt(17) lgkmcnt(0)
s_barrier
s_sub_u32 s95,s94,s96
s_cmp_lt_i32 s95,0
s_cbranch_scc1 LC_pc30
LC_pb30:
s_add_u32 s96,s94,115
Maav 84,196,180
s_add_u32 s4,s80,114688
RA 180,11,17312
CV 156,116,117
Maav 88,196,184
s_and_b32 s4,s4,0x1e000
RA 184,11,17376
CV 157,118,119
Maav 92,196,188
s_xor_b32 s5,s4,0x30000
EX 150
Maav 96,196,192
M0 78,72416
EX 151
Maav 100,200,176
RA 188,11,17440
CV 160,120,121
Maav 104,200,180
DN 19,32,4,0
Maav 108,200,184
EX 152
Maav 112,200,188
EX 153
Maav 116,200,192
EX 154
Maav 120,204,176
RA 192,11,17504
CV 161,122,123
Maav 124,204,180
EX 155
Maav 128,204,184
CV 164,124,125
CV 165,126,127
Maav 132,204,188
M0 78,76640
CV 168,128,129
CV 169,130,131
Maav 136,204,192
DN 33,32,4,0
Maav 140,208,176
RA 196,11,17824
CV 172,132,133
Maav 144,208,180
CV 173,134,135
CV 158,136,137
Maav 148,208,184
CV 159,138,139
CV 162,140,141
Maav 152,208,188
CV 163,142,143
CV 166,144,145
Maav 156,208,192
CV 167,146,147
CV 170,148,149
Maav 160,212,176
CV 171,150,151
CV 174,152,153
Maav 164,212,180
M0 78,80864
CV 175,154,155
Maav 168,212,184
DN 19,32,5,0
Maav 172,212,188
Maav 176,212,192
WL 3
Mvavc 116,180,36,196
RA 200,11,17888
Mvav 116,184,40
Mvavc 120,180,52,200
Mvav 120,184,56
Mvavc 124,180,68,204
M0 78,85088
Mvav 124,184,72
RA 204,11,17952
Mvavc 128,180,84,208
DN 33,32,5,0
Mvav 128,184,88
Mvavc 132,180,100,212
Mvav 132,184,104
WL 3
Mvav 116,188,44
RA 208,11,18016
Mvav 116,192,48
Mvav 120,188,60
Mvav 120,192,64
M0 78,89728
Mvav 124,188,76
DN 19,36,6,0
Mvav 124,192,80
s_nop 0
TR 180,13,34624
TR 182,13,35136
Mvav 128,188,92
EX 116
Mvav 128,192,96
EX 117
Mvav 132,188,108
EX 118
Mvav 132,192,112
WL 4
EX 119
Mvavc 136,196,36,196
s_nop 0
TR 184,13,34656
TR 186,13,35168
Mvav 136,200,40
M0 78,93952
EX 120
Mvavc 140,196,52,200
DN 33,36,6,0
Mvav 140,200,56
EX 121
Mvavc 144,196,68,204
EX 122
Mvav 144,200,72
s_nop 0
TR 188,13,34688
TR 190,13,35200
Mvavc 148,196,84,208
EX 123
Mvav 148,200,88
EX 124
Mvavc 152,196,100,212
EX 125
Mvav 152,200,104
WL 6
EX 126
Mvav 136,204,44
M0 78,98176
TR 192,13,34720
TR 194,13,35232
Mvav 136,208,48
DN 19,36,7,0
Mvav 140,204,60
EX 127
Mvav 140,208,64
EX 128
Mvav 144,204,76
EX 129
Mvav 144,208,80
s_nop 0
TR 196,13,34752
TR 198,13,35264
Mvav 148,204,92
EX 130
Mvav 148,208,96
EX 131
Mvav 152,204,108
M0 78,102400
EX 132
Mvav 152,208,112
WL 6
DN 33,36,7,0
Maav 0,180,156
s_nop 0
TR 200,13,34784
TR 202,13,35296
Maav 4,180,160
EX 133
Maav 8,180,164
EX 134
Maav 12,180,168
EX 135
Maav 16,180,172
EX 136
Maav 20,184,156
s_nop 0
TR 204,13,34816
TR 206,13,35328
Maav 24,184,160
EX 137
Maav 28,184,164
EX 138
Maav 32,184,168
EX 139
Maav 36,184,172
WL 6
EX 140
Maav 40,188,156
s_nop 0
TR 208,13,34848
TR 210,13,35360
Maav 44,188,160
EX 141
Maav 48,188,164
EX 142
Maav 52,188,168
EX 143
Maav 56,188,172
EX 144
Maav 60,192,156
EX 145
Maav 64,192,160
EX 146
Maav 68,192,164
EX 147
Maav 72,192,168
EX 148
Maav 76,192,172
WL 4
EX 149
Maav 80,196,156
s_memrealtime s[94:95]
s_waitcnt vmcnt(16) lgkmcnt(0)
s_barrier
s_sub_u32 s95,s94,s96
s_cmp_lt_i32 s95,0
s_cbranch_scc1 LC_pc31
LC_pb31:
s_add_u32 s96,s94,115
Maav 84,196,160
s_add_u32 s6,s80,122880
RA 180,11,51936
CV 176,116,117
Maav 88,196,164
s_and_b32 s6,s6,0x1e000
RA 184,11,52000
CV 177,118,119
Maav 92,196,168
s_xor_b32 s7,s6,0x30000
EX 150
Maav 96,196,172
M0 78,107040
EX 151
Maav 100,200,156
RA 188,11,52064
CV 180,120,121
Maav 104,200,160
DN 19,32,6,0
Maav 108,200,164
EX 152
Maav 112,200,168
EX 153
Maav 116,200,172
EX 154
Maav 120,204,156
RA 192,11,52128
CV 181,122,123
Maav 124,204,160
EX 155
Maav 128,204,164
CV 184,124,125
CV 185,126,127
Maav 132,204,168
M0 78,111264
CV 188,128,129
CV 189,130,131
Maav 136,204,172
DN 33,32,6,0
Maav 140,208,156
RA 196,11,52448
CV 192,132,133
Maav 144,208,160
CV 193,134,135
CV 178,136,137
Maav 148,208,164
CV 179,138,139
CV 182,140,141
Maav 152,208,168
CV 183,142,143
CV 186,144,145
Maav 156,208,172
CV 187,146,147
CV 190,148,149
Maav 160,212,156
CV 191,150,151
CV 194,152,153
Maav 164,212,160
M0 78,115488
CV 195,154,155
Maav 168,212,164
DN 19,32,7,0
Maav 172,212,168
Maav 176,212,172
WL 3
Mvavc 116,180,36,196
RA 200,11,52512
Mvav 116,184,40
Mvavc 120,180,52,200
Mvav 120,184,56
Mvavc 124,180,68,204
M0 78,119712
Mvav 124,184,72
RA 204,11,52576
Mvavc 128,180,84,208
DN 33,32,7,0
Mvav 128,184,88
Mvavc 132,180,100,212
Mvav 132,184,104
WL 3
Mvav 116,188,44
RA 208,11,52640
Mvav 116,192,48
Mvav 120,188,60
Mvav 120,192,64
M0 78,124352
Mvav 124,188,76
DN 19,36,4,0
Mvav 124,192,80
s_nop 0
TR 180,12,0
TR 182,12,512
Mvav 128,188,92
EX 116
Mvav 128,192,96
EX 117
Mvav 132,188,108
EX 118
Mvav 132,192,112
WL 4
EX 119
Mvavc 136,196,36,196
s_nop 0
TR 184,12,32
TR 186,12,544
Mvav 136,200,40
M0 78,128576
EX 120
Mvavc 140,196,52,200
DN 33,36,4,0
Mvav 140,200,56
EX 121
Mvavc 144,196,68,204
EX 122
Mvav 144,200,72
s_nop 0
TR 188,12,64
TR 190,12,576
Mvavc 148,196,84,208
EX 123
Mvav 148,200,88
EX 124
Mvavc 152,196,100,212
EX 125
Mvav 152,200,104
WL 6
EX 126
Mvav 136,204,44
M0 78,132800
TR 192,12,96
TR 194,12,608
Mvav 136,208,48
DN 19,36,5,0
Mvav 140,204,60
EX 127
Mvav 140,208,64
EX 128
Mvav 144,204,76
EX 129
Mvav 144,208,80
s_nop 0
TR 196,12,128
TR 198,12,640
Mvav 148,204,92
EX 130
Mvav 148,208,96
EX 131
Mvav 152,204,108
M0 78,137024
EX 132
Mvav 152,208,112
WL 6
DN 33,36,5,0
Maav 0,180,176
s_nop 0
TR 200,12,160
TR 202,12,672
Maav 4,180,180
EX 133
Maav 8,180,184
EX 134
Maav 12,180,188
EX 135
Maav 16,180,192
EX 136
Maav 20,184,176
s_nop 0
TR 204,12,192
TR 206,12,704
Maav 24,184,180
EX 137
Maav 28,184,184
EX 138
Maav 32,184,188
EX 139
Maav 36,184,192
WL 6
EX 140
Maav 40,188,176
s_nop 0
TR 208,12,224
TR 210,12,736
Maav 44,188,180
EX 141
Maav 48,188,184
EX 142
Maav 52,188,188
EX 143
Maav 56,188,192
EX 144
Maav 60,192,176
EX 145
Maav 64,192,180
EX 146
Maav 68,192,184
EX 147
Maav 72,192,188
EX 148
Maav 76,192,192
WL 4
EX 149
Maav 80,196,176
s_memrealtime s[94:95]
s_waitcnt vmcnt(16) lgkmcnt(0)
s_barrier
s_sub_u32 s95,s94,s96
s_cmp_lt_i32 s95,0
s_cbranch_scc1 LC_pc32
LC_pb32:
s_add_u32 s96,s94,115
Maav 84,196,180
M0 78,141664
RA 180,10,17312
CV 156,116,117
Maav 88,196,184
RA 184,10,17376
CV 157,118,119
Maav 92,196,188
DN 19,36,6,0
Maav 96,196,192
EX 150
Maav 100,200,176
RA 188,10,17440
CV 160,120,121
Maav 104,200,180
s_add_u32 s98,s71,57344
v_lshl_add_u32 v8,v2,1,s98
CV 161,122,123
Maav 108,200,184
buffer_load_dword v8,v8,s[60:63],0 offen
Maav 112,200,188
EX 151
Maav 116,200,192
EX 152
Maav 120,204,176
M0 78,145888
RA 192,10,17504
CV 164,124,125
Maav 124,204,180
DN 33,36,6,0
Maav 128,204,184
EX 153
Maav 132,204,188
EX 154
Maav 136,204,192
EX 155
Maav 140,208,176
RA 196,10,17824
CV 165,126,127
Maav 144,208,180
CV 168,128,129
CV 169,130,131
Maav 148,208,184
CV 172,132,133
CV 173,134,135
Maav 152,208,188
M0 78,150112
CV 158,136,137
CV 159,138,139
Maav 156,208,192
DN 19,36,7,0
Maav 160,212,176
CV 162,140,141
CV 163,142,143
Maav 164,212,180
CV 166,144,145
CV 167,146,147
Maav 168,212,184
CV 170,148,149
CV 171,150,151
Maav 172,212,188
CV 175,154,155
Maav 176,212,192
WL 3
Mvavc 116,180,36,196
RA 200,10,17888
CV 174,152,153
Mvav 116,184,40
M0 78,154336
Mvavc 120,180,52,200
DN 33,36,7,0
Mvav 120,184,56
Mvavc 124,180,68,204
Mvav 124,184,72
RA 204,10,17952
Mvavc 128,180,84,208
Mvav 128,184,88
Mvavc 132,180,100,212
Mvav 132,184,104
WL 3
Mvav 116,188,44
s_add_u32 s4,s80,0
RA 208,10,18016
Mvav 116,192,48
s_and_b32 s4,s4,0x1e000
Mvav 120,188,60
s_or_b32 s4,s4,0x40000
Mvav 120,192,64
s_xor_b32 s5,s4,0x30000
M0 78,20480
Mvav 124,188,76
DN 19,32,4,0
Mvav 124,192,80
s_nop 0
TR 180,12,34624
TR 182,12,35136
Mvav 128,188,92
EX 116
Mvav 128,192,96
EX 117
Mvav 132,188,108
EX 118
Mvav 132,192,112
WL 4
EX 119
Mvavc 136,196,36,196
s_nop 0
TR 184,12,34656
TR 186,12,35168
Mvav 136,200,40
M0 78,24704
EX 120
Mvavc 140,196,52,200
DN 33,32,4,0
Mvav 140,200,56
EX 121
Mvavc 144,196,68,204
EX 122
Mvav 144,200,72
s_nop 0
TR 188,12,34688
TR 190,12,35200
Mvavc 148,196,84,208
EX 123
Mvav 148,200,88
EX 124
Mvavc 152,196,100,212
EX 125
Mvav 152,200,104
WL 6
EX 126
Mvav 136,204,44
M0 78,28928
TR 192,12,34720
TR 194,12,35232
Mvav 136,208,48
DN 19,32,5,0
Mvav 140,204,60
EX 127
Mvav 140,208,64
EX 128
Mvav 144,204,76
EX 129
Mvav 144,208,80
s_nop 0
TR 196,12,34752
TR 198,12,35264
Mvav 148,204,92
EX 130
Mvav 148,208,96
EX 131
Mvav 152,204,108
M0 78,33152
EX 132
Mvav 152,208,112
WL 6
DN 33,32,5,0
Maav 0,180,156
s_nop 0
TR 200,12,34784
TR 202,12,35296
Maav 4,180,160
EX 133
Maav 8,180,164
EX 134
Maav 12,180,168
EX 135
Maav 16,180,172
EX 136
Maav 20,184,156
s_nop 0
TR 204,12,34816
TR 206,12,35328
Maav 24,184,160
EX 137
Maav 28,184,164
EX 138
Maav 32,184,168
EX 139
Maav 36,184,172
WL 6
EX 140
Maav 40,188,156
s_nop 0
TR 208,12,34848
TR 210,12,35360
Maav 44,188,160
EX 141
Maav 48,188,164
EX 142
Maav 52,188,168
EX 143
Maav 56,188,172
EX 144
Maav 60,192,156
EX 145
Maav 64,192,160
EX 146
Maav 68,192,164
EX 147
Maav 72,192,168
EX 148
Maav 76,192,172
WL 4
EX 149
Maav 80,196,156
s_memrealtime s[94:95]
s_waitcnt vmcnt(17) lgkmcnt(0)
s_barrier
s_sub_u32 s95,s94,s96
s_cmp_lt_i32 s95,0
s_cbranch_scc1 LC_pc33
LC_pb33:
s_add_u32 s96,s94,115
Maav 84,196,160
s_add_u32 s6,s80,8192
RA 180,10,51936
CV 176,116,117
Maav 88,196,164
s_and_b32 s6,s6,0x1e000
RA 184,10,52000
CV 177,118,119
Maav 92,196,168
s_or_b32 s6,s6,0x40000
EX 150
Maav 96,196,172
s_xor_b32 s7,s6,0x30000
EX 151
Maav 100,200,156
M0 78,37792
RA 188,10,52064
CV 180,120,121
Maav 104,200,160
DN 19,32,6,0
Maav 108,200,164
EX 152
Maav 112,200,168
EX 153
Maav 116,200,172
EX 154
Maav 120,204,156
RA 192,10,52128
CV 181,122,123
Maav 124,204,160
EX 155
Maav 128,204,164
CV 184,124,125
CV 185,126,127
Maav 132,204,168
M0 78,42016
CV 188,128,129
CV 189,130,131
Maav 136,204,172
DN 33,32,6,0
Maav 140,208,156
RA 196,10,52448
CV 192,132,133
Maav 144,208,160
CV 193,134,135
CV 178,136,137
Maav 148,208,164
CV 179,138,139
CV 182,140,141
Maav 152,208,168
CV 183,142,143
CV 186,144,145
Maav 156,208,172
CV 187,146,147
CV 190,148,149
Maav 160,212,156
CV 191,150,151
CV 194,152,153
Maav 164,212,160
M0 78,46240
CV 195,154,155
Maav 168,212,164
DN 19,32,7,0
Maav 172,212,168
Maav 176,212,172
WL 3
Mvavc 116,180,36,196
RA 200,10,52512
Mvav 116,184,40
Mvavc 120,180,52,200
Mvav 120,184,56
Mvavc 124,180,68,204
M0 78,50464
Mvav 124,184,72
RA 204,10,52576
Mvavc 128,180,84,208
DN 33,32,7,0
Mvav 128,184,88
Mvavc 132,180,100,212
Mvav 132,184,104
WL 3
Mvav 116,188,44
RA 208,10,52640
Mvav 116,192,48
Mvav 120,188,60
Mvav 120,192,64
M0 78,55104
Mvav 124,188,76
DN 19,36,4,0
Mvav 124,192,80
s_nop 0
TR 180,13,0
TR 182,13,512
Mvav 128,188,92
EX 116
Mvav 128,192,96
EX 117
Mvav 132,188,108
EX 118
Mvav 132,192,112
WL 4
EX 119
Mvavc 136,196,36,196
s_nop 0
TR 184,13,32
TR 186,13,544
Mvav 136,200,40
M0 78,59328
EX 120
Mvavc 140,196,52,200
DN 33,36,4,0
Mvav 140,200,56
EX 121
Mvavc 144,196,68,204
EX 122
Mvav 144,200,72
s_nop 0
TR 188,13,64
TR 190,13,576
Mvavc 148,196,84,208
EX 123
Mvav 148,200,88
EX 124
Mvavc 152,196,100,212
EX 125
Mvav 152,200,104
WL 6
EX 126
Mvav 136,204,44
M0 78,63552
TR 192,13,96
TR 194,13,608
Mvav 136,208,48
DN 19,36,5,0
Mvav 140,204,60
EX 127
Mvav 140,208,64
EX 128
Mvav 144,204,76
EX 129
Mvav 144,208,80
s_nop 0
TR 196,13,128
TR 198,13,640
Mvav 148,204,92
EX 130
Mvav 148,208,96
EX 131
Mvav 152,204,108
M0 78,67776
EX 132
Mvav 152,208,112
WL 6
DN 33,36,5,0
Maav 0,180,176
s_nop 0
TR 200,13,160
TR 202,13,672
Maav 4,180,180
EX 133
Maav 8,180,184
EX 134
Maav 12,180,188
EX 135
Maav 16,180,192
EX 136
Maav 20,184,176
s_nop 0
TR 204,13,192
TR 206,13,704
Maav 24,184,180
EX 137
Maav 28,184,184
EX 138
Maav 32,184,188
EX 139
Maav 36,184,192
WL 6
EX 140
Maav 40,188,176
s_nop 0
TR 208,13,224
TR 210,13,736
Maav 44,188,180
EX 141
Maav 48,188,184
EX 142
Maav 52,188,188
EX 143
Maav 56,188,192
EX 144
Maav 60,192,176
EX 145
Maav 64,192,180
EX 146
Maav 68,192,184
EX 147
Maav 72,192,188
EX 148
Maav 76,192,192
WL 4
EX 149
Maav 80,196,176
s_memrealtime s[94:95]
s_waitcnt vmcnt(17) lgkmcnt(0)
s_barrier
s_sub_u32 s95,s94,s96
s_cmp_lt_i32 s95,0
s_cbranch_scc1 LC_pc34
LC_pb34:
s_add_u32 s96,s94,115
Maav 84,196,180
s_mov_b32 s79,512
RA 180,11,17312
CV 156,116,117
Maav 88,196,184
M0 77,89728
RA 184,11,17376
CV 157,118,119
Maav 92,196,188
DM 20,28,79,0
Maav 96,196,192
s_mov_b32 s79,8704
EX 150
Maav 100,200,176
M0 77,90816
RA 188,11,17440
CV 160,120,121
Maav 104,200,180
DM 20,28,79,0
Maav 108,200,184
s_mov_b32 s79,16896
EX 151
Maav 112,200,188
M0 77,91904
EX 152
Maav 116,200,192
DM 20,28,79,0
Maav 120,204,176
s_mov_b32 s79,25088
RA 192,11,17504
CV 161,122,123
Maav 124,204,180
M0 77,92992
EX 153
Maav 128,204,184
DM 20,28,79,0
Maav 132,204,188
s_mov_b32 s79,0
EX 154
Maav 136,204,192
M0 69,0
EX 155
Maav 140,208,176
RA 196,11,17824
CV 164,124,125
Maav 144,208,180
DM 21,24,79,0
Maav 148,208,184
s_mov_b32 s79,36864
CV 165,126,127
CV 168,128,129
Maav 152,208,188
M0 69,4096
CV 169,130,131
CV 172,132,133
Maav 156,208,192
DM 21,24,79,0
Maav 160,212,176
s_mov_b32 s79,73728
CV 173,134,135
CV 158,136,137
Maav 164,212,180
M0 69,8192
CV 159,138,139
CV 162,140,141
Maav 168,212,184
DM 21,24,79,0
Maav 172,212,188
s_mov_b32 s79,110592
CV 163,142,143
CV 166,144,145
Maav 176,212,192
WL 3
CV 167,146,147
CV 170,148,149
Mvavc 116,180,36,196
M0 69,12288
RA 200,11,17888
CV 171,150,151
Mvav 116,184,40
DM 21,24,79,0
Mvavc 120,180,52,200
s_mov_b32 s79,147456
CV 174,152,153
CV 175,154,155
Mvav 120,184,56
M0 69,16384
Mvavc 124,180,68,204
DM 21,24,79,0
Mvav 124,184,72
s_add_u32 s4,s80,16384
RA 204,11,17952
Mvavc 128,180,84,208
s_and_b32 s4,s4,0x1e000
Mvav 128,184,88
s_or_b32 s4,s4,0x40000
Mvavc 132,180,100,212
s_xor_b32 s5,s4,0x30000
Mvav 132,184,104
WL 3
Mvav 116,188,44
M0 78,72416
RA 208,11,18016
Mvav 116,192,48
DN 19,32,4,0
Mvav 120,188,60
Mvav 120,192,64
Mvav 124,188,76
Mvav 124,192,80
s_nop 0
TR 180,13,34624
TR 182,13,35136
Mvav 128,188,92
EX 116
Mvav 128,192,96
EX 117
Mvav 132,188,108
M0 78,76640
EX 118
Mvav 132,192,112
WL 4
DN 33,32,4,0
Mvavc 136,196,36,196
s_nop 0
TR 184,13,34656
TR 186,13,35168
Mvav 136,200,40
EX 119
Mvavc 140,196,52,200
EX 120
Mvav 140,200,56
EX 121
Mvavc 144,196,68,204
EX 122
Mvav 144,200,72
s_nop 0
TR 188,13,34688
TR 190,13,35200
Mvavc 148,196,84,208
M0 78,80864
EX 123
Mvav 148,200,88
DN 19,32,5,0
Mvavc 152,196,100,212
EX 124
Mvav 152,200,104
WL 6
EX 125
Mvav 136,204,44
s_nop 0
TR 192,13,34720
TR 194,13,35232
Mvav 136,208,48
EX 126
Mvav 140,204,60
EX 127
Mvav 140,208,64
EX 128
Mvav 144,204,76
M0 78,85088
EX 129
Mvav 144,208,80
s_nop 0
TR 196,13,34752
TR 198,13,35264
Mvav 148,204,92
DN 33,32,5,0
Mvav 148,208,96
EX 130
Mvav 152,204,108
EX 131
Mvav 152,208,112
WL 6
EX 132
Maav 0,180,156
s_nop 0
TR 200,13,34784
TR 202,13,35296
Maav 4,180,160
EX 133
Maav 8,180,164
EX 134
Maav 12,180,168
EX 135
Maav 16,180,172
EX 136
Maav 20,184,156
s_nop 0
TR 204,13,34816
TR 206,13,35328
Maav 24,184,160
EX 137
Maav 28,184,164
EX 138
CV 178,136,137
Maav 32,184,168
EX 139
Maav 36,184,172
WL 6
EX 140
CV 179,138,139
Maav 40,188,156
s_nop 0
TR 208,13,34848
TR 210,13,35360
Maav 44,188,160
EX 141
Maav 48,188,164
EX 142
CV 182,140,141
Maav 52,188,168
EX 143
Maav 56,188,172
EX 144
CV 183,142,143
Maav 60,192,156
EX 145
Maav 64,192,160
EX 146
CV 186,144,145
Maav 68,192,164
EX 147
Maav 72,192,168
EX 148
CV 187,146,147
Maav 76,192,172
WL 4
EX 149
Maav 80,196,156
s_memrealtime s[94:95]
s_waitcnt vmcnt(25) lgkmcnt(0)
s_barrier
s_sub_u32 s95,s94,s96
s_cmp_lt_i32 s95,0
s_cbranch_scc1 LC_pc35
LC_pb35:
s_add_u32 s96,s94,115
CV 190,148,149
Maav 84,196,160
s_mov_b32 s79,262656
TR 180,13,51936
TR 182,13,52448
Maav 88,196,164
M0 77,107040
TR 184,13,51968
TR 186,13,52480
Maav 92,196,168
DM 20,28,79,0
Maav 96,196,172
s_mov_b32 s79,270848
EX 150
Maav 100,200,156
M0 77,108128
TR 188,13,52000
TR 190,13,52512
Maav 104,200,160
DM 20,28,79,0
Maav 108,200,164
s_mov_b32 s79,279040
EX 151
Maav 112,200,168
M0 77,109216
EX 152
CV 191,150,151
Maav 116,200,172
DM 20,28,79,0
Maav 120,204,156
s_mov_b32 s79,287232
TR 192,13,52032
TR 194,13,52544
Maav 124,204,160
M0 77,110304
EX 153
Maav 128,204,164
DM 20,28,79,0
CV 194,152,153
Maav 132,204,168
s_mov_b32 s79,524800
EX 154
Maav 136,204,172
M0 77,124352
EX 155
Maav 140,208,156
s_nop 0
TR 196,13,52064
TR 198,13,52576
CV 195,154,155
Maav 144,208,160
DM 20,28,79,0
Maav 148,208,164
s_mov_b32 s79,532992
CV 176,116,117
CV 177,118,119
Maav 152,208,168
M0 77,125440
CV 180,120,121
CV 181,122,123
Maav 156,208,172
DM 20,28,79,0
Maav 160,212,156
s_mov_b32 s79,541184
CV 184,124,125
CV 185,126,127
Maav 164,212,160
M0 77,126528
CV 188,128,129
CV 189,130,131
Maav 168,212,164
DM 20,28,79,0
Maav 172,212,168
s_mov_b32 s79,549376
CV 192,132,133
CV 193,134,135
Maav 176,212,172
M0 77,127616
Maav 160,212,176
DM 20,28,79,0
Maav 164,212,180
Maav 168,212,184
Maav 172,212,188
Maav 176,212,192
WL 6
Maav 0,180,176
s_nop 0
TR 200,13,52096
TR 202,13,52608
Maav 4,180,180
Maav 8,180,184
Maav 12,180,188
AR 28,160
AR 29,164
Maav 16,180,192
AR 30,168
AR 31,172
Maav 20,184,176
s_nop 0
TR 204,13,52128
TR 206,13,52640
Maav 24,184,180
AR 32,176
Maav 28,184,184
v_cmp_nle_f32 vcc,v28,s75
s_nop 0
s_or_b64 s[86:87],s[86:87],vcc
Maav 32,184,188
v_cmp_nle_f32 vcc,v29,s75
s_nop 0
s_or_b64 s[86:87],s[86:87],vcc
Maav 36,184,192
WL 6
v_cmp_nle_f32 vcc,v30,s75
s_nop 0
s_or_b64 s[86:87],s[86:87],vcc
Maav 40,188,176
s_nop 0
TR 208,13,52160
TR 210,13,52672
Maav 44,188,180
v_cmp_nle_f32 vcc,v31,s75
s_nop 0
s_or_b64 s[86:87],s[86:87],vcc
Maav 48,188,184
v_cmp_nle_f32 vcc,v32,s75
s_nop 0
s_or_b64 s[86:87],s[86:87],vcc
Maav 52,188,188
v_rcp_f32 v28,v28
Maav 56,188,192
v_rcp_f32 v29,v29
Maav 60,192,176
v_rcp_f32 v30,v30
Maav 64,192,180
v_rcp_f32 v31,v31
Maav 68,192,184
v_rcp_f32 v32,v32
Maav 72,192,188
AR 196,0
AR 197,1
Maav 76,192,192
WL 4
AR 198,2
AR 199,3
Maav 80,196,176
s_waitcnt vmcnt(12) lgkmcnt(0)
s_barrier
Maav 84,196,180
s_nop 0
TR 180,14,0
TR 182,14,256
Maav 88,196,184
RA 216,15,0
AR 200,20
Maav 92,196,188
RA 220,15,2048
AR 201,21
Maav 96,196,192
RA 224,15,4096
AR 202,22
Maav 100,200,176
s_mov_b32 s79,128
RA 228,15,6144
AR 203,23
Maav 104,200,180
M0 69,141664
RA 232,15,8192
v_mul_f32 v196,v196,v28
Maav 108,200,184
s_nop 0
TR 184,14,8
TR 186,14,264
Maav 112,200,188
s_nop 0
TR 188,14,64
TR 190,14,320
Maav 116,200,192
DM 21,24,79,0
Maav 120,204,176
s_mov_b32 s79,36992
TR 192,14,72
TR 194,14,328
Maav 124,204,180
M0 69,145760
v_mul_f32 v197,v197,v28
v_mul_f32 v198,v198,v28
Maav 128,204,184
DM 21,24,79,0
Maav 132,204,188
s_mov_b32 s79,73856
v_mul_f32 v199,v199,v28
v_mul_f32 v200,v200,v28
Maav 136,204,192
M0 69,149856
v_mul_f32 v201,v201,v28
v_mul_f32 v202,v202,v28
Maav 140,208,176
s_nop 0
TR 196,14,128
TR 198,14,384
Maav 144,208,180
DM 21,24,79,0
Maav 148,208,184
s_mov_b32 s79,110720
v_mul_f32 v203,v203,v28
CV 216,196,197
Maav 152,208,188
M0 69,153952
CV 217,198,199
CV 218,200,201
Maav 156,208,192
WL 6
DM 21,24,79,0
Mvaaz 36,180,216
s_mov_b32 s79,147584
TR 200,14,136
TR 202,14,392
Mvaaz 52,180,220
M0 69,158048
CV 219,202,203
AR 204,4
Mvaaz 68,180,224
DM 21,24,79,0
Mvaaz 84,180,228
AR 205,5
AR 206,6
Mvaaz 100,180,232
AR 207,7
AR 208,24
Mvaaz 116,184,216
s_nop 0
TR 204,14,192
TR 206,14,448
Mvaaz 120,184,220
AR 209,25
AR 210,26
Mvaaz 124,184,224
AR 211,27
v_mul_f32 v204,v204,v29
Mvaaz 128,184,228
v_mul_f32 v205,v205,v29
v_mul_f32 v206,v206,v29
Mvaaz 132,184,232
WL 6
v_mul_f32 v207,v207,v29
v_mul_f32 v208,v208,v29
Mvaaz 40,188,216
s_nop 0
TR 208,14,200
TR 210,14,456
Mvaaz 56,188,220
v_mul_f32 v209,v209,v29
v_mul_f32 v210,v210,v29
Mvaaz 72,188,224
v_mul_f32 v211,v211,v29
CV 220,204,205
Mvaaz 88,188,228
CV 221,206,207
CV 222,208,209
Mvaaz 104,188,232
CV 223,210,211
AR 196,8
Mvaaz 136,192,216
AR 197,9
AR 198,10
Mvaaz 140,192,220
AR 199,11
AR 200,28
Mvaaz 144,192,224
AR 201,29
AR 202,30
Mvaaz 148,192,228
AR 203,31
v_mul_f32 v196,v196,v30
Mvaaz 152,192,232
WL 4
v_mul_f32 v197,v197,v30
v_mul_f32 v198,v198,v30
Mvaaz 44,196,216
s_waitcnt vmcnt(9) lgkmcnt(0)
s_barrier
Mvaaz 60,196,220
s_add_u32 s98,s71,65536
TR 180,14,17312
TR 182,14,17568
Mvaaz 76,196,224
RA 236,16,0
v_lshl_add_u32 v8,v2,1,s98
Mvaaz 92,196,228
RA 240,16,2048
v_mul_f32 v199,v199,v30
Mvaaz 108,196,232
RA 244,16,4096
v_mul_f32 v200,v200,v30
Mvaaz 156,200,216
s_mov_b32 s79,786944
RA 248,16,6144
v_mul_f32 v201,v201,v30
Mvaaz 160,200,220
M0 77,89728
RA 252,16,8192
v_mul_f32 v202,v202,v30
Mvaaz 164,200,224
s_nop 0
TR 184,14,17320
TR 186,14,17576
Mvaaz 168,200,228
s_nop 0
TR 188,14,17376
TR 190,14,17632
Mvaaz 172,200,232
DM 20,28,79,0
Mvaaz 48,204,216
s_mov_b32 s79,795136
TR 192,14,17384
TR 194,14,17640
Mvaaz 64,204,220
M0 77,90816
v_mul_f32 v203,v203,v30
CV 224,196,197
Mvaaz 80,204,224
DM 20,28,79,0
Mvaaz 96,204,228
s_mov_b32 s79,803328
CV 225,198,199
CV 226,200,201
Mvaaz 112,204,232
M0 77,91904
CV 227,202,203
AR 204,12
Mvaaz 176,208,216
s_nop 0
TR 196,14,17440
TR 198,14,17696
Mvaaz 180,208,220
DM 20,28,79,0
Mvaaz 184,208,224
s_mov_b32 s79,811520
AR 205,13
AR 206,14
Mvaaz 188,208,228
M0 77,92992
AR 207,15
AR 208,32
Mvaaz 192,208,232
WL 6
DM 20,28,79,0
Mvaa 36,180,236
s_nop 0
TR 200,14,17448
TR 202,14,17704
Mvaa 52,180,240
AR 209,33
AR 210,34
Mvaa 68,180,244
buffer_load_dword v8,v8,s[60:63],0 offen
Mvaa 84,180,248
AR 211,35
v_mul_f32 v204,v204,v31
Mvaa 100,180,252
v_mul_f32 v205,v205,v31
v_mul_f32 v206,v206,v31
Mvaa 116,184,236
s_nop 0
TR 204,14,17504
TR 206,14,17760
Mvaa 120,184,240
v_mul_f32 v207,v207,v31
v_mul_f32 v208,v208,v31
Mvaa 124,184,244
v_mul_f32 v209,v209,v31
v_mul_f32 v210,v210,v31
Mvaa 128,184,248
v_mul_f32 v211,v211,v31
CV 228,204,205
Mvaa 132,184,252
WL 6
CV 229,206,207
CV 230,208,209
Mvaa 40,188,236
s_nop 0
TR 208,14,17512
TR 210,14,17768
Mvaa 56,188,240
CV 231,210,211
AR 196,16
Mvaa 72,188,244
AR 197,17
AR 198,18
Mvaa 88,188,248
AR 199,19
AR 200,36
Mvaa 104,188,252
AR 201,37
AR 202,38
Mvaa 136,192,236
AR 203,39
v_mul_f32 v196,v196,v32
Mvaa 140,192,240
v_mul_f32 v197,v197,v32
v_mul_f32 v198,v198,v32
Mvaa 144,192,244
v_mul_f32 v199,v199,v32
v_mul_f32 v200,v200,v32
Mvaa 148,192,248
v_mul_f32 v201,v201,v32
v_mul_f32 v202,v202,v32
Mvaa 152,192,252
WL 4
v_mul_f32 v203,v203,v32
CV 232,196,197
Mvaa 44,196,236
s_waitcnt vmcnt(5) lgkmcnt(0)
s_barrier
Mvaa 60,196,240
s_nop 0
TR 180,14,34624
TR 182,14,34880
Mvaa 76,196,244
RA 216,17,0
CV 233,198,199
Mvaa 92,196,248
RA 220,17,2048
CV 234,200,201
Mvaa 108,196,252
RA 224,17,4096
CV 235,202,203
Mvaa 156,200,236
s_mov_b32 s79,256
RA 228,17,6144
AR 204,40
Mvaa 160,200,240
M0 69,0
RA 232,17,8192
AR 205,41
Mvaa 164,200,244
s_nop 0
TR 184,14,34632
TR 186,14,34888
Mvaa 168,200,248
s_nop 0
TR 188,14,34688
TR 190,14,34944
Mvaa 172,200,252
DM 21,24,79,0
Mvaa 48,204,236
s_mov_b32 s79,37120
TR 192,14,34696
TR 194,14,34952
Mvaa 64,204,240
M0 69,4096
AR 206,42
AR 207,43
Mvaa 80,204,244
DM 21,24,79,0
Mvaa 96,204,248
s_mov_b32 s79,73984
AR 208,60
AR 209,61
Mvaa 112,204,252
M0 69,8192
AR 210,62
AR 211,63
Mvaa 176,208,236
s_nop 0
TR 196,14,34752
TR 198,14,35008
Mvaa 180,208,240
DM 21,24,79,0
Mvaa 184,208,244
s_mov_b32 s79,110848
v_mul_f32 v204,v204,v28
v_mul_f32 v205,v205,v28
Mvaa 188,208,248
M0 69,12288
v_mul_f32 v206,v206,v28
v_mul_f32 v207,v207,v28
Mvaa 192,208,252
WL 6
DM 21,24,79,0
Mvaa 36,180,216
s_mov_b32 s79,147712
TR 200,14,34760
TR 202,14,35016
Mvaa 52,180,220
M0 69,16384
v_mul_f32 v208,v208,v28
v_mul_f32 v209,v209,v28
Mvaa 68,180,224
DM 21,24,79,0
Mvaa 84,180,228
s_mov_b32 s79,1049088
v_mul_f32 v210,v210,v28
v_mul_f32 v211,v211,v28
Mvaa 100,180,232
M0 77,107040
CV 236,204,205
CV 237,206,207
Mvaa 116,184,216
s_nop 0
TR 204,14,34816
TR 206,14,35072
Mvaa 120,184,220
DM 20,28,79,0
Mvaa 124,184,224
s_mov_b32 s79,1057280
CV 238,208,209
CV 239,210,211
Mvaa 128,184,228
M0 77,108128
AR 196,44
AR 197,45
Mvaa 132,184,232
WL 6
DM 20,28,79,0
Mvaa 40,188,216
s_mov_b32 s79,1065472
TR 208,14,34824
TR 210,14,35080
Mvaa 56,188,220
M0 77,109216
AR 198,46
AR 199,47
Mvaa 72,188,224
DM 20,28,79,0
Mvaa 88,188,228
s_mov_b32 s79,1073664
AR 200,64
AR 201,65
Mvaa 104,188,232
M0 77,110304
AR 202,66
AR 203,67
Mvaa 136,192,216
DM 20,28,79,0
Mvaa 140,192,220
v_mul_f32 v196,v196,v29
v_mul_f32 v197,v197,v29
Mvaa 144,192,224
v_mul_f32 v198,v198,v29
v_mul_f32 v199,v199,v29
Mvaa 148,192,228
v_mul_f32 v200,v200,v29
v_mul_f32 v201,v201,v29
Mvaa 152,192,232
WL 4
v_mul_f32 v202,v202,v29
v_mul_f32 v203,v203,v29
Mvaa 44,196,216
s_waitcnt vmcnt(10) lgkmcnt(0)
s_barrier
Mvaa 60,196,220
s_nop 0
TR 180,14,0
TR 182,14,256
Mvaa 76,196,224
RA 236,18,0
CV 240,196,197
Mvaa 92,196,228
RA 240,18,2048
CV 241,198,199
Mvaa 108,196,232
RA 244,18,4096
CV 242,200,201
Mvaa 156,200,216
s_mov_b32 s79,1311232
RA 248,18,6144
CV 243,202,203
Mvaa 160,200,220
M0 77,124352
RA 252,18,8192
AR 204,48
Mvaa 164,200,224
s_nop 0
TR 184,14,8
TR 186,14,264
Mvaa 168,200,228
s_nop 0
TR 188,14,64
TR 190,14,320
Mvaa 172,200,232
DM 20,28,79,0
Mvaa 48,204,216
s_mov_b32 s79,1319424
TR 192,14,72
TR 194,14,328
Mvaa 64,204,220
M0 77,125440
AR 205,49
AR 206,50
Mvaa 80,204,224
DM 20,28,79,0
Mvaa 96,204,228
s_mov_b32 s79,1327616
AR 207,51
AR 208,68
Mvaa 112,204,232
M0 77,126528
AR 209,69
AR 210,70
Mvaa 176,208,216
s_nop 0
TR 196,14,128
TR 198,14,384
Mvaa 180,208,220
DM 20,28,79,0
Mvaa 184,208,224
s_mov_b32 s79,1335808
AR 211,71
v_mul_f32 v204,v204,v30
Mvaa 188,208,228
M0 77,127616
v_mul_f32 v205,v205,v30
v_mul_f32 v206,v206,v30
Mvaa 192,208,232
WL 6
DM 20,28,79,0
Mvaa 36,180,236
s_nop 0
TR 200,14,136
TR 202,14,392
Mvaa 52,180,240
v_mul_f32 v207,v207,v30
v_mul_f32 v208,v208,v30
Mvaa 68,180,244
v_mul_f32 v209,v209,v30
v_mul_f32 v210,v210,v30
Mvaa 84,180,248
v_mul_f32 v211,v211,v30
CV 244,204,205
Mvaa 100,180,252
CV 245,206,207
CV 246,208,209
Mvaa 116,184,236
s_nop 0
TR 204,14,192
TR 206,14,448
Mvaa 120,184,240
CV 247,210,211
AR 196,52
Mvaa 124,184,244
AR 197,53
AR 198,54
Mvaa 128,184,248
AR 199,55
AR 200,72
Mvaa 132,184,252
WL 6
AR 201,73
AR 202,74
Mvaa 40,188,236
s_nop 0
TR 208,14,200
TR 210,14,456
Mvaa 56,188,240
AR 203,75
v_mul_f32 v196,v196,v31
Mvaa 72,188,244
v_mul_f32 v197,v197,v31
v_mul_f32 v198,v198,v31
Mvaa 88,188,248
v_mul_f32 v199,v199,v31
v_mul_f32 v200,v200,v31
Mvaa 104,188,252
v_mul_f32 v201,v201,v31
v_mul_f32 v202,v202,v31
Mvaa 136,192,236
v_mul_f32 v203,v203,v31
CV 248,196,197
Mvaa 140,192,240
CV 249,198,199
CV 250,200,201
Mvaa 144,192,244
CV 251,202,203
AR 204,56
Mvaa 148,192,248
AR 205,57
AR 206,58
Mvaa 152,192,252
WL 4
AR 207,59
AR 208,76
Mvaa 44,196,236
s_waitcnt vmcnt(4) lgkmcnt(0)
s_barrier
Mvaa 60,196,240
s_nop 0
TR 180,14,17312
TR 182,14,17568
Mvaa 76,196,244
RA 216,15,0
AR 209,77
Mvaa 92,196,248
RA 220,15,2048
AR 210,78
Mvaa 108,196,252
RA 224,15,4096
AR 211,79
Mvaa 156,200,236
s_mov_b32 s79,384
RA 228,15,6144
v_mul_f32 v204,v204,v32
Mvaa 160,200,240
M0 69,141664
RA 232,15,8192
v_mul_f32 v205,v205,v32
Mvaa 164,200,244
s_nop 0
TR 184,14,17320
TR 186,14,17576
Mvaa 168,200,248
s_nop 0
TR 188,14,17376
TR 190,14,17632
Mvaa 172,200,252
DM 21,24,79,0
Mvaa 48,204,236
s_mov_b32 s79,37248
TR 192,14,17384
TR 194,14,17640
Mvaa 64,204,240
M0 69,145760
v_mul_f32 v206,v206,v32
v_mul_f32 v207,v207,v32
Mvaa 80,204,244
DM 21,24,79,0
Mvaa 96,204,248
s_mov_b32 s79,74112
v_mul_f32 v208,v208,v32
v_mul_f32 v209,v209,v32
Mvaa 112,204,252
M0 69,149856
v_mul_f32 v210,v210,v32
v_mul_f32 v211,v211,v32
Mvaa 176,208,236
s_nop 0
TR 196,14,17440
TR 198,14,17696
Mvaa 180,208,240
DM 21,24,79,0
Mvaa 184,208,244
s_mov_b32 s79,110976
CV 252,204,205
CV 253,206,207
Mvaa 188,208,248
M0 69,153952
CV 254,208,209
CV 255,210,211
Mvaa 192,208,252
WL 6
DM 21,24,79,0
Mvaa 36,180,216
s_mov_b32 s79,147840
TR 200,14,17448
TR 202,14,17704
Mvaa 52,180,220
M0 69,158048
Mvaa 68,180,224
DM 21,24,79,0
Mvaa 84,180,228
s_mov_b32 s79,1573376
Mvaa 100,180,232
M0 77,89728
Mvaa 116,184,216
s_nop 0
TR 204,14,17504
TR 206,14,17760
Mvaa 120,184,220
DM 20,28,79,0
Mvaa 124,184,224
s_mov_b32 s79,1581568
Mvaa 128,184,228
M0 77,90816
Mvaa 132,184,232
WL 6
DM 20,28,79,0
Mvaa 40,188,216
s_mov_b32 s79,1589760
TR 208,14,17512
TR 210,14,17768
Mvaa 56,188,220
M0 77,91904
Mvaa 72,188,224
DM 20,28,79,0
Mvaa 88,188,228
s_mov_b32 s79,1597952
Mvaa 104,188,232
M0 77,92992
Mvaa 136,192,216
DM 20,28,79,0
Mvaa 140,192,220
Mvaa 144,192,224
Mvaa 148,192,228
Mvaa 152,192,232
WL 4
Mvaa 44,196,216
s_waitcnt vmcnt(9) lgkmcnt(0)
s_barrier
Mvaa 60,196,220
s_nop 0
TR 180,14,34624
TR 182,14,34880
Mvaa 76,196,224
RA 20,16,0
Mvaa 92,196,228
RA 24,16,2048
Mvaa 108,196,232
RA 28,16,4096
Mvaa 156,200,216
s_mov_b32 s79,1835520
RA 32,16,6144
Mvaa 160,200,220
M0 77,107040
RA 36,16,8192
Mvaa 164,200,224
s_nop 0
TR 184,14,34632
TR 186,14,34888
Mvaa 168,200,228
s_nop 0
TR 188,14,34688
TR 190,14,34944
Mvaa 172,200,232
DM 20,28,79,0
Mvaa 48,204,216
s_mov_b32 s79,1843712
TR 192,14,34696
TR 194,14,34952
Mvaa 64,204,220
M0 77,108128
Mvaa 80,204,224
DM 20,28,79,0
Mvaa 96,204,228
s_mov_b32 s79,1851904
Mvaa 112,204,232
M0 77,109216
Mvaa 176,208,216
s_nop 0
TR 196,14,34752
TR 198,14,35008
Mvaa 180,208,220
DM 20,28,79,0
Mvaa 184,208,224
s_mov_b32 s79,1860096
Mvaa 188,208,228
M0 77,110304
Mvaa 192,208,232
WL 6
DM 20,28,79,0
Mvaa 36,180,20
s_nop 0
TR 200,14,34760
TR 202,14,35016
Mvaa 52,180,24
AR 196,80
AR 197,81
Mvaa 68,180,28
AR 198,82
AR 199,83
Mvaa 84,180,32
AR 200,100
AR 201,101
Mvaa 100,180,36
AR 202,102
AR 203,103
Mvaa 116,184,20
s_nop 0
TR 204,14,34816
TR 206,14,35072
Mvaa 120,184,24
v_mul_f32 v196,v196,v28
v_mul_f32 v197,v197,v28
Mvaa 124,184,28
v_mul_f32 v198,v198,v28
v_mul_f32 v199,v199,v28
Mvaa 128,184,32
v_mul_f32 v200,v200,v28
v_mul_f32 v201,v201,v28
Mvaa 132,184,36
WL 6
v_mul_f32 v202,v202,v28
v_mul_f32 v203,v203,v28
Mvaa 40,188,20
s_nop 0
TR 208,14,34824
TR 210,14,35080
Mvaa 56,188,24
CV 196,196,197
CV 197,198,199
Mvaa 72,188,28
CV 198,200,201
CV 199,202,203
Mvaa 88,188,32
v_accvgpr_write_b32 a216,v196
v_accvgpr_write_b32 a217,v197
Mvaa 104,188,36
v_accvgpr_write_b32 a218,v198
v_accvgpr_write_b32 a219,v199
Mvaa 136,192,20
AR 204,84
AR 205,85
Mvaa 140,192,24
AR 206,86
AR 207,87
Mvaa 144,192,28
AR 208,104
AR 209,105
Mvaa 148,192,32
AR 210,106
AR 211,107
Mvaa 152,192,36
WL 4
v_mul_f32 v204,v204,v29
v_mul_f32 v205,v205,v29
Mvaa 44,196,20
s_waitcnt vmcnt(4) lgkmcnt(0)
s_barrier
Mvaa 60,196,24
s_nop 0
TR 180,14,0
TR 182,14,256
Mvaa 76,196,28
RA 0,17,0
v_mul_f32 v206,v206,v29
Mvaa 92,196,32
RA 4,17,2048
v_mul_f32 v207,v207,v29
Mvaa 108,196,36
RA 8,17,4096
v_mul_f32 v208,v208,v29
Mvaa 156,200,20
s_mov_b32 s79,512
RA 12,17,6144
v_mul_f32 v209,v209,v29
Mvaa 160,200,24
M0 69,0
RA 16,17,8192
v_mul_f32 v210,v210,v29
Mvaa 164,200,28
s_nop 0
TR 184,14,8
TR 186,14,264
Mvaa 168,200,32
s_nop 0
TR 188,14,64
TR 190,14,320
Mvaa 172,200,36
DM 21,24,79,0
Mvaa 48,204,20
s_mov_b32 s79,37376
TR 192,14,72
TR 194,14,328
Mvaa 64,204,24
M0 69,4096
v_mul_f32 v211,v211,v29
CV 204,204,205
Mvaa 80,204,28
DM 21,24,79,0
Mvaa 96,204,32
s_mov_b32 s79,74240
CV 205,206,207
CV 206,208,209
Mvaa 112,204,36
M0 69,8192
CV 207,210,211
v_accvgpr_write_b32 a220,v204
Mvaa 176,208,20
s_nop 0
TR 196,14,128
TR 198,14,384
Mvaa 180,208,24
DM 21,24,79,0
Mvaa 184,208,28
s_mov_b32 s79,111104
v_accvgpr_write_b32 a221,v205
v_accvgpr_write_b32 a222,v206
Mvaa 188,208,32
M0 69,12288
v_accvgpr_write_b32 a223,v207
AR 196,88
Mvaa 192,208,36
WL 6
DM 21,24,79,0
Mvaa 36,180,0
s_mov_b32 s79,147968
TR 200,14,136
TR 202,14,392
Mvaa 52,180,4
M0 69,16384
AR 197,89
AR 198,90
Mvaa 68,180,8
DM 21,24,79,0
Mvaa 84,180,12
s_mov_b32 s79,2097664
AR 199,91
AR 200,108
Mvaa 100,180,16
M0 77,124352
AR 201,109
AR 202,110
Mvaa 116,184,0
s_nop 0
TR 204,14,192
TR 206,14,448
Mvaa 120,184,4
DM 20,28,79,0
Mvaa 124,184,8
s_mov_b32 s79,2105856
AR 203,111
v_mul_f32 v196,v196,v30
Mvaa 128,184,12
M0 77,125440
v_mul_f32 v197,v197,v30
v_mul_f32 v198,v198,v30
Mvaa 132,184,16
WL 6
DM 20,28,79,0
Mvaa 40,188,0
s_mov_b32 s79,2114048
TR 208,14,200
TR 210,14,456
Mvaa 56,188,4
M0 77,126528
v_mul_f32 v199,v199,v30
v_mul_f32 v200,v200,v30
Mvaa 72,188,8
DM 20,28,79,0
Mvaa 88,188,12
s_mov_b32 s79,2122240
v_mul_f32 v201,v201,v30
v_mul_f32 v202,v202,v30
Mvaa 104,188,16
M0 77,127616
v_mul_f32 v203,v203,v30
CV 196,196,197
Mvaa 136,192,0
DM 20,28,79,0
Mvaa 140,192,4
CV 197,198,199
CV 198,200,201
Mvaa 144,192,8
CV 199,202,203
v_accvgpr_write_b32 a224,v196
Mvaa 148,192,12
v_accvgpr_write_b32 a225,v197
v_accvgpr_write_b32 a226,v198
Mvaa 152,192,16
WL 4
v_accvgpr_write_b32 a227,v199
AR 204,92
Mvaa 44,196,0
s_waitcnt vmcnt(9) lgkmcnt(0)
s_barrier
Mvaa 60,196,4
s_nop 0
TR 180,14,17312
TR 182,14,17568
Mvaa 76,196,8
RA 20,18,0
AR 205,93
Mvaa 92,196,12
RA 24,18,2048
AR 206,94
Mvaa 108,196,16
RA 28,18,4096
AR 207,95
Mvaa 156,200,0
s_mov_b32 s79,2359808
RA 32,18,6144
AR 208,112
Mvaa 160,200,4
M0 77,89728
RA 36,18,8192
AR 209,113
Mvaa 164,200,8
s_nop 0
TR 184,14,17320
TR 186,14,17576
Mvaa 168,200,12
s_nop 0
TR 188,14,17376
TR 190,14,17632
Mvaa 172,200,16
DM 20,28,79,0
Mvaa 48,204,0
s_mov_b32 s79,2368000
TR 192,14,17384
TR 194,14,17640
Mvaa 64,204,4
M0 77,90816
AR 210,114
AR 211,115
Mvaa 80,204,8
DM 20,28,79,0
Mvaa 96,204,12
s_mov_b32 s79,2376192
v_mul_f32 v204,v204,v31
v_mul_f32 v205,v205,v31
Mvaa 112,204,16
M0 77,91904
v_mul_f32 v206,v206,v31
v_mul_f32 v207,v207,v31
Mvaa 176,208,0
s_nop 0
TR 196,14,17440
TR 198,14,17696
Mvaa 180,208,4
DM 20,28,79,0
Mvaa 184,208,8
s_mov_b32 s79,2384384
v_mul_f32 v208,v208,v31
v_mul_f32 v209,v209,v31
Mvaa 188,208,12
M0 77,92992
v_mul_f32 v210,v210,v31
v_mul_f32 v211,v211,v31
Mvaa 192,208,16
WL 6
DM 20,28,79,0
Mvaa 36,180,20
s_nop 0
TR 200,14,17448
TR 202,14,17704
Mvaa 52,180,24
CV 204,204,205
CV 205,206,207
Mvaa 68,180,28
CV 206,208,209
CV 207,210,211
Mvaa 84,180,32
v_accvgpr_write_b32 a228,v204
v_accvgpr_write_b32 a229,v205
Mvaa 100,180,36
v_accvgpr_write_b32 a230,v206
v_accvgpr_write_b32 a231,v207
Mvaa 116,184,20
s_nop 0
TR 204,14,17504
TR 206,14,17760
Mvaa 120,184,24
AR 196,96
AR 197,97
Mvaa 124,184,28
AR 198,98
AR 199,99
Mvaa 128,184,32
AR 200,116
AR 201,117
Mvaa 132,184,36
WL 6
AR 202,118
AR 203,119
Mvaa 40,188,20
s_nop 0
TR 208,14,17512
TR 210,14,17768
Mvaa 56,188,24
v_mul_f32 v196,v196,v32
v_mul_f32 v197,v197,v32
Mvaa 72,188,28
v_mul_f32 v198,v198,v32
v_mul_f32 v199,v199,v32
Mvaa 88,188,32
v_mul_f32 v200,v200,v32
v_mul_f32 v201,v201,v32
Mvaa 104,188,36
v_mul_f32 v202,v202,v32
v_mul_f32 v203,v203,v32
Mvaa 136,192,20
CV 196,196,197
CV 197,198,199
Mvaa 140,192,24
CV 198,200,201
CV 199,202,203
Mvaa 144,192,28
v_accvgpr_write_b32 a232,v196
v_accvgpr_write_b32 a233,v197
Mvaa 148,192,32
v_accvgpr_write_b32 a234,v198
v_accvgpr_write_b32 a235,v199
Mvaa 152,192,36
WL 4
AR 204,120
AR 205,121
Mvaa 44,196,20
s_waitcnt vmcnt(4) lgkmcnt(0)
s_barrier
Mvaa 60,196,24
s_nop 0
TR 180,14,34624
TR 182,14,34880
Mvaa 76,196,28
RA 0,15,0
AR 206,122
Mvaa 92,196,32
RA 4,15,2048
AR 207,123
Mvaa 108,196,36
RA 8,15,4096
AR 208,140
Mvaa 156,200,20
s_mov_b32 s79,640
RA 12,15,6144
AR 209,141
Mvaa 160,200,24
M0 69,141664
RA 16,15,8192
AR 210,142
Mvaa 164,200,28
s_nop 0
TR 184,14,34632
TR 186,14,34888
Mvaa 168,200,32
s_nop 0
TR 188,14,34688
TR 190,14,34944
Mvaa 172,200,36
DM 21,24,79,0
Mvaa 48,204,20
s_mov_b32 s79,37504
TR 192,14,34696
TR 194,14,34952
Mvaa 64,204,24
M0 69,145760
AR 211,143
v_mul_f32 v204,v204,v28
Mvaa 80,204,28
DM 21,24,79,0
Mvaa 96,204,32
s_mov_b32 s79,74368
v_mul_f32 v205,v205,v28
v_mul_f32 v206,v206,v28
Mvaa 112,204,36
M0 69,149856
v_mul_f32 v207,v207,v28
v_mul_f32 v208,v208,v28
Mvaa 176,208,20
s_nop 0
TR 196,14,34752
TR 198,14,35008
Mvaa 180,208,24
DM 21,24,79,0
Mvaa 184,208,28
s_mov_b32 s79,111232
v_mul_f32 v209,v209,v28
v_mul_f32 v210,v210,v28
Mvaa 188,208,32
M0 69,153952
v_mul_f32 v211,v211,v28
CV 204,204,205
Mvaa 192,208,36
WL 6
DM 21,24,79,0
Mvaa 36,180,0
s_mov_b32 s79,148096
TR 200,14,34760
TR 202,14,35016
Mvaa 52,180,4
M0 69,158048
CV 205,206,207
CV 206,208,209
Mvaa 68,180,8
DM 21,24,79,0
Mvaa 84,180,12
s_mov_b32 s79,2621952
CV 207,210,211
v_accvgpr_write_b32 a236,v204
Mvaa 100,180,16
M0 77,107040
v_accvgpr_write_b32 a237,v205
v_accvgpr_write_b32 a238,v206
Mvaa 116,184,0
s_nop 0
TR 204,14,34816
TR 206,14,35072
Mvaa 120,184,4
DM 20,28,79,0
Mvaa 124,184,8
s_mov_b32 s79,2630144
v_accvgpr_write_b32 a239,v207
AR 196,124
Mvaa 128,184,12
M0 77,108128
AR 197,125
AR 198,126
Mvaa 132,184,16
WL 6
DM 20,28,79,0
Mvaa 40,188,0
s_mov_b32 s79,2638336
TR 208,14,34824
TR 210,14,35080
Mvaa 56,188,4
M0 77,109216
AR 199,127
AR 200,144
Mvaa 72,188,8
DM 20,28,79,0
Mvaa 88,188,12
s_mov_b32 s79,2646528
AR 201,145
AR 202,146
Mvaa 104,188,16
M0 77,110304
AR 203,147
v_mul_f32 v196,v196,v29
Mvaa 136,192,0
DM 20,28,79,0
Mvaa 140,192,4
v_mul_f32 v197,v197,v29
v_mul_f32 v198,v198,v29
Mvaa 144,192,8
v_mul_f32 v199,v199,v29
v_mul_f32 v200,v200,v29
Mvaa 148,192,12
v_mul_f32 v201,v201,v29
v_mul_f32 v202,v202,v29
Mvaa 152,192,16
WL 4
v_mul_f32 v203,v203,v29
CV 196,196,197
Mvaa 44,196,0
s_waitcnt vmcnt(9) lgkmcnt(0)
s_barrier
Mvaa 60,196,4
s_nop 0
TR 180,14,0
TR 182,14,256
Mvaa 76,196,8
RA 20,16,0
CV 197,198,199
Mvaa 92,196,12
RA 24,16,2048
CV 198,200,201
Mvaa 108,196,16
RA 28,16,4096
CV 199,202,203
Mvaa 156,200,0
s_mov_b32 s79,2884096
RA 32,16,6144
v_accvgpr_write_b32 a240,v196
Mvaa 160,200,4
M0 77,124352
RA 36,16,8192
v_accvgpr_write_b32 a241,v197
Mvaa 164,200,8
s_nop 0
TR 184,14,8
TR 186,14,264
Mvaa 168,200,12
s_nop 0
TR 188,14,64
TR 190,14,320
Mvaa 172,200,16
DM 20,28,79,0
Mvaa 48,204,0
s_mov_b32 s79,2892288
TR 192,14,72
TR 194,14,328
Mvaa 64,204,4
M0 77,125440
v_accvgpr_write_b32 a242,v198
v_accvgpr_write_b32 a243,v199
Mvaa 80,204,8
DM 20,28,79,0
Mvaa 96,204,12
s_mov_b32 s79,2900480
AR 204,128
AR 205,129
Mvaa 112,204,16
M0 77,126528
AR 206,130
AR 207,131
Mvaa 176,208,0
s_nop 0
TR 196,14,128
TR 198,14,384
Mvaa 180,208,4
DM 20,28,79,0
Mvaa 184,208,8
s_mov_b32 s79,2908672
AR 208,148
AR 209,149
Mvaa 188,208,12
M0 77,127616
AR 210,150
AR 211,151
Mvaa 192,208,16
WL 6
DM 20,28,79,0
Mvaa 36,180,20
s_nop 0
TR 200,14,136
TR 202,14,392
Mvaa 52,180,24
s_add_u32 s98,s71,73728
v_lshl_add_u32 v8,v2,1,s98
v_mul_f32 v204,v204,v30
Mvaa 68,180,28
buffer_load_dword v8,v8,s[60:63],0 offen
Mvaa 84,180,32
v_mul_f32 v205,v205,v30
v_mul_f32 v206,v206,v30
Mvaa 100,180,36
v_mul_f32 v207,v207,v30
v_mul_f32 v208,v208,v30
Mvaa 116,184,20
s_nop 0
TR 204,14,192
TR 206,14,448
Mvaa 120,184,24
v_mul_f32 v209,v209,v30
v_mul_f32 v210,v210,v30
Mvaa 124,184,28
v_mul_f32 v211,v211,v30
CV 204,204,205
Mvaa 128,184,32
CV 205,206,207
CV 206,208,209
Mvaa 132,184,36
WL 6
CV 207,210,211
v_accvgpr_write_b32 a244,v204
Mvaa 40,188,20
s_nop 0
TR 208,14,200
TR 210,14,456
Mvaa 56,188,24
v_accvgpr_write_b32 a245,v205
v_accvgpr_write_b32 a246,v206
Mvaa 72,188,28
v_accvgpr_write_b32 a247,v207
AR 196,132
Mvaa 88,188,32
AR 197,133
AR 198,134
Mvaa 104,188,36
AR 199,135
AR 200,152
Mvaa 136,192,20
AR 201,153
AR 202,154
Mvaa 140,192,24
AR 203,155
v_mul_f32 v196,v196,v31
Mvaa 144,192,28
v_mul_f32 v197,v197,v31
v_mul_f32 v198,v198,v31
Mvaa 148,192,32
v_mul_f32 v199,v199,v31
v_mul_f32 v200,v200,v31
Mvaa 152,192,36
WL 4
v_mul_f32 v201,v201,v31
v_mul_f32 v202,v202,v31
Mvaa 44,196,20
s_waitcnt vmcnt(5) lgkmcnt(0)
s_barrier
Mvaa 60,196,24
s_nop 0
TR 180,14,17312
TR 182,14,17568
Mvaa 76,196,28
RA 0,17,0
v_mul_f32 v203,v203,v31
Mvaa 92,196,32
RA 4,17,2048
CV 196,196,197
Mvaa 108,196,36
RA 8,17,4096
CV 197,198,199
Mvaa 156,200,20
s_mov_b32 s79,768
RA 12,17,6144
CV 198,200,201
Mvaa 160,200,24
M0 69,0
RA 16,17,8192
CV 199,202,203
Mvaa 164,200,28
s_nop 0
TR 184,14,17320
TR 186,14,17576
Mvaa 168,200,32
s_nop 0
TR 188,14,17376
TR 190,14,17632
Mvaa 172,200,36
DM 21,24,79,0
Mvaa 48,204,20
s_mov_b32 s79,37632
TR 192,14,17384
TR 194,14,17640
Mvaa 64,204,24
M0 69,4096
v_accvgpr_write_b32 a248,v196
v_accvgpr_write_b32 a249,v197
Mvaa 80,204,28
DM 21,24,79,0
Mvaa 96,204,32
s_mov_b32 s79,74496
v_accvgpr_write_b32 a250,v198
v_accvgpr_write_b32 a251,v199
Mvaa 112,204,36
M0 69,8192
AR 204,136
AR 205,137
Mvaa 176,208,20
s_nop 0
TR 196,14,17440
TR 198,14,17696
Mvaa 180,208,24
DM 21,24,79,0
Mvaa 184,208,28
s_mov_b32 s79,111360
AR 206,138
AR 207,139
Mvaa 188,208,32
M0 69,12288
AR 208,156
AR 209,157
Mvaa 192,208,36
WL 6
DM 21,24,79,0
Mvaa 36,180,0
s_mov_b32 s79,148224
TR 200,14,17448
TR 202,14,17704
Mvaa 52,180,4
M0 69,16384
AR 210,158
AR 211,159
Mvaa 68,180,8
DM 21,24,79,0
Mvaa 84,180,12
s_mov_b32 s79,3146240
v_mul_f32 v204,v204,v32
v_mul_f32 v205,v205,v32
Mvaa 100,180,16
M0 77,89728
v_mul_f32 v206,v206,v32
v_mul_f32 v207,v207,v32
Mvaa 116,184,0
s_nop 0
TR 204,14,17504
TR 206,14,17760
Mvaa 120,184,4
DM 20,28,79,0
Mvaa 124,184,8
s_mov_b32 s79,3154432
v_mul_f32 v208,v208,v32
v_mul_f32 v209,v209,v32
Mvaa 128,184,12
M0 77,90816
v_mul_f32 v210,v210,v32
v_mul_f32 v211,v211,v32
Mvaa 132,184,16
WL 6
DM 20,28,79,0
Mvaa 40,188,0
s_mov_b32 s79,3162624
TR 208,14,17512
TR 210,14,17768
Mvaa 56,188,4
M0 77,91904
CV 204,204,205
CV 205,206,207
Mvaa 72,188,8
DM 20,28,79,0
Mvaa 88,188,12
s_mov_b32 s79,3170816
CV 206,208,209
CV 207,210,211
Mvaa 104,188,16
M0 77,92992
v_accvgpr_write_b32 a252,v204
v_accvgpr_write_b32 a253,v205
Mvaa 136,192,0
DM 20,28,79,0
Mvaa 140,192,4
v_accvgpr_write_b32 a254,v206
v_accvgpr_write_b32 a255,v207
Mvaa 144,192,8
Mvaa 148,192,12
Mvaa 152,192,16
WL 4
Mvaa 44,196,0
s_waitcnt vmcnt(10) lgkmcnt(0)
s_barrier
Mvaa 60,196,4
s_nop 0
TR 180,14,34624
TR 182,14,34880
Mvaa 76,196,8
RA 20,18,0
Mvaa 92,196,12
RA 24,18,2048
Mvaa 108,196,16
RA 28,18,4096
Mvaa 156,200,0
s_mov_b32 s79,3408384
RA 32,18,6144
Mvaa 160,200,4
M0 77,107040
RA 36,18,8192
Mvaa 164,200,8
s_nop 0
TR 184,14,34632
TR 186,14,34888
Mvaa 168,200,12
s_nop 0
TR 188,14,34688
TR 190,14,34944
Mvaa 172,200,16
DM 20,28,79,0
Mvaa 48,204,0
s_mov_b32 s79,3416576
TR 192,14,34696
TR 194,14,34952
Mvaa 64,204,4
M0 77,108128
Mvaa 80,204,8
DM 20,28,79,0
Mvaa 96,204,12
s_mov_b32 s79,3424768
Mvaa 112,204,16
M0 77,109216
Mvaa 176,208,0
s_nop 0
TR 196,14,34752
TR 198,14,35008
Mvaa 180,208,4
DM 20,28,79,0
Mvaa 184,208,8
s_mov_b32 s79,3432960
Mvaa 188,208,12
M0 77,110304
Mvaa 192,208,16
WL 6
DM 20,28,79,0
Mvaa 36,180,20
s_nop 0
TR 200,14,34760
TR 202,14,35016
Mvaa 52,180,24
Mvaa 68,180,28
Mvaa 84,180,32
Mvaa 100,180,36
Mvaa 116,184,20
s_nop 0
TR 204,14,34816
TR 206,14,35072
Mvaa 120,184,24
Mvaa 124,184,28
Mvaa 128,184,32
Mvaa 132,184,36
WL 6
Mvaa 40,188,20
s_nop 0
TR 208,14,34824
TR 210,14,35080
Mvaa 56,188,24
Mvaa 72,188,28
Mvaa 88,188,32
Mvaa 104,188,36
Mvaa 136,192,20
Mvaa 140,192,24
Mvaa 144,192,28
Mvaa 148,192,32
Mvaa 152,192,36
WL 4
Mvaa 44,196,20
s_waitcnt vmcnt(4) lgkmcnt(0)
s_barrier
Mvaa 60,196,24
s_nop 0
TR 180,14,0
TR 182,14,256
Mvaa 76,196,28
RA 0,15,0
Mvaa 92,196,32
RA 4,15,2048
Mvaa 108,196,36
RA 8,15,4096
Mvaa 156,200,20
s_mov_b32 s79,896
RA 12,15,6144
Mvaa 160,200,24
M0 69,141664
RA 16,15,8192
Mvaa 164,200,28
s_nop 0
TR 184,14,8
TR 186,14,264
Mvaa 168,200,32
s_nop 0
TR 188,14,64
TR 190,14,320
Mvaa 172,200,36
DM 21,24,79,0
Mvaa 48,204,20
s_mov_b32 s79,37760
TR 192,14,72
TR 194,14,328
Mvaa 64,204,24
M0 69,145760
Mvaa 80,204,28
DM 21,24,79,0
Mvaa 96,204,32
s_mov_b32 s79,74624
Mvaa 112,204,36
M0 69,149856
Mvaa 176,208,20
s_nop 0
TR 196,14,128
TR 198,14,384
Mvaa 180,208,24
DM 21,24,79,0
Mvaa 184,208,28
s_mov_b32 s79,111488
Mvaa 188,208,32
M0 69,153952
Mvaa 192,208,36
WL 6
DM 21,24,79,0
Mvaa 36,180,0
s_mov_b32 s79,148352
TR 200,14,136
TR 202,14,392
Mvaa 52,180,4
M0 69,158048
Mvaa 68,180,8
DM 21,24,79,0
Mvaa 84,180,12
s_mov_b32 s79,3670528
Mvaa 100,180,16
M0 77,124352
Mvaa 116,184,0
s_nop 0
TR 204,14,192
TR 206,14,448
Mvaa 120,184,4
DM 20,28,79,0
Mvaa 124,184,8
s_mov_b32 s79,3678720
Mvaa 128,184,12
M0 77,125440
Mvaa 132,184,16
WL 6
DM 20,28,79,0
Mvaa 40,188,0
s_mov_b32 s79,3686912
TR 208,14,200
TR 210,14,456
Mvaa 56,188,4
M0 77,126528
Mvaa 72,188,8
DM 20,28,79,0
Mvaa 88,188,12
s_mov_b32 s79,3695104
Mvaa 104,188,16
M0 77,127616
Mvaa 136,192,0
DM 20,28,79,0
Mvaa 140,192,4
Mvaa 144,192,8
Mvaa 148,192,12
Mvaa 152,192,16
WL 4
Mvaa 44,196,0
s_waitcnt vmcnt(9) lgkmcnt(0)
s_barrier
Mvaa 60,196,4
s_nop 0
TR 180,14,17312
TR 182,14,17568
Mvaa 76,196,8
RA 20,16,0
Mvaa 92,196,12
RA 24,16,2048
Mvaa 108,196,16
RA 28,16,4096
Mvaa 156,200,0
s_mov_b32 s79,3932672
RA 32,16,6144
Mvaa 160,200,4
M0 77,89728
RA 36,16,8192
Mvaa 164,200,8
s_nop 0
TR 184,14,17320
TR 186,14,17576
Mvaa 168,200,12
s_nop 0
TR 188,14,17376
TR 190,14,17632
Mvaa 172,200,16
DM 20,28,79,0
Mvaa 48,204,0
s_mov_b32 s79,3940864
TR 192,14,17384
TR 194,14,17640
Mvaa 64,204,4
M0 77,90816
Mvaa 80,204,8
DM 20,28,79,0
Mvaa 96,204,12
s_mov_b32 s79,3949056
Mvaa 112,204,16
M0 77,91904
Mvaa 176,208,0
s_nop 0
TR 196,14,17440
TR 198,14,17696
Mvaa 180,208,4
DM 20,28,79,0
Mvaa 184,208,8
s_mov_b32 s79,3957248
Mvaa 188,208,12
M0 77,92992
Mvaa 192,208,16
WL 6
DM 20,28,79,0
Mvaa 36,180,20
s_nop 0
TR 200,14,17448
TR 202,14,17704
Mvaa 52,180,24
Mvaa 68,180,28
Mvaa 84,180,32
Mvaa 100,180,36
Mvaa 116,184,20
s_nop 0
TR 204,14,17504
TR 206,14,17760
Mvaa 120,184,24
Mvaa 124,184,28
Mvaa 128,184,32
Mvaa 132,184,36
WL 6
Mvaa 40,188,20
s_nop 0
TR 208,14,17512
TR 210,14,17768
Mvaa 56,188,24
Mvaa 72,188,28
Mvaa 88,188,32
Mvaa 104,188,36
Mvaa 136,192,20
Mvaa 140,192,24
Mvaa 144,192,28
Mvaa 148,192,32
Mvaa 152,192,36
WL 4
Mvaa 44,196,20
s_waitcnt vmcnt(4) lgkmcnt(0)
s_barrier
Mvaa 60,196,24
s_nop 0
TR 180,14,34624
TR 182,14,34880
Mvaa 76,196,28
RA 0,17,0
Mvaa 92,196,32
RA 4,17,2048
Mvaa 108,196,36
RA 8,17,4096
Mvaa 156,200,20
s_mov_b32 s79,1024
RA 12,17,6144
Mvaa 160,200,24
M0 69,0
RA 16,17,8192
Mvaa 164,200,28
s_nop 0
TR 184,14,34632
TR 186,14,34888
Mvaa 168,200,32
s_nop 0
TR 188,14,34688
TR 190,14,34944
Mvaa 172,200,36
DM 21,24,79,0
Mvaa 48,204,20
s_mov_b32 s79,37888
TR 192,14,34696
TR 194,14,34952
Mvaa 64,204,24
M0 69,4096
Mvaa 80,204,28
DM 21,24,79,0
Mvaa 96,204,32
s_mov_b32 s79,74752
Mvaa 112,204,36
M0 69,8192
Mvaa 176,208,20
s_nop 0
TR 196,14,34752
TR 198,14,35008
Mvaa 180,208,24
DM 21,24,79,0
Mvaa 184,208,28
s_mov_b32 s79,111616
Mvaa 188,208,32
M0 69,12288
Mvaa 192,208,36
WL 6
DM 21,24,79,0
Mvaa 36,180,0
s_mov_b32 s79,148480
TR 200,14,34760
TR 202,14,35016
Mvaa 52,180,4
M0 69,16384
Mvaa 68,180,8
DM 21,24,79,0
Mvaa 84,180,12
s_mov_b32 s79,4194816
Mvaa 100,180,16
M0 77,107040
Mvaa 116,184,0
s_nop 0
TR 204,14,34816
TR 206,14,35072
Mvaa 120,184,4
DM 20,28,79,0
Mvaa 124,184,8
s_mov_b32 s79,4203008
Mvaa 128,184,12
M0 77,108128
Mvaa 132,184,16
WL 6
DM 20,28,79,0
Mvaa 40,188,0
s_mov_b32 s79,4211200
TR 208,14,34824
TR 210,14,35080
Mvaa 56,188,4
M0 77,109216
Mvaa 72,188,8
DM 20,28,79,0
Mvaa 88,188,12
s_mov_b32 s79,4219392
Mvaa 104,188,16
M0 77,110304
Mvaa 136,192,0
DM 20,28,79,0
Mvaa 140,192,4
Mvaa 144,192,8
Mvaa 148,192,12
Mvaa 152,192,16
WL 4
Mvaa 44,196,0
s_waitcnt vmcnt(9) lgkmcnt(0)
s_barrier
Mvaa 60,196,4
s_nop 0
TR 180,14,0
TR 182,14,256
Mvaa 76,196,8
RA 20,18,0
Mvaa 92,196,12
RA 24,18,2048
Mvaa 108,196,16
RA 28,18,4096
Mvaa 156,200,0
s_mov_b32 s79,4456960
RA 32,18,6144
Mvaa 160,200,4
M0 77,124352
RA 36,18,8192
Mvaa 164,200,8
s_nop 0
TR 184,14,8
TR 186,14,264
Mvaa 168,200,12
s_nop 0
TR 188,14,64
TR 190,14,320
Mvaa 172,200,16
DM 20,28,79,0
Mvaa 48,204,0
s_mov_b32 s79,4465152
TR 192,14,72
TR 194,14,328
Mvaa 64,204,4
M0 77,125440
Mvaa 80,204,8
DM 20,28,79,0
Mvaa 96,204,12
s_mov_b32 s79,4473344
Mvaa 112,204,16
M0 77,126528
Mvaa 176,208,0
s_nop 0
TR 196,14,128
TR 198,14,384
Mvaa 180,208,4
DM 20,28,79,0
Mvaa 184,208,8
s_mov_b32 s79,4481536
Mvaa 188,208,12
M0 77,127616
Mvaa 192,208,16
WL 6
DM 20,28,79,0
Mvaa 36,180,20
s_nop 0
TR 200,14,136
TR 202,14,392
Mvaa 52,180,24
Mvaa 68,180,28
Mvaa 84,180,32
Mvaa 100,180,36
Mvaa 116,184,20
s_nop 0
TR 204,14,192
TR 206,14,448
Mvaa 120,184,24
Mvaa 124,184,28
Mvaa 128,184,32
Mvaa 132,184,36
WL 6
Mvaa 40,188,20
s_nop 0
TR 208,14,200
TR 210,14,456
Mvaa 56,188,24
Mvaa 72,188,28
Mvaa 88,188,32
Mvaa 104,188,36
Mvaa 136,192,20
Mvaa 140,192,24
Mvaa 144,192,28
Mvaa 148,192,32
Mvaa 152,192,36
WL 4
Mvaa 44,196,20
s_waitcnt vmcnt(4) lgkmcnt(0)
s_barrier
Mvaa 60,196,24
s_nop 0
TR 180,14,17312
TR 182,14,17568
Mvaa 76,196,28
RA 0,15,0
Mvaa 92,196,32
RA 4,15,2048
Mvaa 108,196,36
RA 8,15,4096
Mvaa 156,200,20
RA 12,15,6144
Mvaa 160,200,24
M0 78,89728
RA 16,15,8192
Mvaa 164,200,28
s_nop 0
TR 184,14,17320
TR 186,14,17576
Mvaa 168,200,32
s_nop 0
TR 188,14,17376
TR 190,14,17632
Mvaa 172,200,36
DN 19,36,6,0
Mvaa 48,204,20
M0 78,93952
TR 192,14,17384
TR 194,14,17640
Mvaa 64,204,24
Mvaa 80,204,28
DN 33,36,6,0
Mvaa 96,204,32
M0 78,98176
Mvaa 112,204,36
Mvaa 176,208,20
s_nop 0
TR 196,14,17440
TR 198,14,17696
Mvaa 180,208,24
DN 19,36,7,0
Mvaa 184,208,28
M0 78,102400
Mvaa 188,208,32
Mvaa 192,208,36
WL 6
DN 33,36,7,0
Mvaa 36,180,0
s_nop 0
TR 200,14,17448
TR 202,14,17704
Mvaa 52,180,4
Mvaa 68,180,8
Mvaa 84,180,12
Mvaa 100,180,16
Mvaa 116,184,0
s_nop 0
TR 204,14,17504
TR 206,14,17760
Mvaa 120,184,4
Mvaa 124,184,8
Mvaa 128,184,12
Mvaa 132,184,16
WL 6
Mvaa 40,188,0
s_nop 0
TR 208,14,17512
TR 210,14,17768
Mvaa 56,188,4
Mvaa 72,188,8
Mvaa 88,188,12
Mvaa 104,188,16
Mvaa 136,192,0
Mvaa 140,192,4
Mvaa 144,192,8
Mvaa 148,192,12
Mvaa 152,192,16
WL 4
Mvaa 44,196,0
s_waitcnt vmcnt(4) lgkmcnt(0)
s_barrier
Mvaa 60,196,4
s_nop 0
TR 180,14,34624
TR 182,14,34880
Mvaa 76,196,8
s_add_u32 s6,s80,24576
RA 20,16,0
Mvaa 92,196,12
s_and_b32 s6,s6,0x1e000
RA 24,16,2048
Mvaa 108,196,16
s_or_b32 s6,s6,0x40000
RA 28,16,4096
Mvaa 156,200,0
s_xor_b32 s7,s6,0x30000
RA 32,16,6144
Mvaa 160,200,4
M0 78,107040
RA 36,16,8192
Mvaa 164,200,8
s_nop 0
TR 184,14,34632
TR 186,14,34888
Mvaa 168,200,12
s_nop 0
TR 188,14,34688
TR 190,14,34944
Mvaa 172,200,16
DN 19,32,6,0
Mvaa 48,204,0
M0 78,111264
TR 192,14,34696
TR 194,14,34952
Mvaa 64,204,4
Mvaa 80,204,8
DN 33,32,6,0
Mvaa 96,204,12
M0 78,115488
Mvaa 112,204,16
Mvaa 176,208,0
s_nop 0
TR 196,14,34752
TR 198,14,35008
Mvaa 180,208,4
DN 19,32,7,0
Mvaa 184,208,8
M0 78,119712
Mvaa 188,208,12
Mvaa 192,208,16
WL 6
DN 33,32,7,0
Mvaa 36,180,20
s_nop 0
TR 200,14,34760
TR 202,14,35016
Mvaa 52,180,24
Mvaa 68,180,28
Mvaa 84,180,32
Mvaa 100,180,36
Mvaa 116,184,20
s_nop 0
TR 204,14,34816
TR 206,14,35072
Mvaa 120,184,24
Mvaa 124,184,28
Mvaa 128,184,32
v_mul_f32 v36,s84,v36
v_mul_f32 v37,s84,v37
Mvaa 132,184,36
WL 6
v_mul_f32 v38,s84,v38
v_mul_f32 v39,s84,v39
Mvaa 40,188,20
s_nop 0
TR 208,14,34824
TR 210,14,35080
Mvaa 56,188,24
CV 36,36,37
CV 37,38,39
Mvaa 72,188,28
v_mul_f32 v116,s84,v116
v_mul_f32 v117,s84,v117
Mvaa 88,188,32
v_mul_f32 v118,s84,v118
v_mul_f32 v119,s84,v119
Mvaa 104,188,36
CV 38,116,117
CV 39,118,119
Mvaa 136,192,20
v_mul_f32 v52,s84,v52
v_mul_f32 v53,s84,v53
Mvaa 140,192,24
v_mul_f32 v54,s84,v54
v_mul_f32 v55,s84,v55
Mvaa 144,192,28
CV 52,52,53
CV 53,54,55
Mvaa 148,192,32
v_mul_f32 v120,s84,v120
v_mul_f32 v121,s84,v121
Mvaa 152,192,36
WL 4
v_mul_f32 v122,s84,v122
v_mul_f32 v123,s84,v123
v_mul_f32 v56,s84,v56
Mvaa 44,196,20
WL 0
s_barrier
v_mul_f32 v57,s84,v57
Mvaa 60,196,24
M0 78,124352
RA 180,10,0
CV 54,120,121
v_mul_f32 v58,s84,v58
Mvaa 76,196,28
s_add_u32 s98,s71,81920
RA 184,10,64
v_lshl_add_u32 v8,v2,1,s98
v_mul_f32 v59,s84,v59
Mvaa 92,196,32
DN 19,36,4,0
CV 56,56,57
Mvaa 108,196,36
M0 78,128576
CV 55,122,123
v_mul_f32 v68,s84,v68
CV 57,58,59
Mvaa 156,200,20
RA 188,10,128
v_mul_f32 v69,s84,v69
v_mul_f32 v140,s84,v140
Mvaa 160,200,24
DN 33,36,4,0
v_mul_f32 v141,s84,v141
Mvaa 164,200,28
M0 78,132800
v_mul_f32 v70,s84,v70
v_mul_f32 v71,s84,v71
v_mul_f32 v142,s84,v142
Mvaa 168,200,32
CV 68,68,69
CV 69,70,71
v_mul_f32 v143,s84,v143
Mvaa 172,200,36
DN 19,36,5,0
CV 58,140,141
Mvaa 48,204,20
M0 78,137024
RA 192,10,192
v_mul_f32 v124,s84,v124
CV 59,142,143
Mvaa 64,204,24
v_mul_f32 v125,s84,v125
v_mul_f32 v126,s84,v126
v_mul_f32 v104,s84,v104
Mvaa 80,204,28
DN 33,36,5,0
v_mul_f32 v105,s84,v105
Mvaa 96,204,32
s_add_u32 s4,s80,32768
v_mul_f32 v127,s84,v127
CV 70,124,125
v_mul_f32 v106,s84,v106
Mvaa 112,204,36
s_and_b32 s4,s4,0x1e000
CV 71,126,127
v_mul_f32 v84,s84,v84
v_mul_f32 v107,s84,v107
Mvaa 176,208,20
s_or_b32 s4,s4,0x40000
RA 196,10,512
v_mul_f32 v85,s84,v85
CV 104,104,105
Mvaa 180,208,24
s_xor_b32 s5,s4,0x30000
v_mul_f32 v86,s84,v86
v_mul_f32 v87,s84,v87
CV 105,106,107
Mvaa 184,208,28
M0 78,141664
CV 84,84,85
CV 85,86,87
v_mul_f32 v152,s84,v152
Mvaa 188,208,32
DN 19,32,4,0
v_mul_f32 v153,s84,v153
Mvaa 192,208,36
WL 3
v_mul_f32 v128,s84,v128
v_mul_f32 v129,s84,v129
v_mul_f32 v154,s84,v154
Mvavz 116,180,36
M0 78,145888
RA 200,10,576
v_mul_f32 v130,s84,v130
v_mul_f32 v131,s84,v131
Mvavz 120,180,52
DN 33,32,4,0
CV 86,128,129
CV 87,130,131
Mvavz 124,180,68
M0 78,150112
v_mul_f32 v100,s84,v100
v_mul_f32 v101,s84,v101
v_mul_f32 v102,s84,v102
v_mul_f32 v103,s84,v103
CV 100,100,101
CV 101,102,103
v_mul_f32 v132,s84,v132
v_mul_f32 v133,s84,v133
v_mul_f32 v134,s84,v134
v_mul_f32 v135,s84,v135
CV 102,132,133
CV 103,134,135
Mvavz 128,180,84
v_mul_f32 v40,s84,v40
v_mul_f32 v41,s84,v41
v_mul_f32 v42,s84,v42
v_mul_f32 v43,s84,v43
CV 40,40,41
CV 41,42,43
v_mul_f32 v136,s84,v136
v_mul_f32 v137,s84,v137
v_mul_f32 v138,s84,v138
v_mul_f32 v139,s84,v139
CV 42,136,137
CV 43,138,139
Mvavz 132,180,100
DN 19,32,5,0
v_mul_f32 v155,s84,v155
Mvav 116,184,40
M0 78,154336
RA 204,10,640
v_mul_f32 v72,s84,v72
v_mul_f32 v73,s84,v73
v_mul_f32 v74,s84,v74
v_mul_f32 v75,s84,v75
CV 72,72,73
CV 73,74,75
v_mul_f32 v144,s84,v144
v_mul_f32 v145,s84,v145
v_mul_f32 v146,s84,v146
v_mul_f32 v147,s84,v147
CV 74,144,145
CV 75,146,147
Mvav 120,184,56
v_mul_f32 v88,s84,v88
v_mul_f32 v89,s84,v89
v_mul_f32 v90,s84,v90
v_mul_f32 v91,s84,v91
CV 88,88,89
CV 89,90,91
v_mul_f32 v148,s84,v148
v_mul_f32 v149,s84,v149
v_mul_f32 v150,s84,v150
v_mul_f32 v151,s84,v151
CV 90,148,149
CV 91,150,151
Mvav 124,184,72
DN 33,32,5,0
CV 106,152,153
CV 107,154,155
Mvav 128,184,88
v_mul_f32 v44,s84,v44
v_mul_f32 v45,s84,v45
v_mul_f32 v46,s84,v46
v_mul_f32 v47,s84,v47
CV 44,44,45
CV 45,46,47
v_mul_f32 v156,s84,v156
v_mul_f32 v157,s84,v157
v_mul_f32 v158,s84,v158
v_mul_f32 v159,s84,v159
CV 46,156,157
CV 47,158,159
Mvav 132,184,104
WL 3
v_mul_f32 v60,s84,v60
v_mul_f32 v61,s84,v61
v_mul_f32 v62,s84,v62
v_mul_f32 v63,s84,v63
CV 60,60,61
CV 61,62,63
v_mul_f32 v160,s84,v160
v_mul_f32 v161,s84,v161
v_mul_f32 v162,s84,v162
v_mul_f32 v163,s84,v163
CV 62,160,161
CV 63,162,163
Mvav 116,188,44
RA 208,10,704
v_mul_f32 v76,s84,v76
v_mul_f32 v77,s84,v77
v_mul_f32 v78,s84,v78
v_mul_f32 v79,s84,v79
CV 76,76,77
CV 77,78,79
v_mul_f32 v164,s84,v164
v_mul_f32 v165,s84,v165
v_mul_f32 v166,s84,v166
v_mul_f32 v167,s84,v167
CV 78,164,165
CV 79,166,167
Mvav 120,188,60
buffer_load_dword v8,v8,s[60:63],0 offen
v_mul_f32 v92,s84,v92
v_mul_f32 v93,s84,v93
v_mul_f32 v94,s84,v94
v_mul_f32 v95,s84,v95
CV 92,92,93
CV 93,94,95
v_mul_f32 v168,s84,v168
v_mul_f32 v169,s84,v169
v_mul_f32 v170,s84,v170
v_mul_f32 v171,s84,v171
CV 94,168,169
CV 95,170,171
Mvav 124,188,76
v_mul_f32 v108,s84,v108
v_mul_f32 v109,s84,v109
v_mul_f32 v110,s84,v110
v_mul_f32 v111,s84,v111
CV 108,108,109
CV 109,110,111
v_mul_f32 v172,s84,v172
v_mul_f32 v173,s84,v173
v_mul_f32 v174,s84,v174
v_mul_f32 v175,s84,v175
CV 110,172,173
CV 111,174,175
Mvav 128,188,92
v_mul_f32 v48,s84,v48
v_mul_f32 v49,s84,v49
v_mul_f32 v50,s84,v50
v_mul_f32 v51,s84,v51
CV 48,48,49
CV 49,50,51
v_mul_f32 v176,s84,v176
v_mul_f32 v177,s84,v177
v_mul_f32 v178,s84,v178
v_mul_f32 v179,s84,v179
CV 50,176,177
CV 51,178,179
Mvav 132,188,108
v_mul_f32 v64,s84,v64
v_mul_f32 v65,s84,v65
v_mul_f32 v66,s84,v66
v_mul_f32 v67,s84,v67
CV 64,64,65
CV 65,66,67
v_mul_f32 v180,s84,v180
v_mul_f32 v181,s84,v181
v_mul_f32 v182,s84,v182
v_mul_f32 v183,s84,v183
CV 66,180,181
CV 67,182,183
Mvav 116,192,48
v_mul_f32 v80,s84,v80
v_mul_f32 v81,s84,v81
v_mul_f32 v82,s84,v82
v_mul_f32 v83,s84,v83
CV 80,80,81
CV 81,82,83
v_mul_f32 v184,s84,v184
v_mul_f32 v185,s84,v185
v_mul_f32 v186,s84,v186
v_mul_f32 v187,s84,v187
CV 82,184,185
CV 83,186,187
Mvav 120,192,64
v_mul_f32 v96,s84,v96
v_mul_f32 v97,s84,v97
v_mul_f32 v98,s84,v98
v_mul_f32 v99,s84,v99
CV 96,96,97
CV 97,98,99
v_mul_f32 v188,s84,v188
v_mul_f32 v189,s84,v189
v_mul_f32 v190,s84,v190
v_mul_f32 v191,s84,v191
CV 98,188,189
CV 99,190,191
Mvav 124,192,80
v_mul_f32 v112,s84,v112
v_mul_f32 v113,s84,v113
v_mul_f32 v114,s84,v114
v_mul_f32 v115,s84,v115
CV 112,112,113
CV 113,114,115
v_mul_f32 v192,s84,v192
v_mul_f32 v193,s84,v193
v_mul_f32 v194,s84,v194
v_mul_f32 v195,s84,v195
CV 114,192,193
CV 115,194,195
Mvav 128,192,96
s_nop 0
Mvav 132,192,112
WL 2
s_cmp_lg_u32 s85,0
s_cbranch_scc1 LC_safe1
LC_back1:
v_max3_f32 v196,v116,v117,v118
v_max3_f32 v200,v120,v121,v122
v_max3_f32 v204,v124,v125,v126
v_max3_f32 v208,v128,v129,v130
s_nop 0
v_max3_f32 v212,v132,v133,v134
v_max3_f32 v196,v196,v119,v22
v_max3_f32 v200,v200,v123,v23
v_max3_f32 v204,v204,v127,v24
v_max3_f32 v208,v208,v131,v25
v_max3_f32 v212,v212,v135,v26
v_mov_b32 v197,v196
v_mov_b32 v201,v200
v_mov_b32 v205,v204
v_mov_b32 v209,v208
v_mov_b32 v213,v212
v_permlane16_swap_b32 v196,v197
v_permlane16_swap_b32 v200,v201
v_permlane16_swap_b32 v204,v205
v_permlane16_swap_b32 v208,v209
v_permlane16_swap_b32 v212,v213
v_max_f32 v196,v196,v197
v_max_f32 v200,v200,v201
v_max_f32 v204,v204,v205
v_max_f32 v208,v208,v209
v_max_f32 v212,v212,v213
v_mov_b32 v197,v196
v_mov_b32 v201,v200
v_mov_b32 v205,v204
v_mov_b32 v209,v208
v_mov_b32 v213,v212
v_permlane32_swap_b32 v196,v197
v_permlane32_swap_b32 v200,v201
v_permlane32_swap_b32 v204,v205
v_permlane32_swap_b32 v208,v209
v_permlane32_swap_b32 v212,v213
v_max_f32 v196,v196,v197
v_max_f32 v200,v200,v201
v_max_f32 v204,v204,v205
v_max_f32 v208,v208,v209
v_max_f32 v212,v212,v213
v_sub_f32 v199,0,v196
v_sub_f32 v198,0,v196
v_sub_f32 v197,0,v196
v_sub_f32 v196,0,v196
v_sub_f32 v203,0,v200
v_sub_f32 v202,0,v200
v_sub_f32 v201,0,v200
v_sub_f32 v200,0,v200
v_sub_f32 v207,0,v204
v_sub_f32 v206,0,v204
v_sub_f32 v205,0,v204
v_sub_f32 v204,0,v204
v_sub_f32 v211,0,v208
v_sub_f32 v210,0,v208
v_sub_f32 v209,0,v208
v_sub_f32 v208,0,v208
v_sub_f32 v215,0,v212
v_sub_f32 v214,0,v212
v_sub_f32 v213,0,v212
v_sub_f32 v212,0,v212
Mvavc 136,196,36,196
s_memrealtime s[94:95]
WL 0
s_barrier
s_add_u32 s96,s94,115
Mvav 136,200,40
M0 78,20480
RA 180,10,17312
v_add_f32 v116,v116,v196
Mvavc 140,196,52,200
RA 184,10,17376
v_add_f32 v120,v120,v200
Mvav 140,200,56
DN 19,36,6,0
Mvavc 144,196,68,204
EX 116
Mvav 144,200,72
RA 188,10,17440
v_add_f32 v117,v117,v196
Mvavc 148,196,84,208
EX 117
Mvav 148,200,88
v_add_f32 v118,v118,v196
v_add_f32 v124,v124,v204
CV 156,116,117
Mvavc 152,196,100,212
EX 118
Mvav 152,200,104
EX 120
Mvav 136,204,44
M0 78,24704
RA 192,10,17504
v_add_f32 v119,v119,v196
Mvav 136,208,48
DN 33,36,6,0
Mvav 140,204,60
EX 119
Mvav 140,208,64
v_add_f32 v121,v121,v200
v_add_f32 v128,v128,v208
CV 157,118,119
Mvav 144,204,76
EX 121
Mvav 144,208,80
RA 196,10,17824
v_add_f32 v122,v122,v200
Mvav 148,204,92
EX 122
Mvav 148,208,96
v_add_f32 v123,v123,v200
v_add_f32 v132,v132,v212
Mvav 152,204,108
M0 78,28928
EX 123
Mvav 152,208,112
WL 3
DN 19,36,7,0
CV 161,122,123
Mvavc 116,180,36,196
RA 200,10,17888
CV 160,120,121
Mvav 116,184,40
EX 124
Mvavc 120,180,52,200
v_add_f32 v125,v125,v204
EX 125
v_add_f32 v126,v126,v204
EX 126
v_add_f32 v127,v127,v204
EX 127
Mvav 120,184,56
EX 128
CV 164,124,125
CV 165,126,127
Mvavc 124,180,68,204
v_add_f32 v129,v129,v208
EX 129
v_add_f32 v130,v130,v208
EX 130
v_add_f32 v131,v131,v208
EX 131
Mvav 124,184,72
RA 204,10,17952
CV 168,128,129
CV 169,130,131
Mvavc 128,180,84,208
M0 78,33152
EX 132
v_add_f32 v133,v133,v212
EX 133
v_add_f32 v134,v134,v212
EX 134
v_add_f32 v135,v135,v212
EX 135
Mvav 128,184,88
DN 33,36,7,0
CV 172,132,133
CV 173,134,135
Mvavc 132,180,100,212
EX 136
Mvav 132,184,104
WL 3
EX 137
Mvav 116,188,44
RA 208,10,18016
CV 158,136,137
Mvav 116,192,48
EX 138
Mvav 120,188,60
EX 139
Mvav 120,192,64
EX 140
CV 159,138,139
Mvav 124,188,76
EX 141
Mvav 124,192,80
s_nop 0
TR 180,12,34624
TR 182,12,35136
CV 162,140,141
Mvav 128,188,92
EX 142
Mvav 128,192,96
EX 143
Mvav 132,188,108
EX 144
CV 163,142,143
Mvav 132,192,112
WL 4
EX 145
Mvavc 136,196,36,196
s_nop 0
TR 184,12,34656
TR 186,12,35168
CV 166,144,145
Mvav 136,200,40
EX 146
Mvavc 140,196,52,200
EX 147
Mvav 140,200,56
EX 148
CV 167,146,147
Mvavc 144,196,68,204
EX 149
EX 150
EX 151
Mvav 144,200,72
s_nop 0
TR 188,12,34688
TR 190,12,35200
CV 170,148,149
CV 171,150,151
Mvavc 148,196,84,208
EX 152
EX 153
EX 154
EX 155
Mvav 148,200,88
CV 174,152,153
CV 175,154,155
Mvavc 152,196,100,212
EX 116
Mvav 152,200,104
WL 6
EX 117
Mvav 136,204,44
s_nop 0
TR 192,12,34720
TR 194,12,35232
Mvav 136,208,48
EX 118
Mvav 140,204,60
EX 119
Mvav 140,208,64
EX 120
Mvav 144,204,76
EX 121
Mvav 144,208,80
s_nop 0
TR 196,12,34752
TR 198,12,35264
Mvav 148,204,92
EX 122
Mvav 148,208,96
EX 123
Mvav 152,204,108
EX 124
Mvav 152,208,112
WL 6
EX 125
Maavz 0,180,156
s_nop 0
TR 200,12,34784
TR 202,12,35296
Maavz 4,180,160
EX 126
Maavz 8,180,164
EX 127
Maavz 12,180,168
EX 128
Maavz 16,180,172
EX 129
Maavz 20,184,156
s_nop 0
TR 204,12,34816
TR 206,12,35328
Maavz 24,184,160
EX 130
Maavz 28,184,164
EX 131
Maavz 32,184,168
EX 132
Maavz 36,184,172
WL 6
EX 133
Maavz 40,188,156
s_nop 0
TR 208,12,34848
TR 210,12,35360
Maavz 44,188,160
EX 134
Maavz 48,188,164
EX 135
Maavz 52,188,168
EX 136
Maavz 56,188,172
EX 137
Maavz 60,192,156
EX 138
Maavz 64,192,160
EX 139
Maavz 68,192,164
EX 140
Maavz 72,192,168
EX 141
Maavz 76,192,172
WL 4
EX 142
Maavz 80,196,156
s_memrealtime s[94:95]
s_waitcnt vmcnt(17) lgkmcnt(0)
s_barrier
s_sub_u32 s95,s94,s96
s_cmp_lt_i32 s95,0
s_cbranch_scc1 LC_pc56
LC_pb56:
s_add_u32 s96,s94,115
Maavz 84,196,160
s_add_u32 s6,s80,40960
RA 180,10,51936
CV 176,116,117
Maavz 88,196,164
s_and_b32 s6,s6,0x1e000
RA 184,10,52000
CV 177,118,119
Maavz 92,196,168
s_or_b32 s6,s6,0x40000
EX 143
Maavz 96,196,172
s_xor_b32 s7,s6,0x30000
EX 144
Maavz 100,200,156
M0 78,37792
RA 188,10,52064
CV 180,120,121
Maavz 104,200,160
DN 19,32,6,0
Maavz 108,200,164
EX 145
Maavz 112,200,168
EX 146
Maavz 116,200,172
EX 147
Maavz 120,204,156
RA 192,10,52128
CV 181,122,123
Maavz 124,204,160
EX 148
Maavz 128,204,164
EX 149
Maavz 132,204,168
M0 78,42016
EX 150
Maavz 136,204,172
DN 33,32,6,0
Maavz 140,208,156
RA 196,10,52448
CV 184,124,125
Maavz 144,208,160
EX 151
Maavz 148,208,164
EX 152
Maavz 152,208,168
EX 153
Maavz 156,208,172
EX 154
Maavz 160,212,156
EX 155
Maavz 164,212,160
M0 78,46240
CV 185,126,127
CV 188,128,129
Maavz 168,212,164
DN 19,32,7,0
Maavz 172,212,168
CV 189,130,131
CV 192,132,133
Maavz 176,212,172
WL 3
CV 193,134,135
CV 178,136,137
Mvavc 116,180,36,196
RA 200,10,52512
CV 179,138,139
Mvav 116,184,40
CV 182,140,141
CV 183,142,143
Mvavc 120,180,52,200
CV 186,144,145
CV 187,146,147
Mvav 120,184,56
CV 190,148,149
CV 191,150,151
Mvavc 124,180,68,204
M0 78,50464
CV 195,154,155
Mvav 124,184,72
RA 204,10,52576
CV 194,152,153
Mvavc 128,180,84,208
DN 33,32,7,0
Mvav 128,184,88
Mvavc 132,180,100,212
Mvav 132,184,104
WL 3
Mvav 116,188,44
RA 208,10,52640
Mvav 116,192,48
Mvav 120,188,60
Mvav 120,192,64
M0 78,55104
Mvav 124,188,76
DN 19,36,4,0
Mvav 124,192,80
s_nop 0
TR 180,13,0
TR 182,13,512
Mvav 128,188,92
EX 116
Mvav 128,192,96
EX 117
Mvav 132,188,108
EX 118
Mvav 132,192,112
WL 4
EX 119
Mvavc 136,196,36,196
s_nop 0
TR 184,13,32
TR 186,13,544
Mvav 136,200,40
M0 78,59328
EX 120
Mvavc 140,196,52,200
DN 33,36,4,0
Mvav 140,200,56
EX 121
Mvavc 144,196,68,204
EX 122
Mvav 144,200,72
s_nop 0
TR 188,13,64
TR 190,13,576
Mvavc 148,196,84,208
EX 123
Mvav 148,200,88
EX 124
Mvavc 152,196,100,212
EX 125
Mvav 152,200,104
WL 6
EX 126
Mvav 136,204,44
M0 78,63552
TR 192,13,96
TR 194,13,608
Mvav 136,208,48
DN 19,36,5,0
Mvav 140,204,60
EX 127
Mvav 140,208,64
EX 128
Mvav 144,204,76
EX 129
Mvav 144,208,80
s_nop 0
TR 196,13,128
TR 198,13,640
Mvav 148,204,92
EX 130
Mvav 148,208,96
EX 131
Mvav 152,204,108
M0 78,67776
EX 132
Mvav 152,208,112
WL 6
DN 33,36,5,0
Maav 0,180,176
s_nop 0
TR 200,13,160
TR 202,13,672
Maav 4,180,180
EX 133
Maav 8,180,184
EX 134
Maav 12,180,188
EX 135
Maav 16,180,192
EX 136
Maav 20,184,176
s_nop 0
TR 204,13,192
TR 206,13,704
Maav 24,184,180
EX 137
Maav 28,184,184
EX 138
Maav 32,184,188
EX 139
Maav 36,184,192
WL 6
EX 140
Maav 40,188,176
s_nop 0
TR 208,13,224
TR 210,13,736
Maav 44,188,180
EX 141
Maav 48,188,184
EX 142
Maav 52,188,188
EX 143
Maav 56,188,192
EX 144
Maav 60,192,176
EX 145
Maav 64,192,180
EX 146
Maav 68,192,184
EX 147
Maav 72,192,188
EX 148
Maav 76,192,192
WL 4
EX 149
Maav 80,196,176
s_memrealtime s[94:95]
s_waitcnt vmcnt(17) lgkmcnt(0)
s_barrier
s_sub_u32 s95,s94,s96
s_cmp_lt_i32 s95,0
s_cbranch_scc1 LC_pc57
LC_pb57:
s_add_u32 s96,s94,115
Maav 84,196,180
s_add_u32 s4,s80,49152
RA 180,11,17312
CV 156,116,117
Maav 88,196,184
s_and_b32 s4,s4,0x1e000
RA 184,11,17376
CV 157,118,119
Maav 92,196,188
s_or_b32 s4,s4,0x40000
EX 150
Maav 96,196,192
s_xor_b32 s5,s4,0x30000
EX 151
Maav 100,200,176
M0 78,72416
RA 188,11,17440
CV 160,120,121
Maav 104,200,180
DN 19,32,4,0
Maav 108,200,184
EX 152
Maav 112,200,188
EX 153
Maav 116,200,192
EX 154
Maav 120,204,176
RA 192,11,17504
CV 161,122,123
Maav 124,204,180
EX 155
Maav 128,204,184
CV 164,124,125
CV 165,126,127
Maav 132,204,188
M0 78,76640
CV 168,128,129
CV 169,130,131
Maav 136,204,192
DN 33,32,4,0
Maav 140,208,176
RA 196,11,17824
CV 172,132,133
Maav 144,208,180
CV 173,134,135
CV 158,136,137
Maav 148,208,184
CV 159,138,139
CV 162,140,141
Maav 152,208,188
CV 163,142,143
CV 166,144,145
Maav 156,208,192
CV 167,146,147
CV 170,148,149
Maav 160,212,176
CV 171,150,151
CV 174,152,153
Maav 164,212,180
M0 78,80864
CV 175,154,155
Maav 168,212,184
DN 19,32,5,0
Maav 172,212,188
Maav 176,212,192
WL 3
Mvavc 116,180,36,196
RA 200,11,17888
Mvav 116,184,40
Mvavc 120,180,52,200
Mvav 120,184,56
Mvavc 124,180,68,204
M0 78,85088
Mvav 124,184,72
RA 204,11,17952
Mvavc 128,180,84,208
DN 33,32,5,0
Mvav 128,184,88
Mvavc 132,180,100,212
Mvav 132,184,104
WL 3
Mvav 116,188,44
RA 208,11,18016
Mvav 116,192,48
Mvav 120,188,60
Mvav 120,192,64
M0 78,89728
Mvav 124,188,76
DN 19,36,6,0
Mvav 124,192,80
s_nop 0
TR 180,13,34624
TR 182,13,35136
Mvav 128,188,92
EX 116
Mvav 128,192,96
EX 117
Mvav 132,188,108
EX 118
Mvav 132,192,112
WL 4
EX 119
Mvavc 136,196,36,196
s_nop 0
TR 184,13,34656
TR 186,13,35168
Mvav 136,200,40
M0 78,93952
EX 120
Mvavc 140,196,52,200
DN 33,36,6,0
Mvav 140,200,56
EX 121
Mvavc 144,196,68,204
EX 122
Mvav 144,200,72
s_nop 0
TR 188,13,34688
TR 190,13,35200
Mvavc 148,196,84,208
EX 123
Mvav 148,200,88
EX 124
Mvavc 152,196,100,212
EX 125
Mvav 152,200,104
WL 6
EX 126
Mvav 136,204,44
M0 78,98176
TR 192,13,34720
TR 194,13,35232
Mvav 136,208,48
DN 19,36,7,0
Mvav 140,204,60
EX 127
Mvav 140,208,64
EX 128
Mvav 144,204,76
EX 129
Mvav 144,208,80
s_nop 0
TR 196,13,34752
TR 198,13,35264
Mvav 148,204,92
EX 130
Mvav 148,208,96
EX 131
Mvav 152,204,108
M0 78,102400
EX 132
Mvav 152,208,112
WL 6
DN 33,36,7,0
Maav 0,180,156
s_nop 0
TR 200,13,34784
TR 202,13,35296
Maav 4,180,160
EX 133
Maav 8,180,164
EX 134
Maav 12,180,168
EX 135
Maav 16,180,172
EX 136
Maav 20,184,156
s_nop 0
TR 204,13,34816
TR 206,13,35328
Maav 24,184,160
EX 137
Maav 28,184,164
EX 138
Maav 32,184,168
EX 139
Maav 36,184,172
WL 6
EX 140
Maav 40,188,156
s_nop 0
TR 208,13,34848
TR 210,13,35360
Maav 44,188,160
EX 141
Maav 48,188,164
EX 142
Maav 52,188,168
EX 143
Maav 56,188,172
EX 144
Maav 60,192,156
EX 145
Maav 64,192,160
EX 146
Maav 68,192,164
EX 147
Maav 72,192,168
EX 148
Maav 76,192,172
WL 4
EX 149
Maav 80,196,156
s_memrealtime s[94:95]
s_waitcnt vmcnt(16) lgkmcnt(0)
s_barrier
s_sub_u32 s95,s94,s96
s_cmp_lt_i32 s95,0
s_cbranch_scc1 LC_pc58
LC_pb58:
s_add_u32 s96,s94,115
Maav 84,196,160
s_add_u32 s6,s80,57344
RA 180,11,51936
CV 176,116,117
Maav 88,196,164
s_and_b32 s6,s6,0x1e000
RA 184,11,52000
CV 177,118,119
Maav 92,196,168
s_or_b32 s6,s6,0x40000
EX 150
Maav 96,196,172
s_xor_b32 s7,s6,0x30000
EX 151
Maav 100,200,156
M0 78,107040
RA 188,11,52064
CV 180,120,121
Maav 104,200,160
DN 19,32,6,0
Maav 108,200,164
EX 152
Maav 112,200,168
EX 153
Maav 116,200,172
EX 154
Maav 120,204,156
RA 192,11,52128
CV 181,122,123
Maav 124,204,160
EX 155
Maav 128,204,164
CV 184,124,125
CV 185,126,127
Maav 132,204,168
M0 78,111264
CV 188,128,129
CV 189,130,131
Maav 136,204,172
DN 33,32,6,0
Maav 140,208,156
RA 196,11,52448
CV 192,132,133
Maav 144,208,160
CV 193,134,135
CV 178,136,137
Maav 148,208,164
CV 179,138,139
CV 182,140,141
Maav 152,208,168
CV 183,142,143
CV 186,144,145
Maav 156,208,172
CV 187,146,147
CV 190,148,149
Maav 160,212,156
CV 191,150,151
CV 194,152,153
Maav 164,212,160
M0 78,115488
CV 195,154,155
Maav 168,212,164
DN 19,32,7,0
Maav 172,212,168
Maav 176,212,172
WL 3
Mvavc 116,180,36,196
RA 200,11,52512
Mvav 116,184,40
Mvavc 120,180,52,200
Mvav 120,184,56
Mvavc 124,180,68,204
M0 78,119712
Mvav 124,184,72
RA 204,11,52576
Mvavc 128,180,84,208
DN 33,32,7,0
Mvav 128,184,88
Mvavc 132,180,100,212
Mvav 132,184,104
WL 3
Mvav 116,188,44
RA 208,11,52640
Mvav 116,192,48
Mvav 120,188,60
Mvav 120,192,64
M0 78,124352
Mvav 124,188,76
DN 19,36,4,0
Mvav 124,192,80
s_nop 0
TR 180,12,0
TR 182,12,512
Mvav 128,188,92
EX 116
Mvav 128,192,96
EX 117
Mvav 132,188,108
EX 118
Mvav 132,192,112
WL 4
EX 119
Mvavc 136,196,36,196
s_nop 0
TR 184,12,32
TR 186,12,544
Mvav 136,200,40
M0 78,128576
EX 120
Mvavc 140,196,52,200
DN 33,36,4,0
Mvav 140,200,56
EX 121
Mvavc 144,196,68,204
EX 122
Mvav 144,200,72
s_nop 0
TR 188,12,64
TR 190,12,576
Mvavc 148,196,84,208
s_add_u32 s98,s71,90112
v_lshl_add_u32 v8,v2,1,s98
CV 156,116,117
Mvav 148,200,88
buffer_load_dword v8,v8,s[60:63],0 offen
Mvavc 152,196,100,212
EX 123
Mvav 152,200,104
WL 6
EX 124
Mvav 136,204,44
M0 78,132800
TR 192,12,96
TR 194,12,608
Mvav 136,208,48
DN 19,36,5,0
Mvav 140,204,60
EX 125
Mvav 140,208,64
EX 126
Mvav 144,204,76
EX 127
Mvav 144,208,80
s_nop 0
TR 196,12,128
TR 198,12,640
Mvav 148,204,92
EX 128
Mvav 148,208,96
EX 129
Mvav 152,204,108
M0 78,137024
EX 130
Mvav 152,208,112
WL 6
DN 33,36,5,0
Maav 0,180,176
s_nop 0
TR 200,12,160
TR 202,12,672
Maav 4,180,180
EX 131
Maav 8,180,184
EX 132
Maav 12,180,188
EX 133
Maav 16,180,192
EX 134
Maav 20,184,176
s_nop 0
TR 204,12,192
TR 206,12,704
Maav 24,184,180
EX 135
Maav 28,184,184
EX 136
Maav 32,184,188
EX 137
Maav 36,184,192
WL 6
EX 138
Maav 40,188,176
s_nop 0
TR 208,12,224
TR 210,12,736
Maav 44,188,180
EX 139
Maav 48,188,184
EX 140
Maav 52,188,188
EX 141
Maav 56,188,192
EX 142
Maav 60,192,176
EX 143
Maav 64,192,180
EX 144
Maav 68,192,184
EX 145
Maav 72,192,188
EX 146
Maav 76,192,192
WL 4
EX 147
Maav 80,196,176
s_memrealtime s[94:95]
s_waitcnt vmcnt(17) lgkmcnt(0)
s_barrier
s_sub_u32 s95,s94,s96
s_cmp_lt_i32 s95,0
s_cbranch_scc1 LC_pc59
LC_pb59:
s_add_u32 s96,s94,115
Maav 84,196,180
s_add_u32 s4,s80,65536
RA 180,10,17312
CV 157,118,119
Maav 88,196,184
s_and_b32 s4,s4,0x1e000
RA 184,10,17376
CV 160,120,121
Maav 92,196,188
s_or_b32 s4,s4,0x40000
EX 148
Maav 96,196,192
s_xor_b32 s5,s4,0x30000
EX 149
Maav 100,200,176
M0 78,141664
RA 188,10,17440
CV 161,122,123
Maav 104,200,180
DN 19,32,4,0
Maav 108,200,184
EX 150
Maav 112,200,188
EX 151
Maav 116,200,192
EX 152
Maav 120,204,176
RA 192,10,17504
CV 164,124,125
Maav 124,204,180
EX 153
Maav 128,204,184
EX 154
Maav 132,204,188
M0 78,145888
EX 155
Maav 136,204,192
DN 33,32,4,0
Maav 140,208,176
RA 196,10,17824
CV 165,126,127
Maav 144,208,180
CV 168,128,129
CV 169,130,131
Maav 148,208,184
CV 172,132,133
CV 173,134,135
Maav 152,208,188
CV 158,136,137
CV 159,138,139
Maav 156,208,192
CV 162,140,141
CV 163,142,143
Maav 160,212,176
CV 166,144,145
CV 167,146,147
Maav 164,212,180
M0 78,150112
CV 170,148,149
CV 171,150,151
Maav 168,212,184
DN 19,32,5,0
Maav 172,212,188
CV 174,152,153
CV 175,154,155
Maav 176,212,192
WL 3
Mvavc 116,180,36,196
RA 200,10,17888
Mvav 116,184,40
Mvavc 120,180,52,200
Mvav 120,184,56
Mvavc 124,180,68,204
M0 78,154336
Mvav 124,184,72
RA 204,10,17952
Mvavc 128,180,84,208
DN 33,32,5,0
Mvav 128,184,88
Mvavc 132,180,100,212
Mvav 132,184,104
WL 3
Mvav 116,188,44
RA 208,10,18016
Mvav 116,192,48
Mvav 120,188,60
Mvav 120,192,64
M0 78,20480
Mvav 124,188,76
DN 19,36,6,0
Mvav 124,192,80
s_nop 0
TR 180,12,34624
TR 182,12,35136
Mvav 128,188,92
EX 116
Mvav 128,192,96
EX 117
Mvav 132,188,108
EX 118
Mvav 132,192,112
WL 4
EX 119
Mvavc 136,196,36,196
s_nop 0
TR 184,12,34656
TR 186,12,35168
Mvav 136,200,40
M0 78,24704
EX 120
Mvavc 140,196,52,200
DN 33,36,6,0
Mvav 140,200,56
EX 121
Mvavc 144,196,68,204
EX 122
Mvav 144,200,72
s_nop 0
TR 188,12,34688
TR 190,12,35200
Mvavc 148,196,84,208
EX 123
Mvav 148,200,88
EX 124
Mvavc 152,196,100,212
EX 125
Mvav 152,200,104
WL 6
EX 126
Mvav 136,204,44
M0 78,28928
TR 192,12,34720
TR 194,12,35232
Mvav 136,208,48
DN 19,36,7,0
Mvav 140,204,60
EX 127
Mvav 140,208,64
EX 128
Mvav 144,204,76
EX 129
Mvav 144,208,80
s_nop 0
TR 196,12,34752
TR 198,12,35264
Mvav 148,204,92
EX 130
Mvav 148,208,96
EX 131
Mvav 152,204,108
M0 78,33152
EX 132
Mvav 152,208,112
WL 6
DN 33,36,7,0
Maav 0,180,156
s_nop 0
TR 200,12,34784
TR 202,12,35296
Maav 4,180,160
EX 133
Maav 8,180,164
EX 134
Maav 12,180,168
EX 135
Maav 16,180,172
EX 136
Maav 20,184,156
s_nop 0
TR 204,12,34816
TR 206,12,35328
Maav 24,184,160
EX 137
Maav 28,184,164
EX 138
Maav 32,184,168
EX 139
Maav 36,184,172
WL 6
EX 140
Maav 40,188,156
s_nop 0
TR 208,12,34848
TR 210,12,35360
Maav 44,188,160
EX 141
Maav 48,188,164
EX 142
Maav 52,188,168
EX 143
Maav 56,188,172
EX 144
Maav 60,192,156
EX 145
Maav 64,192,160
EX 146
Maav 68,192,164
EX 147
Maav 72,192,168
EX 148
Maav 76,192,172
WL 4
EX 149
Maav 80,196,156
s_memrealtime s[94:95]
s_waitcnt vmcnt(17) lgkmcnt(0)
s_barrier
s_sub_u32 s95,s94,s96
s_cmp_lt_i32 s95,0
s_cbranch_scc1 LC_pc60
LC_pb60:
s_add_u32 s96,s94,115
Maav 84,196,160
s_add_u32 s6,s80,73728
RA 180,10,51936
CV 176,116,117
Maav 88,196,164
s_and_b32 s6,s6,0x1e000
RA 184,10,52000
CV 177,118,119
Maav 92,196,168
s_or_b32 s6,s6,0x40000
EX 150
Maav 96,196,172
s_xor_b32 s7,s6,0x30000
EX 151
Maav 100,200,156
M0 78,37792
RA 188,10,52064
CV 180,120,121
Maav 104,200,160
DN 19,32,6,0
Maav 108,200,164
EX 152
Maav 112,200,168
EX 153
Maav 116,200,172
EX 154
Maav 120,204,156
RA 192,10,52128
CV 181,122,123
Maav 124,204,160
EX 155
Maav 128,204,164
CV 184,124,125
CV 185,126,127
Maav 132,204,168
M0 78,42016
CV 188,128,129
CV 189,130,131
Maav 136,204,172
DN 33,32,6,0
Maav 140,208,156
RA 196,10,52448
CV 192,132,133
Maav 144,208,160
CV 193,134,135
CV 178,136,137
Maav 148,208,164
CV 179,138,139
CV 182,140,141
Maav 152,208,168
CV 183,142,143
CV 186,144,145
Maav 156,208,172
CV 187,146,147
CV 190,148,149
Maav 160,212,156
CV 191,150,151
CV 194,152,153
Maav 164,212,160
M0 78,46240
CV 195,154,155
Maav 168,212,164
DN 19,32,7,0
Maav 172,212,168
Maav 176,212,172
WL 3
Mvavc 116,180,36,196
RA 200,10,52512
Mvav 116,184,40
Mvavc 120,180,52,200
Mvav 120,184,56
Mvavc 124,180,68,204
M0 78,50464
Mvav 124,184,72
RA 204,10,52576
Mvavc 128,180,84,208
DN 33,32,7,0
Mvav 128,184,88
Mvavc 132,180,100,212
Mvav 132,184,104
WL 3
Mvav 116,188,44
RA 208,10,52640
Mvav 116,192,48
Mvav 120,188,60
Mvav 120,192,64
M0 78,55104
Mvav 124,188,76
DN 19,36,4,0
Mvav 124,192,80
s_nop 0
TR 180,13,0
TR 182,13,512
Mvav 128,188,92
EX 116
Mvav 128,192,96
EX 117
Mvav 132,188,108
EX 118
Mvav 132,192,112
WL 4
EX 119
Mvavc 136,196,36,196
s_nop 0
TR 184,13,32
TR 186,13,544
Mvav 136,200,40
M0 78,59328
EX 120
Mvavc 140,196,52,200
DN 33,36,4,0
Mvav 140,200,56
EX 121
Mvavc 144,196,68,204
EX 122
Mvav 144,200,72
s_nop 0
TR 188,13,64
TR 190,13,576
Mvavc 148,196,84,208
EX 123
Mvav 148,200,88
EX 124
Mvavc 152,196,100,212
EX 125
Mvav 152,200,104
WL 6
EX 126
Mvav 136,204,44
M0 78,63552
TR 192,13,96
TR 194,13,608
Mvav 136,208,48
DN 19,36,5,0
Mvav 140,204,60
EX 127
Mvav 140,208,64
EX 128
Mvav 144,204,76
EX 129
Mvav 144,208,80
s_nop 0
TR 196,13,128
TR 198,13,640
Mvav 148,204,92
EX 130
Mvav 148,208,96
EX 131
Mvav 152,204,108
M0 78,67776
EX 132
Mvav 152,208,112
WL 6
DN 33,36,5,0
Maav 0,180,176
s_nop 0
TR 200,13,160
TR 202,13,672
Maav 4,180,180
EX 133
Maav 8,180,184
EX 134
Maav 12,180,188
EX 135
Maav 16,180,192
EX 136
Maav 20,184,176
s_nop 0
TR 204,13,192
TR 206,13,704
Maav 24,184,180
EX 137
Maav 28,184,184
EX 138
Maav 32,184,188
EX 139
Maav 36,184,192
WL 6
EX 140
Maav 40,188,176
s_nop 0
TR 208,13,224
TR 210,13,736
Maav 44,188,180
EX 141
Maav 48,188,184
EX 142
Maav 52,188,188
EX 143
Maav 56,188,192
EX 144
Maav 60,192,176
EX 145
Maav 64,192,180
EX 146
Maav 68,192,184
EX 147
Maav 72,192,188
EX 148
Maav 76,192,192
WL 4
EX 149
Maav 80,196,176
s_memrealtime s[94:95]
s_waitcnt vmcnt(16) lgkmcnt(0)
s_barrier
s_sub_u32 s95,s94,s96
s_cmp_lt_i32 s95,0
s_cbranch_scc1 LC_pc61
LC_pb61:
s_add_u32 s96,s94,115
Maav 84,196,180
s_add_u32 s4,s80,81920
RA 180,11,17312
CV 156,116,117
Maav 88,196,184
s_and_b32 s4,s4,0x1e000
RA 184,11,17376
CV 157,118,119
Maav 92,196,188
s_or_b32 s4,s4,0x40000
EX 150
Maav 96,196,192
s_xor_b32 s5,s4,0x30000
EX 151
Maav 100,200,176
M0 78,72416
RA 188,11,17440
CV 160,120,121
Maav 104,200,180
DN 19,32,4,0
Maav 108,200,184
EX 152
Maav 112,200,188
EX 153
Maav 116,200,192
EX 154
Maav 120,204,176
RA 192,11,17504
CV 161,122,123
Maav 124,204,180
EX 155
Maav 128,204,184
CV 164,124,125
CV 165,126,127
Maav 132,204,188
M0 78,76640
CV 168,128,129
CV 169,130,131
Maav 136,204,192
DN 33,32,4,0
Maav 140,208,176
RA 196,11,17824
CV 172,132,133
Maav 144,208,180
CV 173,134,135
CV 158,136,137
Maav 148,208,184
CV 159,138,139
CV 162,140,141
Maav 152,208,188
CV 163,142,143
CV 166,144,145
Maav 156,208,192
CV 167,146,147
CV 170,148,149
Maav 160,212,176
CV 171,150,151
CV 174,152,153
Maav 164,212,180
M0 78,80864
CV 175,154,155
Maav 168,212,184
DN 19,32,5,0
Maav 172,212,188
Maav 176,212,192
WL 3
Mvavc 116,180,36,196
RA 200,11,17888
Mvav 116,184,40
Mvavc 120,180,52,200
Mvav 120,184,56
Mvavc 124,180,68,204
M0 78,85088
Mvav 124,184,72
RA 204,11,17952
Mvavc 128,180,84,208
DN 33,32,5,0
Mvav 128,184,88
Mvavc 132,180,100,212
Mvav 132,184,104
WL 3
Mvav 116,188,44
RA 208,11,18016
Mvav 116,192,48
Mvav 120,188,60
Mvav 120,192,64
M0 78,89728
Mvav 124,188,76
DN 19,36,6,0
Mvav 124,192,80
s_nop 0
TR 180,13,34624
TR 182,13,35136
Mvav 128,188,92
EX 116
Mvav 128,192,96
EX 117
Mvav 132,188,108
EX 118
Mvav 132,192,112
WL 4
EX 119
Mvavc 136,196,36,196
s_nop 0
TR 184,13,34656
TR 186,13,35168
Mvav 136,200,40
M0 78,93952
EX 120
Mvavc 140,196,52,200
DN 33,36,6,0
Mvav 140,200,56
EX 121
Mvavc 144,196,68,204
EX 122
Mvav 144,200,72
s_nop 0
TR 188,13,34688
TR 190,13,35200
Mvavc 148,196,84,208
EX 123
Mvav 148,200,88
EX 124
Mvavc 152,196,100,212
EX 125
Mvav 152,200,104
WL 6
EX 126
Mvav 136,204,44
M0 78,98176
TR 192,13,34720
TR 194,13,35232
Mvav 136,208,48
DN 19,36,7,0
Mvav 140,204,60
EX 127
Mvav 140,208,64
EX 128
Mvav 144,204,76
EX 129
Mvav 144,208,80
s_nop 0
TR 196,13,34752
TR 198,13,35264
Mvav 148,204,92
EX 130
Mvav 148,208,96
EX 131
Mvav 152,204,108
M0 78,102400
EX 132
Mvav 152,208,112
WL 6
DN 33,36,7,0
Maav 0,180,156
s_nop 0
TR 200,13,34784
TR 202,13,35296
Maav 4,180,160
EX 133
Maav 8,180,164
EX 134
Maav 12,180,168
EX 135
Maav 16,180,172
EX 136
Maav 20,184,156
s_nop 0
TR 204,13,34816
TR 206,13,35328
Maav 24,184,160
EX 137
Maav 28,184,164
EX 138
Maav 32,184,168
EX 139
Maav 36,184,172
WL 6
EX 140
Maav 40,188,156
s_nop 0
TR 208,13,34848
TR 210,13,35360
Maav 44,188,160
EX 141
Maav 48,188,164
EX 142
Maav 52,188,168
EX 143
Maav 56,188,172
EX 144
Maav 60,192,156
EX 145
Maav 64,192,160
EX 146
Maav 68,192,164
EX 147
Maav 72,192,168
EX 148
Maav 76,192,172
WL 4
EX 149
Maav 80,196,156
s_memrealtime s[94:95]
s_waitcnt vmcnt(16) lgkmcnt(0)
s_barrier
s_sub_u32 s95,s94,s96
s_cmp_lt_i32 s95,0
s_cbranch_scc1 LC_pc62
LC_pb62:
s_add_u32 s96,s94,115
Maav 84,196,160
s_add_u32 s6,s80,90112
RA 180,11,51936
CV 176,116,117
Maav 88,196,164
s_and_b32 s6,s6,0x1e000
RA 184,11,52000
CV 177,118,119
Maav 92,196,168
s_or_b32 s6,s6,0x40000
EX 150
Maav 96,196,172
s_xor_b32 s7,s6,0x30000
EX 151
Maav 100,200,156
M0 78,107040
RA 188,11,52064
CV 180,120,121
Maav 104,200,160
DN 19,32,6,0
Maav 108,200,164
EX 152
Maav 112,200,168
EX 153
Maav 116,200,172
EX 154
Maav 120,204,156
RA 192,11,52128
CV 181,122,123
Maav 124,204,160
EX 155
Maav 128,204,164
CV 184,124,125
CV 185,126,127
Maav 132,204,168
M0 78,111264
CV 188,128,129
CV 189,130,131
Maav 136,204,172
DN 33,32,6,0
Maav 140,208,156
RA 196,11,52448
CV 192,132,133
Maav 144,208,160
CV 193,134,135
CV 178,136,137
Maav 148,208,164
CV 179,138,139
CV 182,140,141
Maav 152,208,168
CV 183,142,143
CV 186,144,145
Maav 156,208,172
CV 187,146,147
CV 190,148,149
Maav 160,212,156
CV 191,150,151
CV 194,152,153
Maav 164,212,160
M0 78,115488
CV 195,154,155
Maav 168,212,164
DN 19,32,7,0
Maav 172,212,168
Maav 176,212,172
WL 3
Mvavc 116,180,36,196
RA 200,11,52512
Mvav 116,184,40
Mvavc 120,180,52,200
Mvav 120,184,56
Mvavc 124,180,68,204
M0 78,119712
Mvav 124,184,72
RA 204,11,52576
Mvavc 128,180,84,208
DN 33,32,7,0
Mvav 128,184,88
Mvavc 132,180,100,212
Mvav 132,184,104
WL 3
Mvav 116,188,44
RA 208,11,52640
Mvav 116,192,48
Mvav 120,188,60
Mvav 120,192,64
M0 78,124352
Mvav 124,188,76
DN 19,36,4,0
Mvav 124,192,80
s_nop 0
TR 180,12,0
TR 182,12,512
Mvav 128,188,92
EX 116
Mvav 128,192,96
EX 117
Mvav 132,188,108
EX 118
Mvav 132,192,112
WL 4
EX 119
Mvavc 136,196,36,196
s_nop 0
TR 184,12,32
TR 186,12,544
Mvav 136,200,40
M0 78,128576
EX 120
Mvavc 140,196,52,200
DN 33,36,4,0
Mvav 140,200,56
EX 121
Mvavc 144,196,68,204
EX 122
Mvav 144,200,72
s_nop 0
TR 188,12,64
TR 190,12,576
Mvavc 148,196,84,208
s_add_u32 s98,s71,98304
v_lshl_add_u32 v8,v2,1,s98
CV 156,116,117
Mvav 148,200,88
buffer_load_dword v8,v8,s[60:63],0 offen
Mvavc 152,196,100,212
EX 123
Mvav 152,200,104
WL 6
EX 124
Mvav 136,204,44
M0 78,132800
TR 192,12,96
TR 194,12,608
Mvav 136,208,48
DN 19,36,5,0
Mvav 140,204,60
EX 125
Mvav 140,208,64
EX 126
Mvav 144,204,76
EX 127
Mvav 144,208,80
s_nop 0
TR 196,12,128
TR 198,12,640
Mvav 148,204,92
EX 128
Mvav 148,208,96
EX 129
Mvav 152,204,108
M0 78,137024
EX 130
Mvav 152,208,112
WL 6
DN 33,36,5,0
Maav 0,180,176
s_nop 0
TR 200,12,160
TR 202,12,672
Maav 4,180,180
EX 131
Maav 8,180,184
EX 132
Maav 12,180,188
EX 133
Maav 16,180,192
EX 134
Maav 20,184,176
s_nop 0
TR 204,12,192
TR 206,12,704
Maav 24,184,180
EX 135
Maav 28,184,184
EX 136
Maav 32,184,188
EX 137
Maav 36,184,192
WL 6
EX 138
Maav 40,188,176
s_nop 0
TR 208,12,224
TR 210,12,736
Maav 44,188,180
EX 139
Maav 48,188,184
EX 140
Maav 52,188,188
EX 141
Maav 56,188,192
EX 142
Maav 60,192,176
EX 143
Maav 64,192,180
EX 144
Maav 68,192,184
EX 145
Maav 72,192,188
EX 146
Maav 76,192,192
WL 4
EX 147
Maav 80,196,176
s_memrealtime s[94:95]
s_waitcnt vmcnt(17) lgkmcnt(0)
s_barrier
s_sub_u32 s95,s94,s96
s_cmp_lt_i32 s95,0
s_cbranch_scc1 LC_pc63
LC_pb63:
s_add_u32 s96,s94,115
Maav 84,196,180
s_add_u32 s4,s80,98304
RA 180,10,17312
CV 157,118,119
Maav 88,196,184
s_and_b32 s4,s4,0x1e000
RA 184,10,17376
CV 160,120,121
Maav 92,196,188
s_or_b32 s4,s4,0x40000
EX 148
Maav 96,196,192
s_xor_b32 s5,s4,0x30000
EX 149
Maav 100,200,176
M0 78,141664
RA 188,10,17440
CV 161,122,123
Maav 104,200,180
DN 19,32,4,0
Maav 108,200,184
EX 150
Maav 112,200,188
EX 151
Maav 116,200,192
EX 152
Maav 120,204,176
RA 192,10,17504
CV 164,124,125
Maav 124,204,180
EX 153
Maav 128,204,184
EX 154
Maav 132,204,188
M0 78,145888
EX 155
Maav 136,204,192
DN 33,32,4,0
Maav 140,208,176
RA 196,10,17824
CV 165,126,127
Maav 144,208,180
CV 168,128,129
CV 169,130,131
Maav 148,208,184
CV 172,132,133
CV 173,134,135
Maav 152,208,188
CV 158,136,137
CV 159,138,139
Maav 156,208,192
CV 162,140,141
CV 163,142,143
Maav 160,212,176
CV 166,144,145
CV 167,146,147
Maav 164,212,180
M0 78,150112
CV 170,148,149
CV 171,150,151
Maav 168,212,184
DN 19,32,5,0
Maav 172,212,188
CV 174,152,153
CV 175,154,155
Maav 176,212,192
WL 3
Mvavc 116,180,36,196
RA 200,10,17888
Mvav 116,184,40
Mvavc 120,180,52,200
Mvav 120,184,56
Mvavc 124,180,68,204
M0 78,154336
Mvav 124,184,72
RA 204,10,17952
Mvavc 128,180,84,208
DN 33,32,5,0
Mvav 128,184,88
Mvavc 132,180,100,212
Mvav 132,184,104
WL 3
Mvav 116,188,44
RA 208,10,18016
Mvav 116,192,48
Mvav 120,188,60
Mvav 120,192,64
M0 78,20480
Mvav 124,188,76
DN 19,36,6,0
Mvav 124,192,80
s_nop 0
TR 180,12,34624
TR 182,12,35136
Mvav 128,188,92
EX 116
Mvav 128,192,96
EX 117
Mvav 132,188,108
EX 118
Mvav 132,192,112
WL 4
EX 119
Mvavc 136,196,36,196
s_nop 0
TR 184,12,34656
TR 186,12,35168
Mvav 136,200,40
M0 78,24704
EX 120
Mvavc 140,196,52,200
DN 33,36,6,0
Mvav 140,200,56
EX 121
Mvavc 144,196,68,204
EX 122
Mvav 144,200,72
s_nop 0
TR 188,12,34688
TR 190,12,35200
Mvavc 148,196,84,208
EX 123
Mvav 148,200,88
EX 124
Mvavc 152,196,100,212
EX 125
Mvav 152,200,104
WL 6
EX 126
Mvav 136,204,44
M0 78,28928
TR 192,12,34720
TR 194,12,35232
Mvav 136,208,48
DN 19,36,7,0
Mvav 140,204,60
EX 127
Mvav 140,208,64
EX 128
Mvav 144,204,76
EX 129
Mvav 144,208,80
s_nop 0
TR 196,12,34752
TR 198,12,35264
Mvav 148,204,92
EX 130
Mvav 148,208,96
EX 131
Mvav 152,204,108
M0 78,33152
EX 132
Mvav 152,208,112
WL 6
DN 33,36,7,0
Maav 0,180,156
s_nop 0
TR 200,12,34784
TR 202,12,35296
Maav 4,180,160
EX 133
Maav 8,180,164
EX 134
Maav 12,180,168
EX 135
Maav 16,180,172
EX 136
Maav 20,184,156
s_nop 0
TR 204,12,34816
TR 206,12,35328
Maav 24,184,160
EX 137
Maav 28,184,164
EX 138
Maav 32,184,168
EX 139
Maav 36,184,172
WL 6
EX 140
Maav 40,188,156
s_nop 0
TR 208,12,34848
TR 210,12,35360
Maav 44,188,160
EX 141
Maav 48,188,164
EX 142
Maav 52,188,168
EX 143
Maav 56,188,172
EX 144
Maav 60,192,156
EX 145
Maav 64,192,160
EX 146
Maav 68,192,164
EX 147
Maav 72,192,168
EX 148
Maav 76,192,172
WL 4
EX 149
Maav 80,196,156
s_memrealtime s[94:95]
s_waitcnt vmcnt(17) lgkmcnt(0)
s_barrier
s_sub_u32 s95,s94,s96
s_cmp_lt_i32 s95,0
s_cbranch_scc1 LC_pc64
LC_pb64:
s_add_u32 s96,s94,115
Maav 84,196,160
s_add_u32 s6,s80,106496
RA 180,10,51936
CV 176,116,117
Maav 88,196,164
s_and_b32 s6,s6,0x1e000
RA 184,10,52000
CV 177,118,119
Maav 92,196,168
s_or_b32 s6,s6,0x40000
EX 150
Maav 96,196,172
s_xor_b32 s7,s6,0x30000
EX 151
Maav 100,200,156
M0 78,37792
RA 188,10,52064
CV 180,120,121
Maav 104,200,160
DN 19,32,6,0
Maav 108,200,164
EX 152
Maav 112,200,168
EX 153
Maav 116,200,172
EX 154
Maav 120,204,156
RA 192,10,52128
CV 181,122,123
Maav 124,204,160
EX 155
Maav 128,204,164
CV 184,124,125
CV 185,126,127
Maav 132,204,168
M0 78,42016
CV 188,128,129
CV 189,130,131
Maav 136,204,172
DN 33,32,6,0
Maav 140,208,156
RA 196,10,52448
CV 192,132,133
Maav 144,208,160
CV 193,134,135
CV 178,136,137
Maav 148,208,164
CV 179,138,139
CV 182,140,141
Maav 152,208,168
CV 183,142,143
CV 186,144,145
Maav 156,208,172
CV 187,146,147
CV 190,148,149
Maav 160,212,156
CV 191,150,151
CV 194,152,153
Maav 164,212,160
M0 78,46240
CV 195,154,155
Maav 168,212,164
DN 19,32,7,0
Maav 172,212,168
Maav 176,212,172
WL 3
Mvavc 116,180,36,196
RA 200,10,52512
Mvav 116,184,40
Mvavc 120,180,52,200
Mvav 120,184,56
Mvavc 124,180,68,204
M0 78,50464
Mvav 124,184,72
RA 204,10,52576
Mvavc 128,180,84,208
DN 33,32,7,0
Mvav 128,184,88
Mvavc 132,180,100,212
Mvav 132,184,104
WL 3
Mvav 116,188,44
RA 208,10,52640
Mvav 116,192,48
Mvav 120,188,60
Mvav 120,192,64
M0 78,55104
Mvav 124,188,76
DN 19,36,4,0
Mvav 124,192,80
s_nop 0
TR 180,13,0
TR 182,13,512
Mvav 128,188,92
EX 116
Mvav 128,192,96
EX 117
Mvav 132,188,108
EX 118
Mvav 132,192,112
WL 4
EX 119
Mvavc 136,196,36,196
s_nop 0
TR 184,13,32
TR 186,13,544
Mvav 136,200,40
M0 78,59328
EX 120
Mvavc 140,196,52,200
DN 33,36,4,0
Mvav 140,200,56
EX 121
Mvavc 144,196,68,204
EX 122
Mvav 144,200,72
s_nop 0
TR 188,13,64
TR 190,13,576
Mvavc 148,196,84,208
EX 123
Mvav 148,200,88
EX 124
Mvavc 152,196,100,212
EX 125
Mvav 152,200,104
WL 6
EX 126
Mvav 136,204,44
M0 78,63552
TR 192,13,96
TR 194,13,608
Mvav 136,208,48
DN 19,36,5,0
Mvav 140,204,60
EX 127
Mvav 140,208,64
EX 128
Mvav 144,204,76
EX 129
Mvav 144,208,80
s_nop 0
TR 196,13,128
TR 198,13,640
Mvav 148,204,92
EX 130
Mvav 148,208,96
EX 131
Mvav 152,204,108
M0 78,67776
EX 132
Mvav 152,208,112
WL 6
DN 33,36,5,0
Maav 0,180,176
s_nop 0
TR 200,13,160
TR 202,13,672
Maav 4,180,180
EX 133
Maav 8,180,184
EX 134
Maav 12,180,188
EX 135
Maav 16,180,192
EX 136
Maav 20,184,176
s_nop 0
TR 204,13,192
TR 206,13,704
Maav 24,184,180
EX 137
Maav 28,184,184
EX 138
Maav 32,184,188
EX 139
Maav 36,184,192
WL 6
EX 140
Maav 40,188,176
s_nop 0
TR 208,13,224
TR 210,13,736
Maav 44,188,180
EX 141
Maav 48,188,184
EX 142
Maav 52,188,188
EX 143
Maav 56,188,192
EX 144
Maav 60,192,176
EX 145
Maav 64,192,180
EX 146
Maav 68,192,184
EX 147
Maav 72,192,188
EX 148
Maav 76,192,192
WL 4
EX 149
Maav 80,196,176
s_memrealtime s[94:95]
s_waitcnt vmcnt(16) lgkmcnt(0)
s_barrier
s_sub_u32 s95,s94,s96
s_cmp_lt_i32 s95,0
s_cbranch_scc1 LC_pc65
LC_pb65:
s_add_u32 s96,s94,115
Maav 84,196,180
s_add_u32 s4,s80,114688
RA 180,11,17312
CV 156,116,117
Maav 88,196,184
s_and_b32 s4,s4,0x1e000
RA 184,11,17376
CV 157,118,119
Maav 92,196,188
s_or_b32 s4,s4,0x40000
EX 150
Maav 96,196,192
s_xor_b32 s5,s4,0x30000
EX 151
Maav 100,200,176
M0 78,72416
RA 188,11,17440
CV 160,120,121
Maav 104,200,180
DN 19,32,4,0
Maav 108,200,184
EX 152
Maav 112,200,188
EX 153
Maav 116,200,192
EX 154
Maav 120,204,176
RA 192,11,17504
CV 161,122,123
Maav 124,204,180
EX 155
Maav 128,204,184
CV 164,124,125
CV 165,126,127
Maav 132,204,188
M0 78,76640
CV 168,128,129
CV 169,130,131
Maav 136,204,192
DN 33,32,4,0
Maav 140,208,176
RA 196,11,17824
CV 172,132,133
Maav 144,208,180
CV 173,134,135
CV 158,136,137
Maav 148,208,184
CV 159,138,139
CV 162,140,141
Maav 152,208,188
CV 163,142,143
CV 166,144,145
Maav 156,208,192
CV 167,146,147
CV 170,148,149
Maav 160,212,176
CV 171,150,151
CV 174,152,153
Maav 164,212,180
M0 78,80864
CV 175,154,155
Maav 168,212,184
DN 19,32,5,0
Maav 172,212,188
Maav 176,212,192
WL 3
Mvavc 116,180,36,196
RA 200,11,17888
Mvav 116,184,40
Mvavc 120,180,52,200
Mvav 120,184,56
Mvavc 124,180,68,204
M0 78,85088
Mvav 124,184,72
RA 204,11,17952
Mvavc 128,180,84,208
DN 33,32,5,0
Mvav 128,184,88
Mvavc 132,180,100,212
Mvav 132,184,104
WL 3
Mvav 116,188,44
RA 208,11,18016
Mvav 116,192,48
Mvav 120,188,60
Mvav 120,192,64
M0 78,89728
Mvav 124,188,76
DN 19,36,6,0
Mvav 124,192,80
s_nop 0
TR 180,13,34624
TR 182,13,35136
Mvav 128,188,92
EX 116
Mvav 128,192,96
EX 117
Mvav 132,188,108
EX 118
Mvav 132,192,112
WL 4
EX 119
Mvavc 136,196,36,196
s_nop 0
TR 184,13,34656
TR 186,13,35168
Mvav 136,200,40
M0 78,93952
EX 120
Mvavc 140,196,52,200
DN 33,36,6,0
Mvav 140,200,56
EX 121
Mvavc 144,196,68,204
EX 122
Mvav 144,200,72
s_nop 0
TR 188,13,34688
TR 190,13,35200
Mvavc 148,196,84,208
EX 123
Mvav 148,200,88
EX 124
Mvavc 152,196,100,212
EX 125
Mvav 152,200,104
WL 6
EX 126
Mvav 136,204,44
M0 78,98176
TR 192,13,34720
TR 194,13,35232
Mvav 136,208,48
DN 19,36,7,0
Mvav 140,204,60
EX 127
Mvav 140,208,64
EX 128
Mvav 144,204,76
EX 129
Mvav 144,208,80
s_nop 0
TR 196,13,34752
TR 198,13,35264
Mvav 148,204,92
EX 130
Mvav 148,208,96
EX 131
Mvav 152,204,108
M0 78,102400
EX 132
Mvav 152,208,112
WL 6
DN 33,36,7,0
Maav 0,180,156
s_nop 0
TR 200,13,34784
TR 202,13,35296
Maav 4,180,160
EX 133
Maav 8,180,164
EX 134
Maav 12,180,168
EX 135
Maav 16,180,172
EX 136
Maav 20,184,156
s_nop 0
TR 204,13,34816
TR 206,13,35328
Maav 24,184,160
EX 137
Maav 28,184,164
EX 138
Maav 32,184,168
EX 139
Maav 36,184,172
WL 6
EX 140
Maav 40,188,156
s_nop 0
TR 208,13,34848
TR 210,13,35360
Maav 44,188,160
EX 141
Maav 48,188,164
EX 142
Maav 52,188,168
EX 143
Maav 56,188,172
EX 144
Maav 60,192,156
EX 145
Maav 64,192,160
EX 146
Maav 68,192,164
EX 147
Maav 72,192,168
EX 148
Maav 76,192,172
WL 4
EX 149
Maav 80,196,156
s_memrealtime s[94:95]
s_waitcnt vmcnt(16) lgkmcnt(0)
s_barrier
s_sub_u32 s95,s94,s96
s_cmp_lt_i32 s95,0
s_cbranch_scc1 LC_pc66
LC_pb66:
s_add_u32 s96,s94,115
Maav 84,196,160
s_add_u32 s6,s80,122880
RA 180,11,51936
CV 176,116,117
Maav 88,196,164
s_and_b32 s6,s6,0x1e000
RA 184,11,52000
CV 177,118,119
Maav 92,196,168
s_or_b32 s6,s6,0x40000
EX 150
Maav 96,196,172
s_xor_b32 s7,s6,0x30000
EX 151
Maav 100,200,156
M0 78,107040
RA 188,11,52064
CV 180,120,121
Maav 104,200,160
DN 19,32,6,0
Maav 108,200,164
EX 152
Maav 112,200,168
EX 153
Maav 116,200,172
EX 154
Maav 120,204,156
RA 192,11,52128
CV 181,122,123
Maav 124,204,160
EX 155
Maav 128,204,164
CV 184,124,125
CV 185,126,127
Maav 132,204,168
M0 78,111264
CV 188,128,129
CV 189,130,131
Maav 136,204,172
DN 33,32,6,0
Maav 140,208,156
RA 196,11,52448
CV 192,132,133
Maav 144,208,160
CV 193,134,135
CV 178,136,137
Maav 148,208,164
CV 179,138,139
CV 182,140,141
Maav 152,208,168
CV 183,142,143
CV 186,144,145
Maav 156,208,172
CV 187,146,147
CV 190,148,149
Maav 160,212,156
CV 191,150,151
CV 194,152,153
Maav 164,212,160
M0 78,115488
CV 195,154,155
Maav 168,212,164
DN 19,32,7,0
Maav 172,212,168
Maav 176,212,172
WL 3
Mvavc 116,180,36,196
RA 200,11,52512
Mvav 116,184,40
Mvavc 120,180,52,200
Mvav 120,184,56
Mvavc 124,180,68,204
M0 78,119712
Mvav 124,184,72
RA 204,11,52576
Mvavc 128,180,84,208
DN 33,32,7,0
Mvav 128,184,88
Mvavc 132,180,100,212
Mvav 132,184,104
WL 3
Mvav 116,188,44
RA 208,11,52640
Mvav 116,192,48
Mvav 120,188,60
Mvav 120,192,64
M0 78,124352
Mvav 124,188,76
DN 19,36,4,0
Mvav 124,192,80
s_nop 0
TR 180,12,0
TR 182,12,512
Mvav 128,188,92
EX 116
Mvav 128,192,96
EX 117
Mvav 132,188,108
EX 118
Mvav 132,192,112
WL 4
EX 119
Mvavc 136,196,36,196
s_nop 0
TR 184,12,32
TR 186,12,544
Mvav 136,200,40
M0 78,128576
EX 120
Mvavc 140,196,52,200
DN 33,36,4,0
Mvav 140,200,56
EX 121
Mvavc 144,196,68,204
EX 122
Mvav 144,200,72
s_nop 0
TR 188,12,64
TR 190,12,576
Mvavc 148,196,84,208
s_add_u32 s98,s71,106496
v_lshl_add_u32 v8,v2,1,s98
CV 156,116,117
Mvav 148,200,88
buffer_load_dword v8,v8,s[60:63],0 offen
Mvavc 152,196,100,212
EX 123
Mvav 152,200,104
WL 6
EX 124
Mvav 136,204,44
M0 78,132800
TR 192,12,96
TR 194,12,608
Mvav 136,208,48
DN 19,36,5,0
Mvav 140,204,60
EX 125
Mvav 140,208,64
EX 126
Mvav 144,204,76
EX 127
Mvav 144,208,80
s_nop 0
TR 196,12,128
TR 198,12,640
Mvav 148,204,92
EX 128
Mvav 148,208,96
EX 129
Mvav 152,204,108
M0 78,137024
EX 130
Mvav 152,208,112
WL 6
DN 33,36,5,0
Maav 0,180,176
s_nop 0
TR 200,12,160
TR 202,12,672
Maav 4,180,180
EX 131
Maav 8,180,184
EX 132
Maav 12,180,188
EX 133
Maav 16,180,192
EX 134
Maav 20,184,176
s_nop 0
TR 204,12,192
TR 206,12,704
Maav 24,184,180
EX 135
Maav 28,184,184
EX 136
Maav 32,184,188
EX 137
Maav 36,184,192
WL 6
EX 138
Maav 40,188,176
s_nop 0
TR 208,12,224
TR 210,12,736
Maav 44,188,180
EX 139
Maav 48,188,184
EX 140
Maav 52,188,188
EX 141
Maav 56,188,192
EX 142
Maav 60,192,176
EX 143
Maav 64,192,180
EX 144
Maav 68,192,184
EX 145
Maav 72,192,188
EX 146
Maav 76,192,192
WL 4
EX 147
Maav 80,196,176
s_memrealtime s[94:95]
s_waitcnt vmcnt(17) lgkmcnt(0)
s_barrier
s_sub_u32 s95,s94,s96
s_cmp_lt_i32 s95,0
s_cbranch_scc1 LC_pc67
LC_pb67:
s_add_u32 s96,s94,115
Maav 84,196,180
M0 78,141664
RA 180,10,17312
CV 157,118,119
Maav 88,196,184
RA 184,10,17376
CV 160,120,121
Maav 92,196,188
DN 19,36,6,0
Maav 96,196,192
EX 148
Maav 100,200,176
RA 188,10,17440
CV 161,122,123
Maav 104,200,180
EX 149
Maav 108,200,184
EX 150
Maav 112,200,188
EX 151
Maav 116,200,192
EX 152
Maav 120,204,176
M0 78,145888
RA 192,10,17504
CV 164,124,125
Maav 124,204,180
DN 33,36,6,0
Maav 128,204,184
EX 153
Maav 132,204,188
EX 154
Maav 136,204,192
EX 155
Maav 140,208,176
RA 196,10,17824
CV 165,126,127
Maav 144,208,180
CV 168,128,129
CV 169,130,131
Maav 148,208,184
CV 172,132,133
CV 173,134,135
Maav 152,208,188
M0 78,150112
CV 158,136,137
CV 159,138,139
Maav 156,208,192
DN 19,36,7,0
Maav 160,212,176
CV 162,140,141
CV 163,142,143
Maav 164,212,180
CV 166,144,145
CV 167,146,147
Maav 168,212,184
CV 170,148,149
CV 171,150,151
Maav 172,212,188
CV 175,154,155
Maav 176,212,192
WL 3
Mvavc 116,180,36,196
RA 200,10,17888
CV 174,152,153
Mvav 116,184,40
M0 78,154336
Mvavc 120,180,52,200
DN 33,36,7,0
Mvav 120,184,56
Mvavc 124,180,68,204
Mvav 124,184,72
RA 204,10,17952
Mvavc 128,180,84,208
Mvav 128,184,88
Mvavc 132,180,100,212
Mvav 132,184,104
WL 3
Mvav 116,188,44
RA 208,10,18016
Mvav 116,192,48
Mvav 120,188,60
Mvav 120,192,64
Mvav 124,188,76
Mvav 124,192,80
s_nop 0
TR 180,12,34624
TR 182,12,35136
Mvav 128,188,92
EX 116
Mvav 128,192,96
EX 117
Mvav 132,188,108
EX 118
Mvav 132,192,112
WL 4
EX 119
Mvavc 136,196,36,196
s_nop 0
TR 184,12,34656
TR 186,12,35168
Mvav 136,200,40
EX 120
Mvavc 140,196,52,200
EX 121
Mvav 140,200,56
EX 122
Mvavc 144,196,68,204
EX 123
Mvav 144,200,72
s_nop 0
TR 188,12,34688
TR 190,12,35200
Mvavc 148,196,84,208
EX 124
Mvav 148,200,88
EX 125
Mvavc 152,196,100,212
EX 126
Mvav 152,200,104
WL 6
EX 127
Mvav 136,204,44
s_nop 0
TR 192,12,34720
TR 194,12,35232
Mvav 136,208,48
EX 128
Mvav 140,204,60
EX 129
Mvav 140,208,64
EX 130
Mvav 144,204,76
EX 131
Mvav 144,208,80
s_nop 0
TR 196,12,34752
TR 198,12,35264
Mvav 148,204,92
EX 132
Mvav 148,208,96
EX 133
Mvav 152,204,108
EX 134
Mvav 152,208,112
WL 6
EX 135
Maav 0,180,156
s_nop 0
TR 200,12,34784
TR 202,12,35296
Maav 4,180,160
EX 136
Maav 8,180,164
EX 137
Maav 12,180,168
EX 138
Maav 16,180,172
EX 139
Maav 20,184,156
s_nop 0
TR 204,12,34816
TR 206,12,35328
Maav 24,184,160
EX 140
Maav 28,184,164
EX 141
Maav 32,184,168
EX 142
Maav 36,184,172
WL 6
EX 143
Maav 40,188,156
s_nop 0
TR 208,12,34848
TR 210,12,35360
Maav 44,188,160
EX 144
Maav 48,188,164
EX 145
Maav 52,188,168
EX 146
Maav 56,188,172
EX 147
Maav 60,192,156
EX 148
Maav 64,192,160
EX 149
Maav 68,192,164
EX 150
Maav 72,192,168
EX 151
Maav 76,192,172
WL 4
EX 152
Maav 80,196,156
s_waitcnt vmcnt(13) lgkmcnt(0)
s_barrier
Maav 84,196,160
RA 180,10,51936
CV 176,116,117
Maav 88,196,164
RA 184,10,52000
CV 177,118,119
Maav 92,196,168
EX 153
Maav 96,196,172
EX 154
Maav 100,200,156
RA 188,10,52064
CV 180,120,121
Maav 104,200,160
EX 155
Maav 108,200,164
CV 181,122,123
CV 184,124,125
Maav 112,200,168
CV 185,126,127
CV 188,128,129
Maav 116,200,172
CV 189,130,131
CV 192,132,133
Maav 120,204,156
RA 192,10,52128
CV 193,134,135
Maav 124,204,160
CV 178,136,137
CV 179,138,139
Maav 128,204,164
CV 182,140,141
CV 183,142,143
Maav 132,204,168
CV 186,144,145
CV 187,146,147
Maav 136,204,172
CV 190,148,149
CV 191,150,151
Maav 140,208,156
RA 196,10,52448
CV 194,152,153
Maav 144,208,160
CV 195,154,155
Maav 148,208,164
Maav 152,208,168
Maav 156,208,172
Maav 160,212,156
Maav 164,212,160
Maav 168,212,164
Maav 172,212,168
Maav 176,212,172
WL 3
Mvavc 116,180,36,196
RA 200,10,52512
Mvav 116,184,40
s_lshr_b32 s91,s22,5
s_mul_i32 s91,s91,73728
s_add_u32 s91,s91,0
s_add_u32 s44,s40,s91
s_addc_u32 s45,s41,0
s_mov_b32 s46,73728
s_mov_b32 s47,0x20000
Mvavc 120,180,52,200
s_lshl_b32 s91,s64,12
v_lshl_add_u32 v28,v2,2,s91
Mvav 120,184,56
buffer_load_dword v29,v28,s[44:47],0 offen
Mvavc 124,180,68,204
v_add_u32 v28,16384,v28
Mvav 124,184,72
RA 204,10,52576
Mvavc 128,180,84,208
Mvav 128,184,88
Mvavc 132,180,100,212
Mvav 132,184,104
WL 3
Mvav 116,188,44
RA 208,10,52640
Mvav 116,192,48
buffer_load_dword v29,v28,s[44:47],0 offen
Mvav 120,188,60
v_add_u32 v28,16384,v28
Mvav 120,192,64
Mvav 124,188,76
Mvav 124,192,80
s_nop 0
TR 180,13,0
TR 182,13,512
Mvav 128,188,92
EX 116
Mvav 128,192,96
EX 117
Mvav 132,188,108
EX 118
Mvav 132,192,112
WL 4
buffer_load_dword v29,v28,s[44:47],0 offen
Mvavc 136,196,36,196
s_nop 0
TR 184,13,32
TR 186,13,544
Mvav 136,200,40
v_add_u32 v28,16384,v28
CV 156,116,117
Mvavc 140,196,52,200
EX 119
Mvav 140,200,56
EX 120
Mvavc 144,196,68,204
EX 121
Mvav 144,200,72
s_nop 0
TR 188,13,64
TR 190,13,576
Mvavc 148,196,84,208
EX 122
Mvav 148,200,88
buffer_load_dword v29,v28,s[44:47],0 offen
Mvavc 152,196,100,212
v_add_u32 v28,16384,v28
CV 157,118,119
Mvav 152,200,104
WL 6
EX 123
Mvav 136,204,44
s_nop 0
TR 192,13,96
TR 194,13,608
Mvav 136,208,48
EX 124
Mvav 140,204,60
EX 125
Mvav 140,208,64
EX 126
Mvav 144,204,76
EX 127
Mvav 144,208,80
s_nop 0
TR 196,13,128
TR 198,13,640
Mvav 148,204,92
buffer_load_dword v29,v28,s[44:47],0 offen
Mvav 148,208,96
v_add_u32 v28,16384,v28
CV 160,120,121
Mvav 152,204,108
EX 128
Mvav 152,208,112
WL 6
EX 129
Maav 0,180,176
s_nop 0
TR 200,13,160
TR 202,13,672
Maav 4,180,180
EX 130
Maav 8,180,184
EX 131
Maav 12,180,188
EX 132
Maav 16,180,192
EX 133
Maav 20,184,176
s_nop 0
TR 204,13,192
TR 206,13,704
Maav 24,184,180
EX 134
Maav 28,184,184
EX 135
Maav 32,184,188
EX 136
Maav 36,184,192
WL 6
EX 137
Maav 40,188,176
s_nop 0
TR 208,13,224
TR 210,13,736
Maav 44,188,180
EX 138
Maav 48,188,184
EX 139
Maav 52,188,188
EX 140
Maav 56,188,192
EX 141
Maav 60,192,176
EX 142
Maav 64,192,180
EX 143
Maav 68,192,184
EX 144
Maav 72,192,188
EX 145
Maav 76,192,192
WL 4
EX 146
Maav 80,196,176
s_waitcnt vmcnt(9) lgkmcnt(0)
s_barrier
Maav 84,196,180
RA 180,11,17312
CV 161,122,123
Maav 88,196,184
RA 184,11,17376
CV 164,124,125
Maav 92,196,188
s_mul_i32 s90,s64,20480
v_add_u32 v34,s90,v2
Maav 96,196,192
s_xor_b32 s90,s64,2
s_mul_i32 s90,s90,20480
v_add_u32 v35,s90,v2
Maav 100,200,176
RA 188,11,17440
CV 165,126,127
Maav 104,200,180
EX 147
Maav 108,200,184
ds_write_b128 v34,v[216:219] offset:0
Maav 112,200,188
EX 148
Maav 116,200,192
EX 149
Maav 120,204,176
RA 192,11,17504
CV 168,128,129
Maav 124,204,180
ds_write_b128 v34,v[220:223] offset:1024
Maav 128,204,184
EX 150
Maav 132,204,188
EX 151
Maav 136,204,192
ds_write_b128 v34,v[224:227] offset:2048
Maav 140,208,176
RA 196,11,17824
CV 169,130,131
Maav 144,208,180
EX 152
Maav 148,208,184
ds_write_b128 v34,v[228:231] offset:3072
Maav 152,208,188
EX 153
Maav 156,208,192
EX 154
Maav 160,212,176
ds_write_b128 v34,v[232:235] offset:4096
Maav 164,212,180
EX 155
Maav 168,212,184
CV 172,132,133
CV 173,134,135
Maav 172,212,188
ds_write_b128 v34,v[236:239] offset:5120
Maav 176,212,192
WL 9
CV 158,136,137
CV 159,138,139
Mvavc 116,180,36,196
RA 200,11,17888
CV 162,140,141
Mvav 116,184,40
ds_write_b128 v34,v[240:243] offset:6144
Mvavc 120,180,52,200
CV 163,142,143
CV 166,144,145
Mvav 120,184,56
CV 167,146,147
CV 170,148,149
Mvavc 124,180,68,204
ds_write_b128 v34,v[244:247] offset:7168
Mvav 124,184,72
RA 204,11,17952
CV 171,150,151
Mvavc 128,180,84,208
CV 174,152,153
CV 175,154,155
Mvav 128,184,88
ds_write_b128 v34,v[248:251] offset:8192
Mvavc 132,180,100,212
Mvav 132,184,104
WL 11
Mvav 116,188,44
RA 208,11,18016
Mvav 116,192,48
ds_write_b128 v34,v[252:255] offset:9216
Mvav 120,188,60
Mvav 120,192,64
Mvav 124,188,76
ds_write_b128 v34,a[216:219] offset:10240
Mvav 124,192,80
s_nop 0
TR 180,13,34624
TR 182,13,35136
Mvav 128,188,92
EX 116
Mvav 128,192,96
ds_write_b128 v34,a[220:223] offset:11264
Mvav 132,188,108
EX 117
Mvav 132,192,112
WL 10
EX 118
Mvavc 136,196,36,196
s_nop 0
TR 184,13,34656
TR 186,13,35168
Mvav 136,200,40
ds_write_b128 v34,a[224:227] offset:12288
Mvavc 140,196,52,200
EX 119
Mvav 140,200,56
EX 120
Mvavc 144,196,68,204
ds_write_b128 v34,a[228:231] offset:13312
Mvav 144,200,72
s_nop 0
TR 188,13,34688
TR 190,13,35200
Mvavc 148,196,84,208
EX 121
Mvav 148,200,88
ds_write_b128 v34,a[232:235] offset:14336
Mvavc 152,196,100,212
EX 122
Mvav 152,200,104
WL 12
EX 123
Mvav 136,204,44
s_nop 0
TR 192,13,34720
TR 194,13,35232
Mvav 136,208,48
ds_write_b128 v34,a[236:239] offset:15360
Mvav 140,204,60
EX 124
Mvav 140,208,64
EX 125
Mvav 144,204,76
ds_write_b128 v34,a[240:243] offset:16384
Mvav 144,208,80
s_nop 0
TR 196,13,34752
TR 198,13,35264
Mvav 148,204,92
EX 126
Mvav 148,208,96
ds_write_b128 v34,a[244:247] offset:17408
Mvav 152,204,108
EX 127
Mvav 152,208,112
WL 12
EX 128
Maav 0,180,156
s_nop 0
TR 200,13,34784
TR 202,13,35296
Maav 4,180,160
ds_write_b128 v34,a[248:251] offset:18432
Maav 8,180,164
EX 129
Maav 12,180,168
EX 130
CV 188,128,129
Maav 16,180,172
ds_write_b128 v34,a[252:255] offset:19456
Maav 20,184,156
s_nop 0
TR 204,13,34816
TR 206,13,35328
Maav 24,184,160
EX 131
Maav 28,184,164
EX 132
CV 189,130,131
Maav 32,184,168
EX 133
Maav 36,184,172
WL 11
EX 134
CV 192,132,133
Maav 40,188,156
s_nop 0
TR 208,13,34848
TR 210,13,35360
Maav 44,188,160
EX 135
Maav 48,188,164
EX 136
CV 193,134,135
Maav 52,188,168
EX 137
Maav 56,188,172
EX 138
CV 178,136,137
Maav 60,192,156
EX 139
Maav 64,192,160
EX 140
CV 179,138,139
Maav 68,192,164
EX 141
Maav 72,192,168
EX 142
CV 182,140,141
Maav 76,192,172
WL 6
EX 143
Maav 80,196,156
s_waitcnt vmcnt(5) lgkmcnt(0)
s_barrier
CV 183,142,143
Maav 84,196,160
s_nop 0
TR 180,13,52064
TR 182,13,52576
Maav 88,196,164
s_nop 0
TR 184,13,52096
TR 186,13,52608
Maav 92,196,168
ds_read_b128 v[36:39],v35 offset:0
EX 144
Maav 96,196,172
ds_read_b128 v[40:43],v35 offset:1024
EX 145
Maav 100,200,156
s_nop 0
TR 188,13,52128
TR 190,13,52640
CV 186,144,145
Maav 104,200,160
ds_read_b128 v[44:47],v35 offset:2048
EX 146
Maav 108,200,164
ds_read_b128 v[48:51],v35 offset:3072
EX 147
Maav 112,200,168
ds_read_b128 v[52:55],v35 offset:4096
EX 148
Maav 116,200,172
ds_read_b128 v[56:59],v35 offset:5120
EX 149
Maav 120,204,156
s_nop 0
TR 192,13,52160
TR 194,13,52672
CV 187,146,147
Maav 124,204,160
ds_read_b128 v[60:63],v35 offset:6144
EX 150
Maav 128,204,164
ds_read_b128 v[64:67],v35 offset:7168
EX 151
Maav 132,204,168
ds_read_b128 v[68:71],v35 offset:8192
EX 152
Maav 136,204,172
ds_read_b128 v[72:75],v35 offset:9216
EX 153
Maav 140,208,156
s_nop 0
TR 196,13,51936
TR 198,13,52448
CV 190,148,149
Maav 144,208,160
ds_read_b128 v[76:79],v35 offset:10240
EX 154
Maav 148,208,164
ds_read_b128 v[80:83],v35 offset:11264
EX 155
Maav 152,208,168
ds_read_b128 v[84:87],v35 offset:12288
CV 176,116,117
CV 191,150,151
Maav 156,208,172
ds_read_b128 v[88:91],v35 offset:13312
CV 177,118,119
CV 194,152,153
Maav 160,212,156
ds_read_b128 v[92:95],v35 offset:14336
CV 180,120,121
CV 195,154,155
Maav 164,212,160
ds_read_b128 v[96:99],v35 offset:15360
CV 181,122,123
Maav 168,212,164
ds_read_b128 v[100:103],v35 offset:16384
CV 184,124,125
Maav 172,212,168
ds_read_b128 v[104:107],v35 offset:17408
CV 185,126,127
Maav 176,212,172
ds_read_b128 v[108:111],v35 offset:18432
Maav 160,212,176
ds_read_b128 v[112:115],v35 offset:19456
Maav 164,212,180
Maav 168,212,184
Maav 172,212,188
Maav 176,212,192
WL 15
Maav 80,180,176
s_nop 0
TR 200,13,51968
TR 202,13,52480
Maav 84,180,180
Maav 88,180,184
Maav 92,180,188
AR 28,160
s_waitcnt vmcnt(0)
AR 29,164
Maav 96,180,192
AR 30,168
AR 31,172
Maav 100,184,176
s_nop 0
TR 204,13,52000
TR 206,13,52512
Maav 104,184,180
AR 32,176
Maav 108,184,184
v_cmp_nle_f32 vcc,v28,s75
s_nop 0
s_or_b64 s[86:87],s[86:87],vcc
Maav 112,184,188
v_cmp_nle_f32 vcc,v29,s75
s_nop 0
s_or_b64 s[86:87],s[86:87],vcc
Maav 116,184,192
v_cmp_nle_f32 vcc,v30,s75
s_nop 0
s_or_b64 s[86:87],s[86:87],vcc
Maav 120,188,176
s_nop 0
TR 208,13,52032
TR 210,13,52544
Maav 124,188,180
v_cmp_nle_f32 vcc,v31,s75
s_nop 0
s_or_b64 s[86:87],s[86:87],vcc
Maav 128,188,184
v_cmp_nle_f32 vcc,v32,s75
s_nop 0
s_or_b64 s[86:87],s[86:87],vcc
Maav 132,188,188
v_rcp_f32 v28,v28
Maav 136,188,192
v_rcp_f32 v29,v29
Maav 140,192,176
v_rcp_f32 v30,v30
Maav 144,192,180
v_rcp_f32 v31,v31
Maav 148,192,184
v_rcp_f32 v32,v32
Maav 152,192,188
AR 116,80
AR 117,81
Maav 156,192,192
WL 4
AR 118,82
AR 119,83
Maav 0,196,176
WL 0
s_barrier
Maav 4,196,180
s_mov_b32 s70,1024
s_add_u32 s98,s71,114688
v_lshl_add_u32 v8,v2,1,s98
Maav 8,196,184
s_add_u32 s83,s70,0
AR 120,100
AR 121,101
Maav 12,196,188
M0 82,20480
AR 122,102
AR 123,103
Maav 16,196,192
DM 7,40,83,0
Maav 20,200,176
M0 82,20384
v_mul_f32 v116,v116,v28
v_mul_f32 v117,v117,v28
Maav 24,200,180
DM 7,40,83,1152
Maav 28,200,184
M0 82,20288
v_mul_f32 v118,v118,v28
v_mul_f32 v119,v119,v28
Maav 32,200,188
DM 7,40,83,2304
Maav 36,200,192
M0 82,20192
v_mul_f32 v120,v120,v28
Maav 40,204,176
DM 7,40,83,3456
Maav 44,204,180
v_mul_f32 v121,v121,v28
v_mul_f32 v122,v122,v28
Maav 48,204,184
v_mul_f32 v123,v123,v28
CV 196,116,117
Maav 52,204,188
buffer_load_dword v8,v8,s[60:63],0 offen
Maav 56,204,192
CV 197,118,119
CV 198,120,121
Maav 60,208,176
CV 199,122,123
Maav 64,208,180
Maav 68,208,184
Maav 72,208,188
Maav 76,208,192
s_add_u32 s83,s70,147456
M0 82,37792
AR 124,84
AR 125,85
s_nop 1
DM 7,40,83,0
AR 126,86
AR 127,87
AR 128,104
AR 129,105
AR 130,106
AR 131,107
v_pk_mul_f32 v[124:125],v[124:125],v[28:29] op_sel:[0,1] op_sel_hi:[1,1]
v_pk_mul_f32 v[126:127],v[126:127],v[28:29] op_sel:[0,1] op_sel_hi:[1,1]
v_pk_mul_f32 v[128:129],v[128:129],v[28:29] op_sel:[0,1] op_sel_hi:[1,1]
v_pk_mul_f32 v[130:131],v[130:131],v[28:29] op_sel:[0,1] op_sel_hi:[1,1]
CV 200,124,125
CV 201,126,127
CV 202,128,129
CV 203,130,131
M0 82,37696
AR 132,88
AR 133,89
s_nop 1
DM 7,40,83,1152
AR 134,90
AR 135,91
AR 136,108
AR 137,109
AR 138,110
AR 139,111
v_pk_mul_f32 v[132:133],v[132:133],v[30:31] op_sel_hi:[1,0]
v_pk_mul_f32 v[134:135],v[134:135],v[30:31] op_sel_hi:[1,0]
v_pk_mul_f32 v[136:137],v[136:137],v[30:31] op_sel_hi:[1,0]
v_pk_mul_f32 v[138:139],v[138:139],v[30:31] op_sel_hi:[1,0]
CV 204,132,133
CV 205,134,135
CV 206,136,137
CV 207,138,139
M0 82,37600
AR 140,92
AR 141,93
s_nop 1
DM 7,40,83,2304
AR 142,94
AR 143,95
AR 144,112
AR 145,113
AR 146,114
AR 147,115
v_pk_mul_f32 v[140:141],v[140:141],v[30:31] op_sel:[0,1] op_sel_hi:[1,1]
v_pk_mul_f32 v[142:143],v[142:143],v[30:31] op_sel:[0,1] op_sel_hi:[1,1]
v_pk_mul_f32 v[144:145],v[144:145],v[30:31] op_sel:[0,1] op_sel_hi:[1,1]
v_pk_mul_f32 v[146:147],v[146:147],v[30:31] op_sel:[0,1] op_sel_hi:[1,1]
CV 208,140,141
CV 209,142,143
CV 210,144,145
CV 211,146,147
M0 82,37504
AR 148,96
AR 149,97
s_nop 1
DM 7,40,83,3456
AR 150,98
AR 151,99
AR 152,116
AR 153,117
AR 154,118
AR 155,119
v_pk_mul_f32 v[148:149],v[148:149],v[32:33] op_sel_hi:[1,0]
v_pk_mul_f32 v[150:151],v[150:151],v[32:33] op_sel_hi:[1,0]
v_pk_mul_f32 v[152:153],v[152:153],v[32:33] op_sel_hi:[1,0]
v_pk_mul_f32 v[154:155],v[154:155],v[32:33] op_sel_hi:[1,0]
CV 212,148,149
CV 213,150,151
CV 214,152,153
CV 215,154,155
s_add_u32 s70,s68,1
s_and_b32 s70,s70,7
s_lshl_b32 s70,s70,7
s_add_u32 s83,s70,0
M0 82,55104
AR 116,120
AR 117,121
s_nop 1
DM 7,40,83,0
AR 118,122
AR 119,123
AR 120,140
AR 121,141
AR 122,142
AR 123,143
v_pk_mul_f32 v[116:117],v[116:117],v[28:29] op_sel_hi:[1,0]
v_pk_mul_f32 v[118:119],v[118:119],v[28:29] op_sel_hi:[1,0]
v_pk_mul_f32 v[120:121],v[120:121],v[28:29] op_sel_hi:[1,0]
v_pk_mul_f32 v[122:123],v[122:123],v[28:29] op_sel_hi:[1,0]
CV 156,116,117
CV 157,118,119
CV 158,120,121
CV 159,122,123
M0 82,55008
AR 124,124
AR 125,125
s_nop 1
DM 7,40,83,1152
AR 126,126
AR 127,127
AR 128,144
AR 129,145
AR 130,146
AR 131,147
v_pk_mul_f32 v[124:125],v[124:125],v[28:29] op_sel:[0,1] op_sel_hi:[1,1]
v_pk_mul_f32 v[126:127],v[126:127],v[28:29] op_sel:[0,1] op_sel_hi:[1,1]
v_pk_mul_f32 v[128:129],v[128:129],v[28:29] op_sel:[0,1] op_sel_hi:[1,1]
v_pk_mul_f32 v[130:131],v[130:131],v[28:29] op_sel:[0,1] op_sel_hi:[1,1]
CV 160,124,125
CV 161,126,127
CV 162,128,129
CV 163,130,131
M0 82,54912
AR 132,128
AR 133,129
s_nop 1
DM 7,40,83,2304
AR 134,130
AR 135,131
AR 136,148
AR 137,149
AR 138,150
AR 139,151
v_pk_mul_f32 v[132:133],v[132:133],v[30:31] op_sel_hi:[1,0]
v_pk_mul_f32 v[134:135],v[134:135],v[30:31] op_sel_hi:[1,0]
v_pk_mul_f32 v[136:137],v[136:137],v[30:31] op_sel_hi:[1,0]
v_pk_mul_f32 v[138:139],v[138:139],v[30:31] op_sel_hi:[1,0]
CV 164,132,133
CV 165,134,135
CV 166,136,137
CV 167,138,139
M0 82,54816
AR 140,132
AR 141,133
s_nop 1
DM 7,40,83,3456
AR 142,134
AR 143,135
AR 144,152
AR 145,153
AR 146,154
AR 147,155
v_pk_mul_f32 v[140:141],v[140:141],v[30:31] op_sel:[0,1] op_sel_hi:[1,1]
v_pk_mul_f32 v[142:143],v[142:143],v[30:31] op_sel:[0,1] op_sel_hi:[1,1]
v_pk_mul_f32 v[144:145],v[144:145],v[30:31] op_sel:[0,1] op_sel_hi:[1,1]
v_pk_mul_f32 v[146:147],v[146:147],v[30:31] op_sel:[0,1] op_sel_hi:[1,1]
CV 168,140,141
CV 169,142,143
CV 170,144,145
CV 171,146,147
s_add_u32 s83,s70,147456
M0 82,72416
AR 148,136
AR 149,137
s_nop 1
DM 7,40,83,0
AR 150,138
AR 151,139
AR 152,156
AR 153,157
AR 154,158
AR 155,159
v_pk_mul_f32 v[148:149],v[148:149],v[32:33] op_sel_hi:[1,0]
v_pk_mul_f32 v[150:151],v[150:151],v[32:33] op_sel_hi:[1,0]
v_pk_mul_f32 v[152:153],v[152:153],v[32:33] op_sel_hi:[1,0]
v_pk_mul_f32 v[154:155],v[154:155],v[32:33] op_sel_hi:[1,0]
CV 172,148,149
CV 173,150,151
CV 174,152,153
CV 175,154,155
M0 82,72320
s_nop 0
DM 7,40,83,1152
M0 82,72224
s_nop 0
DM 7,40,83,2304
M0 82,72128
s_nop 0
DM 7,40,83,3456
s_waitcnt vmcnt(8) lgkmcnt(0)
s_barrier
TR 180,3,0
TR 182,3,512
TR 184,3,4224
TR 186,3,4736
TR 188,3,32
TR 190,3,544
TR 192,3,4256
TR 194,3,4768
TR 196,3,8448
TR 198,3,8960
WL 8
Maavz 80,180,216
s_nop 0
TR 200,3,12672
TR 202,3,13184
WL 8
Maav 80,184,236
s_mul_i32 s90,s64,20480
s_add_u32 s90,s90,69248
s_cmp_eq_u32 s64,0
s_cselect_b32 s90,0,s90
v_add_u32 v34,s90,v2
Maavz 84,180,220
s_xor_b32 s91,s64,2
s_mul_i32 s90,s91,20480
s_add_u32 s90,s90,69248
s_cmp_eq_u32 s91,0
s_cselect_b32 s90,0,s90
v_add_u32 v35,s90,v2
Maav 84,184,240
AR 176,0
AR 177,1
Maavz 88,180,224
AR 178,2
AR 179,3
Maav 88,184,244
s_nop 0
TR 204,3,8480
TR 206,3,8992
Maavz 92,180,228
AR 180,20
AR 181,21
Maav 92,184,248
AR 182,22
AR 183,23
Maavz 96,180,232
v_mul_f32 v176,v176,v28
v_mul_f32 v177,v177,v28
Maav 96,184,252
WL 6
v_mul_f32 v178,v178,v28
v_mul_f32 v179,v179,v28
Maavz 100,188,216
s_nop 0
TR 208,3,12704
TR 210,3,13216
Maav 100,192,236
v_mul_f32 v180,v180,v28
v_mul_f32 v181,v181,v28
Maavz 104,188,220
v_mul_f32 v182,v182,v28
v_mul_f32 v183,v183,v28
Maav 104,192,240
CV 116,176,177
CV 117,178,179
Maavz 108,188,224
CV 118,180,181
CV 119,182,183
Maav 108,192,244
s_nop 0
TR 180,5,0
TR 182,5,512
Maavz 112,188,228
ds_write_b128 v34,v[196:199] offset:10240
Maav 112,192,248
ds_write_b128 v34,v[116:119] offset:0
Maavz 116,188,232
AR 184,4
AR 185,5
Maav 116,192,252
WL 8
AR 186,6
AR 187,7
Maaa 80,196,216
s_nop 0
TR 184,5,4224
TR 186,5,4736
Maaa 80,200,236
AR 188,24
AR 189,25
Maaa 84,196,220
AR 190,26
AR 191,27
Maaa 84,200,240
v_mul_f32 v184,v184,v29
v_mul_f32 v185,v185,v29
Maaa 88,196,224
v_mul_f32 v186,v186,v29
v_mul_f32 v187,v187,v29
Maaa 88,200,244
s_nop 0
TR 188,5,32
TR 190,5,544
Maaa 92,196,228
v_mul_f32 v188,v188,v29
v_mul_f32 v189,v189,v29
Maaa 92,200,248
v_mul_f32 v190,v190,v29
v_mul_f32 v191,v191,v29
Maaa 96,196,232
CV 120,184,185
CV 121,186,187
Maaa 96,200,252
WL 8
CV 122,188,189
CV 123,190,191
Maaa 100,204,216
s_nop 0
TR 192,5,4256
TR 194,5,4768
Maaa 100,208,236
ds_write_b128 v34,v[200:203] offset:11264
Maaa 104,204,220
ds_write_b128 v34,v[120:123] offset:1024
Maaa 104,208,240
AR 176,8
AR 177,9
Maaa 108,204,224
AR 178,10
AR 179,11
Maaa 108,208,244
s_nop 0
TR 196,5,8448
TR 198,5,8960
Maaa 112,204,228
AR 180,28
AR 181,29
Maaa 112,208,248
AR 182,30
AR 183,31
Maaa 116,204,232
v_mul_f32 v176,v176,v30
v_mul_f32 v177,v177,v30
Maaa 116,208,252
WL 8
v_mul_f32 v178,v178,v30
v_mul_f32 v179,v179,v30
Maav 80,180,36
s_nop 0
TR 200,5,12672
TR 202,5,13184
Maav 80,184,56
v_mul_f32 v180,v180,v30
v_mul_f32 v181,v181,v30
Maav 84,180,40
v_mul_f32 v182,v182,v30
v_mul_f32 v183,v183,v30
Maav 84,184,60
CV 124,176,177
CV 125,178,179
Maav 88,180,44
CV 126,180,181
CV 127,182,183
Maav 88,184,64
s_nop 0
TR 204,5,8480
TR 206,5,8992
Maav 92,180,48
ds_write_b128 v34,v[204:207] offset:12288
Maav 92,184,68
ds_write_b128 v34,v[124:127] offset:2048
Maav 96,180,52
AR 184,12
AR 185,13
Maav 96,184,72
WL 10
AR 186,14
AR 187,15
Maav 100,188,36
s_nop 0
TR 208,5,12704
TR 210,5,13216
Maav 100,192,56
AR 188,32
AR 189,33
Maav 104,188,40
AR 190,34
AR 191,35
Maav 104,192,60
v_mul_f32 v184,v184,v31
v_mul_f32 v185,v185,v31
Maav 108,188,44
v_mul_f32 v186,v186,v31
v_mul_f32 v187,v187,v31
Maav 108,192,64
v_mul_f32 v188,v188,v31
v_mul_f32 v189,v189,v31
Maav 112,188,48
v_mul_f32 v190,v190,v31
v_mul_f32 v191,v191,v31
Maav 112,192,68
CV 128,184,185
CV 129,186,187
Maav 116,188,52
CV 130,188,189
CV 131,190,191
Maav 116,192,72
WL 6
ds_write_b128 v34,v[208:211] offset:13312
Maav 80,196,76
s_waitcnt vmcnt(0) lgkmcnt(0)
s_barrier
Maav 80,200,96
s_mov_b32 s70,1024
TR 180,3,34624
TR 182,3,35136
Maav 84,196,80
s_add_u32 s83,s70,294912
TR 184,3,38848
TR 186,3,39360
Maav 84,200,100
M0 82,20480
ds_write_b128 v34,v[128:131] offset:3072
Maav 88,196,84
DM 7,40,83,0
Maav 88,200,104
M0 82,20384
TR 188,3,34656
TR 190,3,35168
Maav 92,196,88
DM 7,40,83,1152
Maav 92,200,108
M0 82,20288
AR 176,16
AR 177,17
Maav 96,196,92
DM 7,40,83,2304
Maav 96,200,112
AR 178,18
AR 179,19
Maav 100,204,76
M0 82,20192
TR 192,3,38880
TR 194,3,39392
Maav 100,208,96
DM 7,40,83,3456
Maav 104,204,80
s_add_u32 s83,s70,442368
AR 180,36
AR 181,37
Maav 104,208,100
M0 82,37792
AR 182,38
AR 183,39
Maav 108,204,84
DM 7,40,83,0
Maav 108,208,104
M0 82,37696
TR 196,3,43072
TR 198,3,43584
Maav 112,204,88
DM 7,40,83,1152
Maav 112,208,108
M0 82,37600
v_mul_f32 v176,v176,v32
v_mul_f32 v177,v177,v32
Maav 116,204,92
DM 7,40,83,2304
Maav 116,208,112
WL 7
v_mul_f32 v178,v178,v32
v_mul_f32 v179,v179,v32
Maavz 120,180,216
M0 82,37504
TR 200,3,47296
TR 202,3,47808
Maav 120,184,236
DM 7,40,83,3456
Maavz 124,180,220
v_mul_f32 v180,v180,v32
v_mul_f32 v181,v181,v32
Maav 124,184,240
v_mul_f32 v182,v182,v32
v_mul_f32 v183,v183,v32
Maavz 128,180,224
CV 132,176,177
CV 133,178,179
Maav 128,184,244
s_nop 0
TR 204,3,43104
TR 206,3,43616
Maavz 132,180,228
CV 134,180,181
CV 135,182,183
Maav 132,184,248
ds_write_b128 v34,v[212:215] offset:14336
Maavz 136,180,232
ds_write_b128 v34,v[132:135] offset:4096
Maav 136,184,252
WL 8
AR 184,40
AR 185,41
Maavz 140,188,216
s_nop 0
TR 208,3,47328
TR 210,3,47840
Maav 140,192,236
AR 186,42
AR 187,43
Maavz 144,188,220
AR 188,60
AR 189,61
Maav 144,192,240
AR 190,62
AR 191,63
Maavz 148,188,224
v_mul_f32 v184,v184,v28
v_mul_f32 v185,v185,v28
Maav 148,192,244
s_nop 0
TR 180,5,34624
TR 182,5,35136
Maavz 152,188,228
v_mul_f32 v186,v186,v28
v_mul_f32 v187,v187,v28
Maav 152,192,248
v_mul_f32 v188,v188,v28
v_mul_f32 v189,v189,v28
Maavz 156,188,232
v_mul_f32 v190,v190,v28
v_mul_f32 v191,v191,v28
Maav 156,192,252
WL 8
CV 136,184,185
CV 137,186,187
Maaa 120,196,216
s_nop 0
TR 184,5,38848
TR 186,5,39360
Maaa 120,200,236
CV 138,188,189
CV 139,190,191
Maaa 124,196,220
ds_write_b128 v34,v[156:159] offset:15360
Maaa 124,200,240
ds_write_b128 v34,v[136:139] offset:5120
Maaa 128,196,224
AR 176,44
AR 177,45
Maaa 128,200,244
s_nop 0
TR 188,5,34656
TR 190,5,35168
Maaa 132,196,228
AR 178,46
AR 179,47
Maaa 132,200,248
AR 180,64
AR 181,65
Maaa 136,196,232
AR 182,66
AR 183,67
Maaa 136,200,252
WL 8
v_mul_f32 v176,v176,v29
v_mul_f32 v177,v177,v29
Maaa 140,204,216
s_nop 0
TR 192,5,38880
TR 194,5,39392
Maaa 140,208,236
v_mul_f32 v178,v178,v29
v_mul_f32 v179,v179,v29
Maaa 144,204,220
v_mul_f32 v180,v180,v29
v_mul_f32 v181,v181,v29
Maaa 144,208,240
v_mul_f32 v182,v182,v29
v_mul_f32 v183,v183,v29
Maaa 148,204,224
CV 140,176,177
CV 141,178,179
Maaa 148,208,244
s_nop 0
TR 196,5,43072
TR 198,5,43584
Maaa 152,204,228
CV 142,180,181
CV 143,182,183
Maaa 152,208,248
ds_write_b128 v34,v[160:163] offset:16384
Maaa 156,204,232
ds_write_b128 v34,v[140:143] offset:6144
Maaa 156,208,252
WL 10
AR 184,48
AR 185,49
Maav 120,180,36
s_nop 0
TR 200,5,47296
TR 202,5,47808
Maav 120,184,56
AR 186,50
AR 187,51
Maav 124,180,40
AR 188,68
AR 189,69
Maav 124,184,60
AR 190,70
AR 191,71
Maav 128,180,44
v_mul_f32 v184,v184,v30
v_mul_f32 v185,v185,v30
Maav 128,184,64
s_nop 0
TR 204,5,43104
TR 206,5,43616
Maav 132,180,48
v_mul_f32 v186,v186,v30
v_mul_f32 v187,v187,v30
Maav 132,184,68
v_mul_f32 v188,v188,v30
v_mul_f32 v189,v189,v30
Maav 136,180,52
v_mul_f32 v190,v190,v30
v_mul_f32 v191,v191,v30
Maav 136,184,72
WL 8
CV 144,184,185
CV 145,186,187
Maav 140,188,36
s_nop 0
TR 208,5,47328
TR 210,5,47840
Maav 140,192,56
CV 146,188,189
CV 147,190,191
Maav 144,188,40
ds_write_b128 v34,v[164:167] offset:17408
Maav 144,192,60
ds_write_b128 v34,v[144:147] offset:7168
Maav 148,188,44
AR 176,52
AR 177,53
Maav 148,192,64
AR 178,54
AR 179,55
Maav 152,188,48
AR 180,72
AR 181,73
AR 182,74
AR 183,75
v_mul_f32 v176,v176,v31
v_mul_f32 v177,v177,v31
v_mul_f32 v178,v178,v31
v_mul_f32 v179,v179,v31
v_mul_f32 v180,v180,v31
v_mul_f32 v181,v181,v31
v_mul_f32 v182,v182,v31
v_mul_f32 v183,v183,v31
CV 148,176,177
CV 149,178,179
CV 150,180,181
CV 151,182,183
ds_write_b128 v34,v[168:171] offset:18432
ds_write_b128 v34,v[148:151] offset:8192
AR 184,56
AR 185,57
AR 186,58
AR 187,59
AR 188,76
AR 189,77
AR 190,78
AR 191,79
v_mul_f32 v184,v184,v32
v_mul_f32 v185,v185,v32
v_mul_f32 v186,v186,v32
v_mul_f32 v187,v187,v32
v_mul_f32 v188,v188,v32
v_mul_f32 v189,v189,v32
v_mul_f32 v190,v190,v32
v_mul_f32 v191,v191,v32
CV 152,184,185
CV 153,186,187
CV 154,188,189
CV 155,190,191
ds_write_b128 v34,v[172:175] offset:19456
ds_write_b128 v34,v[152:155] offset:9216
Maav 152,192,68
s_or_b32 s90,s86,s87
v_mov_b32 v28,s90
s_lshl_b32 s91,s64,2
s_add_u32 s91,s91,162144
v_mov_b32 v29,s91
Maav 156,188,52
ds_write_b32 v29,v28 offset:0
Maav 156,192,72
WL 11
v_mov_b32 v27,162144
Maav 120,196,76
s_waitcnt vmcnt(0) lgkmcnt(0)
s_barrier
Maav 120,200,96
s_add_u32 s70,s68,1
TR 180,3,0
TR 182,3,512
Maav 124,196,80
s_and_b32 s70,s70,7
TR 184,3,4224
TR 186,3,4736
Maav 124,200,100
s_lshl_b32 s70,s70,7
ds_read_b128 v[28:31],v27 offset:0
Maav 128,196,84
s_add_u32 s83,s70,294912
RA 0,35,0
Maav 128,200,104
M0 82,55104
TR 188,3,32
TR 190,3,544
Maav 132,196,88
DM 7,40,83,0
Maav 132,200,108
M0 82,55008
RA 4,35,1024
Maav 136,196,92
DM 7,40,83,1152
Maav 136,200,112
RA 8,35,2048
Maav 140,204,76
M0 82,54912
TR 192,3,4256
TR 194,3,4768
Maav 140,208,96
DM 7,40,83,2304
Maav 144,204,80
M0 82,54816
RA 12,35,3072
Maav 144,208,100
DM 7,40,83,3456
Maav 148,204,84
s_add_u32 s83,s70,442368
RA 16,35,4096
Maav 148,208,104
M0 82,72416
TR 196,3,8448
TR 198,3,8960
Maav 152,204,88
DM 7,40,83,0
Maav 152,208,108
M0 82,72320
RA 20,35,5120
Maav 156,204,92
DM 7,40,83,1152
Maav 156,208,112
WL 13
RA 24,35,6144
Maav 80,180,116
M0 82,72224
TR 200,3,12672
TR 202,3,13184
Maav 80,184,136
DM 7,40,83,2304
Maav 84,180,120
M0 82,72128
RA 28,35,7168
Maav 84,184,140
DM 7,40,83,3456
Maav 88,180,124
RA 32,35,8192
Maav 88,184,144
s_nop 0
TR 204,3,8480
TR 206,3,8992
Maav 92,180,128
RA 36,35,9216
Maav 92,184,148
RA 40,35,10240
Maav 96,180,132
RA 44,35,11264
Maav 96,184,152
WL 15
RA 48,35,12288
Maav 100,188,116
s_nop 0
TR 208,3,12704
TR 210,3,13216
Maav 100,192,136
RA 52,35,13312
Maav 104,188,120
RA 56,35,14336
Maav 104,192,140
RA 60,35,15360
Maav 108,188,124
RA 64,35,16384
Maav 108,192,144
s_nop 0
TR 180,5,0
TR 182,5,512
Maav 112,188,128
RA 68,35,17408
Maav 112,192,148
RA 72,35,18432
Maav 116,188,132
RA 76,35,19456
Maav 116,192,152
WL 15
Maav 80,196,196
s_nop 0
TR 184,5,4224
TR 186,5,4736
Maav 80,200,156
Maav 84,196,200
v_or3_b32 v28,v28,v29,v30
v_or_b32 v28,v28,v31
s_nop 0
v_readfirstlane_b32 s90,v28
s_nop 0
s_andn2_b32 s90,s90,s85
s_cbranch_scc1 LC_redo
Maav 84,200,160
v_bfe_u32 v28,v1,2,2
v_lshrrev_b32 v29,4,v1
Maav 88,196,204
v_and_b32 v30,1,v1
v_lshlrev_b32 v30,4,v30
Maav 88,200,164
s_nop 0
TR 188,5,32
TR 190,5,544
Maav 92,196,208
v_mov_b32 v31,0x3f80
v_lshlrev_b32 v30,v30,v31
Maav 92,200,168
v_cmp_eq_u32 vcc,v28,v29
s_nop 1
v_cndmask_b32 v30,0,v30,vcc
Maav 96,196,212
v_and_b32 v28,2,v1
Maav 96,200,172
WL 13
v_cmp_eq_u32 vcc,0,v28
s_nop 1
v_cndmask_b32 v12,0,v30,vcc
v_sub_u32 v13,v30,v12
Maav 100,204,196
s_nop 0
TR 192,5,4256
TR 194,5,4768
Maav 100,208,156
v_mov_b32 v10,0
v_mov_b32 v11,0
Maav 104,204,200
v_mov_b32 v14,0
v_mov_b32 v15,0
Maav 104,208,160
v_and_b32 v192,15,v1
v_mul_u32_u24 v192,0x480,v192
Maav 108,204,204
v_bfe_u32 v28,v1,4,1
v_lshl_add_u32 v192,v28,5,v192
Maav 108,208,164
s_nop 0
TR 196,5,8448
TR 198,5,8960
Maav 112,204,208
v_lshrrev_b32 v28,5,v1
v_lshl_add_u32 v192,v28,4,v192
Maav 112,208,168
v_mov_b32 v195,s89
Maav 116,204,212
s_mov_b32 s17,s89
Maav 116,208,172
WL 6
Maaa 80,180,0
s_mov_b32 s19,s89
TR 200,5,12672
TR 202,5,13184
Maaa 80,184,20
s_add_u32 s18,s88,31
Maaa 84,180,4
s_lshl_b32 s90,s64,6
Maaa 84,184,24
s_lshl_b32 s91,s68,3
Maaa 88,180,8
s_add_u32 s81,s90,s91
Maaa 88,184,28
s_add_u32 s91,s68,2
TR 204,5,8480
TR 206,5,8992
Maaa 92,180,12
s_and_b32 s91,s91,7
Maaa 92,184,32
s_mul_i32 s72,s91,163840
Maaa 96,180,16
s_mul_i32 s92,s64,5120
Maaa 96,184,36
WL 6
Maaa 100,188,0
s_add_u32 s72,s72,s92
TR 208,5,12704
TR 210,5,13216
Maaa 100,192,20
s_lshl_b32 s91,s91,3
Maaa 104,188,4
s_add_u32 s44,s90,s91
Maaa 104,192,24
s_add_u32 s91,s68,1
Maaa 108,188,8
s_and_b32 s91,s91,7
Maaa 108,192,28
s_mul_i32 s73,s91,163840
Maaa 112,188,12
s_mul_i32 s92,s64,5120
Maaa 112,192,32
s_add_u32 s73,s73,s92
Maaa 116,188,16
s_lshl_b32 s91,s91,3
s_add_u32 s45,s90,s91
s_lshl_b32 s92,s64,3
Maaa 116,192,36
WL 4
s_add_u32 s92,s92,s68
s_lshl_b32 s101,s92,10
Maaa 80,196,40
s_waitcnt vmcnt(0) lgkmcnt(0)
s_barrier
Maaa 80,200,60
s_add_u32 s70,s68,2
TR 180,3,34624
TR 182,3,35136
Maaa 84,196,44
s_and_b32 s70,s70,7
TR 184,3,38848
TR 186,3,39360
Maaa 84,200,64
s_lshl_b32 s70,s70,7
s_mul_i32 s90,s92,52
s_lshr_b32 s90,s90,8
Maaa 88,196,48
s_add_u32 s83,s70,0
s_min_u32 s90,s90,3
s_lshl_b32 s46,s90,6
Maaa 88,200,68
M0 82,89728
TR 188,3,34656
TR 190,3,35168
Maaa 92,196,52
DM 7,40,83,0
Maaa 92,200,72
M0 82,89632
s_mul_i32 s91,s90,5
s_sub_u32 s91,s92,s91
Maaa 96,196,56
DM 7,40,83,1152
Maaa 96,200,76
s_mul_i32 s100,s91,18432
s_and_b32 s91,s90,1
Maaa 100,204,40
M0 82,89536
TR 192,3,38880
TR 194,3,39392
Maaa 100,208,60
DM 7,40,83,2304
Maaa 104,204,44
M0 82,89440
s_mul_i32 s91,s91,92160
s_add_u32 s100,s100,s91
Maaa 104,208,64
DM 7,40,83,3456
Maaa 108,204,48
s_add_u32 s83,s70,147456
s_lshr_b32 s90,s90,1
s_lshl_b32 s90,s90,6
Maaa 108,208,68
M0 82,107040
TR 196,3,43072
TR 198,3,43584
Maaa 112,204,52
DM 7,40,83,0
Maaa 112,208,72
M0 82,106944
s_add_u32 s100,s100,s90
s_add_u32 s100,s100,1024
Maaa 116,204,56
DM 7,40,83,1152
Maaa 116,208,76
WL 6
s_mov_b32 s8,s48
s_mov_b32 s9,s49
Maav 120,180,116
M0 82,106848
TR 200,3,47296
TR 202,3,47808
Maav 120,184,136
DM 7,40,83,2304
Maav 124,180,120
M0 82,106752
s_mov_b32 s11,0x20000
Maav 124,184,140
DM 7,40,83,3456
Maav 128,180,124
s_add_u32 s83,s70,294912
s_cmp_lt_u32 s92,20
s_cselect_b32 s10,s50,0
Maav 128,184,144
M0 82,124352
TR 204,3,43104
TR 206,3,43616
Maav 132,180,128
DM 7,40,83,0
Maav 132,184,148
M0 82,124256
AR 176,80
AR 177,81
Maav 136,180,132
s_mov_b32 s12,s52
AR 178,82
AR 179,83
Maav 136,184,152
WL 6
DM 7,40,83,1152
Maav 140,188,116
M0 82,124160
TR 208,3,47328
TR 210,3,47840
Maav 140,192,136
s_mov_b32 s13,s53
AR 180,100
AR 181,101
Maav 144,188,120
DM 7,40,83,2304
Maav 144,192,140
M0 82,124064
AR 182,102
AR 183,103
Maav 148,188,124
s_mov_b32 s15,0x20000
s_cmp_lt_u32 s92,20
s_cselect_b32 s14,s54,0
Maav 148,192,144
s_mov_b32 s99,131072
TR 180,5,34624
TR 182,5,35136
Maav 152,188,128
DM 7,40,83,3456
Maav 152,192,148
s_add_u32 s83,s70,442368
s_mul_i32 s47,s65,92160
CV 176,176,177
Maav 156,188,132
M0 82,141664
CV 177,178,179
CV 178,180,181
Maav 156,192,152
WL 6
DM 7,40,83,0
Maav 120,196,196
M0 82,141568
TR 184,5,38848
TR 186,5,39360
Maav 120,200,156
s_lshl_b32 s90,s68,7
CV 179,182,183
s_add_u32 s23,s74,0
Maav 124,196,200
DM 7,40,83,1152
Maav 124,200,160
M0 82,141472
Maav 128,196,204
s_add_u32 s47,s47,s90
Maav 128,200,164
s_lshl_b32 s90,s66,6
TR 188,5,34656
TR 190,5,35168
Maav 132,196,208
DM 7,40,83,2304
Maav 132,200,168
M0 82,141376
Maav 136,196,212
s_add_u32 s47,s47,s90
Maav 136,200,172
WL 6
DM 7,40,83,3456
Maav 140,204,196
s_nop 0
TR 192,5,38880
TR 194,5,39392
Maav 140,208,156
Maav 144,204,200
buffer_store_dwordx4 v[176:179],v2,s[48:51],s23 offen sc1
Maav 144,208,160
AR 184,84
AR 185,85
Maav 148,204,204
s_add_u32 s70,s68,3
AR 186,86
AR 187,87
Maav 148,208,164
s_and_b32 s70,s70,7
TR 196,5,43072
TR 198,5,43584
Maav 152,204,208
s_lshl_b32 s70,s70,7
AR 188,104
AR 189,105
Maav 152,208,168
s_add_u32 s83,s70,0
AR 190,106
AR 191,107
Maav 156,204,212
M0 82,20480
CV 184,184,185
CV 185,186,187
Maav 156,208,172
WL 6
CV 186,188,189
CV 187,190,191
Maaa 120,180,0
s_add_u32 s23,s74,1024
TR 200,5,47296
TR 202,5,47808
Maaa 120,184,20
DM 7,40,83,0
Maaa 124,180,4
buffer_store_dwordx4 v[184:187],v2,s[48:51],s23 offen sc1
Maaa 124,184,24
AR 176,88
AR 177,89
Maaa 128,180,8
AR 178,90
AR 179,91
Maaa 128,184,28
s_nop 0
TR 204,5,43104
TR 206,5,43616
Maaa 132,180,12
AR 180,108
AR 181,109
Maaa 132,184,32
AR 182,110
AR 183,111
Maaa 136,180,16
M0 82,20384
CV 176,176,177
CV 177,178,179
Maaa 136,184,36
WL 6
CV 178,180,181
CV 179,182,183
Maaa 140,188,0
s_add_u32 s23,s74,2048
TR 208,5,47328
TR 210,5,47840
Maaa 140,192,20
DM 7,40,83,1152
Maaa 144,188,4
buffer_store_dwordx4 v[176:179],v2,s[48:51],s23 offen sc1
Maaa 144,192,24
AR 184,92
AR 185,93
Maaa 148,188,8
AR 186,94
AR 187,95
Maaa 148,192,28
AR 188,112
AR 189,113
Maaa 152,188,12
M0 82,20288
AR 190,114
AR 191,115
Maaa 152,192,32
CV 184,184,185
CV 185,186,187
Maaa 156,188,16
CV 186,188,189
CV 187,190,191
Maaa 156,192,36
WL 4
DM 7,40,83,2304
Maaa 120,196,40
s_waitcnt vmcnt(14) lgkmcnt(0)
s_barrier
Maaa 120,200,60
s_add_u32 s23,s74,3072
TR 180,4,0
TR 182,4,512
Maaa 124,196,44
s_nop 0
TR 184,4,4224
TR 186,4,4736
Maaa 124,200,64
buffer_store_dwordx4 v[184:187],v2,s[48:51],s23 offen sc1
Maaa 128,196,48
AR 176,96
AR 177,97
Maaa 128,200,68
s_nop 0
TR 188,4,32
TR 190,4,544
Maaa 132,196,52
AR 178,98
AR 179,99
Maaa 132,200,72
AR 180,116
AR 181,117
Maaa 136,196,56
AR 182,118
AR 183,119
Maaa 136,200,76
CV 176,176,177
CV 177,178,179
Maaa 140,204,40
s_nop 0
TR 192,4,4256
TR 194,4,4768
Maaa 140,208,60
CV 178,180,181
CV 179,182,183
Maaa 144,204,44
s_add_u32 s23,s74,4096
Maaa 144,208,64
buffer_store_dwordx4 v[176:179],v2,s[48:51],s23 offen sc1
Maaa 148,204,48
Maaa 148,208,68
s_nop 0
TR 196,4,8448
TR 198,4,8960
Maaa 152,204,52
Maaa 152,208,72
Maaa 156,204,56
Maaa 156,208,76
WL 6
Maavz 80,180,216
s_nop 0
TR 200,4,12672
TR 202,4,13184
Maav 80,184,236
s_add_u32 s98,s71,122880
v_lshl_add_u32 v8,v2,1,s98
Maavz 84,180,220
buffer_load_dword v8,v8,s[60:63],0 offen
Maav 84,184,240
AR 184,120
AR 185,121
Maavz 88,180,224
AR 186,122
AR 187,123
Maav 88,184,244
s_nop 0
TR 204,4,8480
TR 206,4,8992
Maavz 92,180,228
AR 188,140
AR 189,141
Maav 92,184,248
AR 190,142
AR 191,143
Maavz 96,180,232
CV 184,184,185
CV 185,186,187
Maav 96,184,252
WL 6
CV 186,188,189
CV 187,190,191
Maavz 100,188,216
s_add_u32 s23,s74,20480
TR 208,4,12704
TR 210,4,13216
Maav 100,192,236
buffer_store_dwordx4 v[184:187],v2,s[48:51],s23 offen sc1
Maavz 104,188,220
AR 176,124
AR 177,125
Maav 104,192,240
AR 178,126
AR 179,127
Maavz 108,188,224
AR 180,144
AR 181,145
Maav 108,192,244
s_nop 0
TR 180,6,0
TR 182,6,512
Maavz 112,188,228
AR 182,146
AR 183,147
Maav 112,192,248
CV 176,176,177
CV 177,178,179
Maavz 116,188,232
CV 178,180,181
CV 179,182,183
Maav 116,192,252
WL 6
v_add_u32_e64 v194,s88,1
Maaa 80,196,216
s_add_u32 s23,s74,21504
TR 184,6,4224
TR 186,6,4736
Maaa 80,200,236
Maaa 84,196,220
Maaa 84,200,240
s_waitcnt vmcnt(2)
s_mov_b64 exec,1
buffer_store_dwordx2 v[194:195],off,s[56:59],s81 sc1
s_mov_b64 exec,-1
Maaa 88,196,224
buffer_store_dwordx4 v[176:179],v2,s[48:51],s23 offen sc1
Maaa 88,200,244
s_nop 0
TR 188,6,32
TR 190,6,544
Maaa 92,196,228
AR 184,128
AR 185,129
Maaa 92,200,248
AR 186,130
AR 187,131
Maaa 96,196,232
AR 188,148
AR 189,149
Maaa 96,200,252
WL 6
AR 190,150
AR 191,151
Maaa 100,204,216
s_nop 0
TR 192,6,4256
TR 194,6,4768
Maaa 100,208,236
CV 184,184,185
CV 185,186,187
Maaa 104,204,220
CV 186,188,189
CV 187,190,191
Maaa 104,208,240
s_add_u32 s23,s74,22528
Maaa 108,204,224
buffer_store_dwordx4 v[184:187],v2,s[48:51],s23 offen sc1
Maaa 108,208,244
s_nop 0
TR 196,6,8448
TR 198,6,8960
Maaa 112,204,228
AR 176,132
AR 177,133
Maaa 112,208,248
AR 178,134
AR 179,135
Maaa 116,204,232
AR 180,152
AR 181,153
Maaa 116,208,252
WL 6
AR 182,154
AR 183,155
Maav 80,180,36
s_nop 0
TR 200,6,12672
TR 202,6,13184
Maav 80,184,56
CV 176,176,177
CV 177,178,179
Maav 84,180,40
CV 178,180,181
CV 179,182,183
Maav 84,184,60
s_add_u32 s23,s74,23552
Maav 88,180,44
buffer_store_dwordx4 v[176:179],v2,s[48:51],s23 offen sc1
Maav 88,184,64
s_nop 0
TR 204,6,8480
TR 206,6,8992
Maav 92,180,48
AR 184,136
AR 185,137
Maav 92,184,68
AR 186,138
AR 187,139
Maav 96,180,52
AR 188,156
AR 189,157
Maav 96,184,72
WL 6
AR 190,158
AR 191,159
Maav 100,188,36
s_nop 0
TR 208,6,12704
TR 210,6,13216
Maav 100,192,56
CV 184,184,185
CV 185,186,187
Maav 104,188,40
CV 186,188,189
CV 187,190,191
Maav 104,192,60
s_add_u32 s23,s74,24576
Maav 108,188,44
buffer_store_dwordx4 v[184:187],v2,s[48:51],s23 offen sc1
Maav 108,192,64
Maav 112,188,48
Maav 112,192,68
Maav 116,188,52
Maav 116,192,72
WL 4
Maav 80,196,76
WL 0
s_barrier
Maav 80,200,96
M0 82,20192
TR 180,4,34624
TR 182,4,35136
Maav 84,196,80
s_nop 0
TR 184,4,38848
TR 186,4,39360
Maav 84,200,100
Maav 88,196,84
DM 7,40,83,3456
Maav 88,200,104
s_add_u32 s83,s70,147456
TR 188,4,34656
TR 190,4,35168
Maav 92,196,88
M0 82,37792
Maav 92,200,108
DM 7,40,83,0
Maav 96,196,92
M0 82,37696
Maav 96,200,112
Maav 100,204,76
s_nop 0
TR 192,4,38880
TR 194,4,39392
Maav 100,208,96
DM 7,40,83,1152
Maav 104,204,80
M0 82,37600
Maav 104,208,100
Maav 108,204,84
DM 7,40,83,2304
Maav 108,208,104
M0 82,37504
TR 196,4,43072
TR 198,4,43584
Maav 112,204,88
Maav 112,208,108
DM 7,40,83,3456
Maav 116,204,92
Maav 116,208,112
WL 6
Maav 80,180,116
s_add_u32 s83,s70,294912
TR 200,4,47296
TR 202,4,47808
Maav 80,184,136
M0 82,55104
v_and_b32 v26,7,v1
v_lshlrev_b32 v26,3,v26
Maav 84,180,120
buffer_load_dwordx2 v[24:25],v26,s[56:59],s46 offen sc1
Maav 84,184,140
DM 7,40,83,0
Maav 88,180,124
M0 82,55008
Maav 88,184,144
s_nop 0
TR 204,4,43104
TR 206,4,43616
Maav 92,180,128
v_add_u32_e64 v194,s88,2
Maav 92,184,148
DM 7,40,83,1152
Maav 96,180,132
Maav 96,184,152
WL 6
Maav 100,188,116
M0 82,54912
TR 208,4,47328
TR 210,4,47840
Maav 100,192,136
s_waitcnt vmcnt(8)
s_mov_b64 exec,1
buffer_store_dwordx2 v[194:195],off,s[56:59],s81 sc1
s_mov_b64 exec,-1
Maav 104,188,120
DM 7,40,83,2304
Maav 104,192,140
M0 82,54816
Maav 108,188,124
Maav 108,192,144
s_nop 0
TR 180,6,34624
TR 182,6,35136
Maav 112,188,128
DM 7,40,83,3456
Maav 112,192,148
s_add_u32 s83,s70,442368
Maav 116,188,132
M0 82,72416
Maav 116,192,152
WL 6
DM 7,40,83,0
Maav 80,196,196
M0 82,72320
TR 184,6,38848
TR 186,6,39360
Maav 80,200,156
Maav 84,196,200
DM 7,40,83,1152
Maav 84,200,160
M0 82,72224
Maav 88,196,204
Maav 88,200,164
s_nop 0
TR 188,6,34656
TR 190,6,35168
Maav 92,196,208
DM 7,40,83,2304
Maav 92,200,168
M0 82,72128
Maav 96,196,212
Maav 96,200,172
WL 6
DM 7,40,83,3456
Maav 100,204,196
s_add_u32 s70,s68,4
TR 192,6,38880
TR 194,6,39392
Maav 100,208,156
s_and_b32 s70,s70,7
Maav 104,204,200
s_lshl_b32 s70,s70,7
Maav 104,208,160
s_add_u32 s83,s70,0
Maav 108,204,204
M0 82,89728
Maav 108,208,164
s_nop 0
TR 196,6,43072
TR 198,6,43584
Maav 112,204,208
DM 7,40,83,0
Maav 112,208,168
M0 82,89632
Maav 116,204,212
Maav 116,208,172
WL 6
DM 7,40,83,1152
Maaa 80,180,0
M0 82,89536
TR 200,6,47296
TR 202,6,47808
Maaa 80,184,20
Maaa 84,180,4
DM 7,40,83,2304
Maaa 84,184,24
M0 82,89440
Maaa 88,180,8
Maaa 88,184,28
s_nop 0
TR 204,6,43104
TR 206,6,43616
Maaa 92,180,12
DM 7,40,83,3456
Maaa 92,184,32
s_add_u32 s83,s70,147456
Maaa 96,180,16
M0 82,107040
s_add_u32 s16,s88,1
s_waitcnt vmcnt(13)
v_cmp_lt_u64 vcc,v[24:25],s[16:17]
v_cmp_gt_u64 s[20:21],v[24:25],s[18:19]
s_nop 0
s_or_b64 s[20:21],s[20:21],vcc
s_cbranch_scc1 LC_spin7
LC_sb7:
Maaa 96,184,36
WL 6
DM 7,40,83,0
Maaa 100,188,0
M0 82,106944
TR 208,6,47328
TR 210,6,47840
Maaa 100,192,20
Maaa 104,188,4
DM 7,40,83,1152
Maaa 104,192,24
M0 82,106848
Maaa 108,188,8
Maaa 108,192,28
DM 7,40,83,2304
Maaa 112,188,12
Maaa 112,192,32
s_add_u32 s76,s101,0
Maaa 116,188,16
buffer_load_dwordx4 a[160:163],v2,s[8:11],s76 offen sc1
Maaa 116,192,36
WL 4
Maaa 80,196,40
WL 0
s_barrier
Maaa 80,200,60
s_add_u32 s76,s101,163840
TR 180,3,0
TR 182,3,512
Maaa 84,196,44
s_nop 0
TR 184,3,4224
TR 186,3,4736
Maaa 84,200,64
buffer_load_dwordx4 a[164:167],v2,s[8:11],s76 offen sc1
Maaa 88,196,48
s_add_u32 s76,s101,327680
Maaa 88,200,68
s_nop 0
TR 188,3,32
TR 190,3,544
Maaa 92,196,52
buffer_load_dwordx4 a[168:171],v2,s[8:11],s76 offen sc1
Maaa 92,200,72
s_add_u32 s76,s101,491520
Maaa 96,196,56
Maaa 96,200,76
buffer_load_dwordx4 a[172:175],v2,s[8:11],s76 offen sc1
Maaa 100,204,40
M0 82,106752
TR 192,3,4256
TR 194,3,4768
Maaa 100,208,60
Maaa 104,204,44
DM 7,40,83,3456
Maaa 104,208,64
s_add_u32 s83,s70,294912
Maaa 108,204,48
M0 82,124352
Maaa 108,208,68
s_nop 0
TR 196,3,8448
TR 198,3,8960
Maaa 112,204,52
DM 7,40,83,0
Maaa 112,208,72
Maaa 116,204,56
Maaa 116,208,76
WL 6
Maavz 120,180,216
M0 82,124256
TR 200,3,12672
TR 202,3,13184
Maav 120,184,236
buffer_load_dwordx2 v[24:25],off,s[56:59],s44 sc1
Maavz 124,180,220
DM 7,40,83,1152
Maav 124,184,240
AR 176,80
AR 177,81
Maavz 128,180,224
AR 178,82
AR 179,83
Maav 128,184,244
s_nop 0
TR 204,3,8480
TR 206,3,8992
Maavz 132,180,228
AR 180,100
AR 181,101
Maav 132,184,248
AR 182,102
AR 183,103
Maavz 136,180,232
CV 176,176,177
CV 177,178,179
Maav 136,184,252
WL 6
CV 178,180,181
CV 179,182,183
Maavz 140,188,216
s_add_u32 s23,s74,40960
TR 208,3,12704
TR 210,3,13216
Maav 140,192,236
buffer_store_dwordx4 v[176:179],v2,s[48:51],s23 offen sc1
Maavz 144,188,220
AR 184,84
AR 185,85
Maav 144,192,240
AR 186,86
AR 187,87
Maavz 148,188,224
AR 188,104
AR 189,105
Maav 148,192,244
s_nop 0
TR 180,5,0
TR 182,5,512
Maavz 152,188,228
AR 190,106
AR 191,107
Maav 152,192,248
CV 184,184,185
CV 185,186,187
Maavz 156,188,232
CV 186,188,189
CV 187,190,191
Maav 156,192,252
WL 6
Maaa 120,196,216
s_add_u32 s23,s74,41984
TR 184,5,4224
TR 186,5,4736
Maaa 120,200,236
buffer_store_dwordx4 v[184:187],v2,s[48:51],s23 offen sc1
Maaa 124,196,220
AR 176,88
AR 177,89
Maaa 124,200,240
AR 178,90
AR 179,91
Maaa 128,196,224
AR 180,108
AR 181,109
Maaa 128,200,244
s_nop 0
TR 188,5,32
TR 190,5,544
Maaa 132,196,228
AR 182,110
AR 183,111
Maaa 132,200,248
CV 176,176,177
CV 177,178,179
Maaa 136,196,232
CV 178,180,181
CV 179,182,183
Maaa 136,200,252
WL 6
Maaa 140,204,216
s_add_u32 s23,s74,43008
TR 192,5,4256
TR 194,5,4768
Maaa 140,208,236
buffer_store_dwordx4 v[176:179],v2,s[48:51],s23 offen sc1
Maaa 144,204,220
AR 184,92
AR 185,93
Maaa 144,208,240
AR 186,94
AR 187,95
Maaa 148,204,224
AR 188,112
AR 189,113
Maaa 148,208,244
s_nop 0
TR 196,5,8448
TR 198,5,8960
Maaa 152,204,228
AR 190,114
AR 191,115
Maaa 152,208,248
CV 184,184,185
CV 185,186,187
Maaa 156,204,232
CV 186,188,189
CV 187,190,191
Maaa 156,208,252
WL 6
Maav 120,180,36
s_add_u32 s23,s74,44032
TR 200,5,12672
TR 202,5,13184
Maav 120,184,56
buffer_store_dwordx4 v[184:187],v2,s[48:51],s23 offen sc1
Maav 124,180,40
AR 176,96
AR 177,97
Maav 124,184,60
AR 178,98
AR 179,99
Maav 128,180,44
AR 180,116
AR 181,117
Maav 128,184,64
s_nop 0
TR 204,5,8480
TR 206,5,8992
Maav 132,180,48
AR 182,118
AR 183,119
Maav 132,184,68
CV 176,176,177
CV 177,178,179
Maav 136,180,52
CV 178,180,181
CV 179,182,183
Maav 136,184,72
WL 6
Maav 140,188,36
s_add_u32 s23,s74,45056
TR 208,5,12704
TR 210,5,13216
Maav 140,192,56
buffer_store_dwordx4 v[176:179],v2,s[48:51],s23 offen sc1
Maav 144,188,40
Maav 144,192,60
Maav 148,188,44
Maav 148,192,64
Maav 152,188,48
Maav 152,192,68
Maav 156,188,52
Maav 156,192,72
WL 4
Maav 120,196,76
s_waitcnt vmcnt(20) lgkmcnt(0)
s_barrier
Maav 120,200,96
M0 82,124160
TR 180,3,34624
TR 182,3,35136
Maav 124,196,80
s_nop 0
TR 184,3,38848
TR 186,3,39360
Maav 124,200,100
DM 7,40,83,2304
Maav 128,196,84
M0 82,124064
Maav 128,200,104
s_nop 0
TR 188,3,34656
TR 190,3,35168
Maav 132,196,88
DM 7,40,83,3456
Maav 132,200,108
s_add_u32 s83,s70,442368
Maav 136,196,92
M0 82,141664
Maav 136,200,112
DM 7,40,83,0
Maav 140,204,76
M0 82,141568
TR 192,3,38880
TR 194,3,39392
Maav 140,208,96
Maav 144,204,80
DM 7,40,83,1152
Maav 144,208,100
M0 82,141472
Maav 148,204,84
Maav 148,208,104
s_nop 0
TR 196,3,43072
TR 198,3,43584
Maav 152,204,88
DM 7,40,83,2304
Maav 152,208,108
M0 82,141376
Maav 156,204,92
Maav 156,208,112
DM 7,40,83,3456
s_waitcnt vmcnt(18)
Mvvaz 16,12,160
s_add_u32 s70,s68,5
Mvvaz 20,10,160
s_and_b32 s70,s70,7
s_waitcnt vmcnt(17)
Mvva 16,12,164
s_lshl_b32 s70,s70,7
Mvva 20,10,164
s_add_u32 s83,s70,0
s_waitcnt vmcnt(16)
Mvva 16,12,168
M0 82,20480
Mvva 20,10,168
DM 7,40,83,0
s_waitcnt vmcnt(16)
Mvva 16,12,172
v_add_u32_e64 v194,s88,3
Mvva 20,10,172
WL 6
Maav 120,180,116
s_nop 0
TR 200,3,47296
TR 202,3,47808
Maav 120,184,136
s_add_u32 s16,s88,2
s_waitcnt vmcnt(13)
v_cmp_lt_u64 vcc,v[24:25],s[16:17]
v_cmp_gt_u64 s[20:21],v[24:25],s[18:19]
s_nop 0
s_or_b64 s[20:21],s[20:21],vcc
s_cbranch_scc1 LC_spin0
LC_sb0:
Maav 124,180,120
s_waitcnt vmcnt(7)
s_mov_b64 exec,1
buffer_store_dwordx2 v[194:195],off,s[56:59],s81 sc1
s_mov_b64 exec,-1
Maav 124,184,140
s_add_u32 s76,s72,20480
Maav 128,180,124
buffer_load_dwordx4 a[160:163],v2,s[48:51],s76 offen sc1
Maav 128,184,144
s_nop 0
TR 204,3,43104
TR 206,3,43616
Maav 132,180,128
Maav 132,184,148
buffer_load_dwordx4 a[164:167],v2,s[48:51],s76 offen offset:1024 sc1
Maav 136,180,132
Maav 136,184,152
WL 6
Maav 140,188,116
s_nop 0
TR 208,3,47328
TR 210,3,47840
Maav 140,192,136
buffer_load_dwordx4 a[168:171],v2,s[48:51],s76 offen offset:2048 sc1
Maav 144,188,120
Maav 144,192,140
Maav 148,188,124
buffer_load_dwordx4 a[172:175],v2,s[48:51],s76 offen offset:3072 sc1
Maav 148,192,144
s_add_u32 s76,s72,24576
TR 180,5,34624
TR 182,5,35136
Maav 152,188,128
Maav 152,192,148
buffer_load_dwordx4 a[176:179],v2,s[48:51],s76 offen sc1
Maav 156,188,132
M0 82,20384
Maav 156,192,152
WL 6
Maav 120,196,196
s_nop 0
TR 184,5,38848
TR 186,5,39360
Maav 120,200,156
DM 7,40,83,1152
Maav 124,196,200
M0 82,20288
Maav 124,200,160
Maav 128,196,204
DM 7,40,83,2304
Maav 128,200,164
M0 82,20192
TR 188,5,34656
TR 190,5,35168
Maav 132,196,208
Maav 132,200,168
DM 7,40,83,3456
Maav 136,196,212
s_add_u32 s83,s70,147456
Maav 136,200,172
WL 6
Maav 140,204,196
M0 82,37792
TR 192,5,38880
TR 194,5,39392
Maav 140,208,156
DM 7,40,83,0
Maav 144,204,200
M0 82,37696
Maav 144,208,160
Maav 148,204,204
DM 7,40,83,1152
Maav 148,208,164
M0 82,37600
TR 196,5,43072
TR 198,5,43584
Maav 152,204,208
Maav 152,208,168
DM 7,40,83,2304
Maav 156,204,212
M0 82,37504
Maav 156,208,172
WL 6
Maaa 120,180,0
s_nop 0
TR 200,5,47296
TR 202,5,47808
Maaa 120,184,20
DM 7,40,83,3456
Maaa 124,180,4
Maaa 124,184,24
Maaa 128,180,8
Maaa 128,184,28
s_nop 0
TR 204,5,43104
TR 206,5,43616
Maaa 132,180,12
Maaa 132,184,32
Maaa 136,180,16
Maaa 136,184,36
WL 6
Maaa 140,188,0
s_nop 0
TR 208,5,47328
TR 210,5,47840
Maaa 140,192,20
Maaa 144,188,4
Maaa 144,192,24
Maaa 148,188,8
Maaa 148,192,28
Maaa 152,188,12
Maaa 152,192,32
Maaa 156,188,16
Maaa 156,192,36
WL 4
Maaa 120,196,40
WL 0
s_barrier
Maaa 120,200,60
s_add_u32 s83,s70,294912
TR 180,4,0
TR 182,4,512
Maaa 124,196,44
M0 82,55104
TR 184,4,4224
TR 186,4,4736
Maaa 124,200,64
DM 7,40,83,0
Maaa 128,196,48
M0 82,55008
Maaa 128,200,68
s_nop 0
TR 188,4,32
TR 190,4,544
Maaa 132,196,52
DM 7,40,83,1152
Maaa 132,200,72
M0 82,54912
Maaa 136,196,56
Maaa 136,200,76
DM 7,40,83,2304
Maaa 140,204,40
M0 82,54816
TR 192,4,4256
TR 194,4,4768
Maaa 140,208,60
Maaa 144,204,44
DM 7,40,83,3456
Maaa 144,208,64
s_add_u32 s83,s70,442368
Maaa 148,204,48
M0 82,72416
Maaa 148,208,68
s_nop 0
TR 196,4,8448
TR 198,4,8960
Maaa 152,204,52
DM 7,40,83,0
Maaa 152,208,72
M0 82,72320
Maaa 156,204,56
Maaa 156,208,76
DM 7,40,83,1152
s_waitcnt vmcnt(17)
Mava 120,12,160
M0 82,72224
Mava 140,10,160
s_waitcnt vmcnt(16)
Mava 124,12,164
DM 7,40,83,2304
Mava 144,10,164
M0 82,72128
s_waitcnt vmcnt(16)
Mava 128,12,168
Mava 148,10,168
DM 7,40,83,3456
s_waitcnt vmcnt(16)
Mava 132,12,172
Mava 152,10,172
s_waitcnt vmcnt(15)
Mava 136,12,176
Mava 156,10,176
WL 6
Maavz 80,180,216
s_nop 0
TR 200,4,12672
TR 202,4,13184
Maav 80,184,236
buffer_load_dwordx2 v[24:25],off,s[56:59],s44 sc1
Maavz 84,180,220
s_add_u32 s76,s101,655360
Maav 84,184,240
AR 184,120
AR 185,121
Maavz 88,180,224
AR 186,122
AR 187,123
Maav 88,184,244
s_nop 0
TR 204,4,8480
TR 206,4,8992
buffer_load_dwordx4 a[160:163],v2,s[8:11],s76 offen sc1
Maavz 92,180,228
s_add_u32 s76,s101,819200
AR 188,140
AR 189,141
Maav 92,184,248
AR 190,142
AR 191,143
Maavz 96,180,232
CV 184,184,185
CV 185,186,187
Maav 96,184,252
WL 6
CV 186,188,189
CV 187,190,191
Maavz 100,188,216
s_add_u32 s23,s74,61440
TR 208,4,12704
TR 210,4,13216
Maav 100,192,236
buffer_store_dwordx4 v[184:187],v2,s[48:51],s23 offen sc1
Maavz 104,188,220
AR 176,124
AR 177,125
Maav 104,192,240
AR 178,126
AR 179,127
Maavz 108,188,224
AR 180,144
AR 181,145
Maav 108,192,244
s_nop 0
TR 180,6,0
TR 182,6,512
Maavz 112,188,228
AR 182,146
AR 183,147
Maav 112,192,248
CV 176,176,177
CV 177,178,179
Maavz 116,188,232
CV 178,180,181
CV 179,182,183
Maav 116,192,252
WL 6
buffer_load_dwordx4 a[164:167],v2,s[8:11],s76 offen sc1
Maaa 80,196,216
s_add_u32 s23,s74,62464
TR 184,6,4224
TR 186,6,4736
Maaa 80,200,236
buffer_store_dwordx4 v[176:179],v2,s[48:51],s23 offen sc1
Maaa 84,196,220
s_add_u32 s76,s101,983040
AR 184,128
AR 185,129
Maaa 84,200,240
AR 186,130
AR 187,131
Maaa 88,196,224
AR 188,148
AR 189,149
Maaa 88,200,244
s_nop 0
TR 188,6,32
TR 190,6,544
Maaa 92,196,228
AR 190,150
AR 191,151
Maaa 92,200,248
CV 184,184,185
CV 185,186,187
Maaa 96,196,232
CV 186,188,189
CV 187,190,191
Maaa 96,200,252
WL 6
buffer_load_dwordx4 a[168:171],v2,s[8:11],s76 offen sc1
Maaa 100,204,216
s_add_u32 s23,s74,63488
TR 192,6,4256
TR 194,6,4768
Maaa 100,208,236
buffer_store_dwordx4 v[184:187],v2,s[48:51],s23 offen sc1
Maaa 104,204,220
s_add_u32 s76,s101,1146880
AR 176,132
AR 177,133
Maaa 104,208,240
AR 178,134
AR 179,135
Maaa 108,204,224
AR 180,152
AR 181,153
Maaa 108,208,244
s_nop 0
TR 196,6,8448
TR 198,6,8960
Maaa 112,204,228
AR 182,154
AR 183,155
Maaa 112,208,248
CV 176,176,177
CV 177,178,179
Maaa 116,204,232
CV 178,180,181
CV 179,182,183
Maaa 116,208,252
WL 6
buffer_load_dwordx4 a[172:175],v2,s[8:11],s76 offen sc1
Maav 80,180,36
s_add_u32 s23,s74,64512
TR 200,6,12672
TR 202,6,13184
Maav 80,184,56
buffer_store_dwordx4 v[176:179],v2,s[48:51],s23 offen sc1
Maav 84,180,40
AR 184,136
AR 185,137
Maav 84,184,60
AR 186,138
AR 187,139
Maav 88,180,44
AR 188,156
AR 189,157
Maav 88,184,64
s_nop 0
TR 204,6,8480
TR 206,6,8992
Maav 92,180,48
AR 190,158
AR 191,159
Maav 92,184,68
CV 184,184,185
CV 185,186,187
Maav 96,180,52
CV 186,188,189
CV 187,190,191
Maav 96,184,72
WL 6
Maav 100,188,36
s_add_u32 s23,s74,65536
TR 208,6,12704
TR 210,6,13216
Maav 100,192,56
buffer_store_dwordx4 v[184:187],v2,s[48:51],s23 offen sc1
Maav 104,188,40
Maav 104,192,60
Maav 108,188,44
Maav 108,192,64
Maav 112,188,48
Maav 112,192,68
Maav 116,188,52
Maav 116,192,72
WL 4
Maav 80,196,76
WL 0
s_barrier
Maav 80,200,96
s_add_u32 s70,s68,6
TR 180,4,34624
TR 182,4,35136
Maav 84,196,80
s_and_b32 s70,s70,7
TR 184,4,38848
TR 186,4,39360
Maav 84,200,100
s_lshl_b32 s70,s70,7
Maav 88,196,84
s_add_u32 s83,s70,0
Maav 88,200,104
M0 82,89728
TR 188,4,34656
TR 190,4,35168
Maav 92,196,88
DM 7,40,83,0
Maav 92,200,108
M0 82,89632
Maav 96,196,92
Maav 96,200,112
DM 7,40,83,1152
Maav 100,204,76
M0 82,89536
TR 192,4,38880
TR 194,4,39392
Maav 100,208,96
Maav 104,204,80
DM 7,40,83,2304
Maav 104,208,100
M0 82,89440
Maav 108,204,84
Maav 108,208,104
s_nop 0
TR 196,4,43072
TR 198,4,43584
Maav 112,204,88
DM 7,40,83,3456
Maav 112,208,108
s_add_u32 s83,s70,147456
Maav 116,204,92
M0 82,107040
Maav 116,208,112
DM 7,40,83,0
s_waitcnt vmcnt(13)
Mvva 16,12,160
M0 82,106944
Mvva 20,10,160
s_waitcnt vmcnt(11)
Mvva 16,12,164
DM 7,40,83,1152
Mvva 20,10,164
M0 82,106848
s_waitcnt vmcnt(10)
Mvva 16,12,168
Mvva 20,10,168
DM 7,40,83,2304
s_waitcnt vmcnt(9)
Mvva 16,12,172
v_add_u32_e64 v194,s88,4
Mvva 20,10,172
WL 6
Maav 80,180,116
s_nop 0
TR 200,4,47296
TR 202,4,47808
Maav 80,184,136
s_add_u32 s16,s88,3
v_cmp_lt_u64 vcc,v[24:25],s[16:17]
v_cmp_gt_u64 s[20:21],v[24:25],s[18:19]
s_nop 0
s_or_b64 s[20:21],s[20:21],vcc
s_cbranch_scc1 LC_spin1
LC_sb1:
Maav 84,180,120
s_waitcnt vmcnt(7)
s_mov_b64 exec,1
buffer_store_dwordx2 v[194:195],off,s[56:59],s81 sc1
s_mov_b64 exec,-1
Maav 84,184,140
s_add_u32 s76,s72,40960
Maav 88,180,124
buffer_load_dwordx4 a[160:163],v2,s[48:51],s76 offen sc1
Maav 88,184,144
s_nop 0
TR 204,4,43104
TR 206,4,43616
Maav 92,180,128
Maav 92,184,148
buffer_load_dwordx4 a[164:167],v2,s[48:51],s76 offen offset:1024 sc1
Maav 96,180,132
Maav 96,184,152
WL 6
Maav 100,188,116
s_nop 0
TR 208,4,47328
TR 210,4,47840
Maav 100,192,136
buffer_load_dwordx4 a[168:171],v2,s[48:51],s76 offen offset:2048 sc1
Maav 104,188,120
Maav 104,192,140
Maav 108,188,124
buffer_load_dwordx4 a[172:175],v2,s[48:51],s76 offen offset:3072 sc1
Maav 108,192,144
s_add_u32 s76,s72,45056
TR 180,6,34624
TR 182,6,35136
Maav 112,188,128
Maav 112,192,148
buffer_load_dwordx4 a[176:179],v2,s[48:51],s76 offen sc1
Maav 116,188,132
M0 82,106752
Maav 116,192,152
WL 6
Maav 80,196,196
s_nop 0
TR 184,6,38848
TR 186,6,39360
Maav 80,200,156
DM 7,40,83,3456
Maav 84,196,200
Maav 84,200,160
Maav 88,196,204
Maav 88,200,164
s_nop 0
TR 188,6,34656
TR 190,6,35168
Maav 92,196,208
Maav 92,200,168
Maav 96,196,212
Maav 96,200,172
WL 6
Maav 100,204,196
s_nop 0
TR 192,6,38880
TR 194,6,39392
Maav 100,208,156
Maav 104,204,200
Maav 104,208,160
Maav 108,204,204
Maav 108,208,164
s_nop 0
TR 196,6,43072
TR 198,6,43584
Maav 112,204,208
Maav 112,208,168
Maav 116,204,212
Maav 116,208,172
WL 6
Maaa 80,180,0
s_nop 0
TR 200,6,47296
TR 202,6,47808
Maaa 80,184,20
Maaa 84,180,4
Maaa 84,184,24
Maaa 88,180,8
Maaa 88,184,28
s_nop 0
TR 204,6,43104
TR 206,6,43616
Maaa 92,180,12
Maaa 92,184,32
Maaa 96,180,16
Maaa 96,184,36
WL 6
Maaa 100,188,0
s_nop 0
TR 208,6,47328
TR 210,6,47840
Maaa 100,192,20
Maaa 104,188,4
Maaa 104,192,24
Maaa 108,188,8
Maaa 108,192,28
Maaa 112,188,12
Maaa 112,192,32
Maaa 116,188,16
Maaa 116,192,36
WL 4
Maaa 80,196,40
WL 0
s_barrier
Maaa 80,200,60
s_add_u32 s83,s70,294912
TR 180,3,0
TR 182,3,512
Maaa 84,196,44
M0 82,124352
TR 184,3,4224
TR 186,3,4736
Maaa 84,200,64
DM 7,40,83,0
Maaa 88,196,48
M0 82,124256
Maaa 88,200,68
s_nop 0
TR 188,3,32
TR 190,3,544
Maaa 92,196,52
DM 7,40,83,1152
Maaa 92,200,72
M0 82,124160
Maaa 96,196,56
Maaa 96,200,76
DM 7,40,83,2304
Maaa 100,204,40
M0 82,124064
TR 192,3,4256
TR 194,3,4768
Maaa 100,208,60
Maaa 104,204,44
DM 7,40,83,3456
Maaa 104,208,64
s_add_u32 s83,s70,442368
Maaa 108,204,48
M0 82,141664
Maaa 108,208,68
s_nop 0
TR 196,3,8448
TR 198,3,8960
Maaa 112,204,52
DM 7,40,83,0
Maaa 112,208,72
M0 82,141568
Maaa 116,204,56
Maaa 116,208,76
DM 7,40,83,1152
s_waitcnt vmcnt(11)
Mava 80,12,160
M0 82,141472
Mava 100,10,160
s_waitcnt vmcnt(10)
Mava 84,12,164
DM 7,40,83,2304
Mava 104,10,164
M0 82,141376
s_waitcnt vmcnt(10)
Mava 88,12,168
Mava 108,10,168
DM 7,40,83,3456
s_waitcnt vmcnt(10)
Mava 92,12,172
Mava 112,10,172
s_waitcnt vmcnt(9)
Mava 96,12,176
Mava 116,10,176
WL 6
Maavz 120,180,216
s_nop 0
TR 200,3,12672
TR 202,3,13184
Maav 120,184,236
buffer_load_dwordx2 v[24:25],off,s[56:59],s44 sc1
Maavz 124,180,220
Maav 124,184,240
AR 176,80
AR 177,81
Maavz 128,180,224
AR 178,82
AR 179,83
Maav 128,184,244
s_nop 0
TR 204,3,8480
TR 206,3,8992
Maavz 132,180,228
AR 180,100
AR 181,101
Maav 132,184,248
AR 182,102
AR 183,103
Maavz 136,180,232
CV 176,176,177
CV 177,178,179
Maav 136,184,252
WL 6
CV 178,180,181
CV 179,182,183
Maavz 140,188,216
s_add_u32 s23,s74,81920
TR 208,3,12704
TR 210,3,13216
Maav 140,192,236
buffer_store_dwordx4 v[176:179],v2,s[48:51],s23 offen sc1
Maavz 144,188,220
AR 184,84
AR 185,85
Maav 144,192,240
AR 186,86
AR 187,87
Maavz 148,188,224
AR 188,104
AR 189,105
Maav 148,192,244
s_nop 0
TR 180,5,0
TR 182,5,512
Maavz 152,188,228
AR 190,106
AR 191,107
Maav 152,192,248
CV 184,184,185
CV 185,186,187
Maavz 156,188,232
CV 186,188,189
CV 187,190,191
Maav 156,192,252
WL 6
Maaa 120,196,216
s_add_u32 s23,s74,82944
TR 184,5,4224
TR 186,5,4736
Maaa 120,200,236
buffer_store_dwordx4 v[184:187],v2,s[48:51],s23 offen sc1
Maaa 124,196,220
AR 176,88
AR 177,89
Maaa 124,200,240
AR 178,90
AR 179,91
Maaa 128,196,224
AR 180,108
AR 181,109
Maaa 128,200,244
s_nop 0
TR 188,5,32
TR 190,5,544
Maaa 132,196,228
AR 182,110
AR 183,111
Maaa 132,200,248
CV 176,176,177
CV 177,178,179
Maaa 136,196,232
CV 178,180,181
CV 179,182,183
Maaa 136,200,252
WL 6
Maaa 140,204,216
s_add_u32 s23,s74,83968
TR 192,5,4256
TR 194,5,4768
Maaa 140,208,236
buffer_store_dwordx4 v[176:179],v2,s[48:51],s23 offen sc1
Maaa 144,204,220
AR 184,92
AR 185,93
Maaa 144,208,240
AR 186,94
AR 187,95
Maaa 148,204,224
AR 188,112
AR 189,113
Maaa 148,208,244
s_nop 0
TR 196,5,8448
TR 198,5,8960
Maaa 152,204,228
AR 190,114
AR 191,115
Maaa 152,208,248
CV 184,184,185
CV 185,186,187
Maaa 156,204,232
CV 186,188,189
CV 187,190,191
Maaa 156,208,252
WL 6
Maav 120,180,36
s_add_u32 s23,s74,84992
TR 200,5,12672
TR 202,5,13184
Maav 120,184,56
buffer_store_dwordx4 v[184:187],v2,s[48:51],s23 offen sc1
Maav 124,180,40
AR 176,96
AR 177,97
Maav 124,184,60
AR 178,98
AR 179,99
Maav 128,180,44
AR 180,116
AR 181,117
Maav 128,184,64
s_nop 0
TR 204,5,8480
TR 206,5,8992
Maav 132,180,48
AR 182,118
AR 183,119
Maav 132,184,68
CV 176,176,177
CV 177,178,179
Maav 136,180,52
CV 178,180,181
CV 179,182,183
Maav 136,184,72
WL 6
Maav 140,188,36
s_add_u32 s23,s74,86016
TR 208,5,12704
TR 210,5,13216
Maav 140,192,56
buffer_store_dwordx4 v[176:179],v2,s[48:51],s23 offen sc1
Maav 144,188,40
Maav 144,192,60
Maav 148,188,44
Maav 148,192,64
Maav 152,188,48
Maav 152,192,68
Maav 156,188,52
Maav 156,192,72
WL 4
Maav 120,196,76
WL 0
s_barrier
Maav 120,200,96
s_nop 0
TR 180,3,34624
TR 182,3,35136
Maav 124,196,80
s_nop 0
TR 184,3,38848
TR 186,3,39360
Maav 124,200,100
s_add_u32 s16,s88,4
s_waitcnt vmcnt(5)
v_cmp_lt_u64 vcc,v[24:25],s[16:17]
v_cmp_gt_u64 s[20:21],v[24:25],s[18:19]
s_nop 0
s_or_b64 s[20:21],s[20:21],vcc
s_cbranch_scc1 LC_spin2
LC_sb2:
Maav 128,196,84
s_add_u32 s76,s72,61440
Maav 128,200,104
s_nop 0
TR 188,3,34656
TR 190,3,35168
Maav 132,196,88
buffer_load_dwordx4 a[160:163],v2,s[48:51],s76 offen sc1
Maav 132,200,108
Maav 136,196,92
Maav 136,200,112
buffer_load_dwordx4 a[164:167],v2,s[48:51],s76 offen offset:1024 sc1
Maav 140,204,76
s_nop 0
TR 192,3,38880
TR 194,3,39392
Maav 140,208,96
Maav 144,204,80
buffer_load_dwordx4 a[168:171],v2,s[48:51],s76 offen offset:2048 sc1
Maav 144,208,100
Maav 148,204,84
Maav 148,208,104
s_add_u32 s70,s68,7
TR 196,3,43072
TR 198,3,43584
Maav 152,204,88
buffer_load_dwordx4 a[172:175],v2,s[48:51],s76 offen offset:3072 sc1
Maav 152,208,108
s_add_u32 s76,s72,65536
Maav 156,204,92
s_and_b32 s70,s70,7
Maav 156,208,112
WL 6
buffer_load_dwordx4 a[176:179],v2,s[48:51],s76 offen sc1
Maav 120,180,116
s_lshl_b32 s70,s70,7
TR 200,3,47296
TR 202,3,47808
Maav 120,184,136
s_add_u32 s83,s70,0
M0 82,20480
Maav 124,180,120
DM 7,40,83,0
Maav 124,184,140
Maav 128,180,124
M0 82,20384
Maav 128,184,144
s_nop 0
TR 204,3,43104
TR 206,3,43616
Maav 132,180,128
v_add_u32_e64 v194,s88,5
Maav 132,184,148
DM 7,40,83,1152
Maav 136,180,132
Maav 136,184,152
WL 6
Maav 140,188,116
M0 82,20288
TR 208,3,47328
TR 210,3,47840
Maav 140,192,136
s_waitcnt vmcnt(7)
s_mov_b64 exec,1
buffer_store_dwordx2 v[194:195],off,s[56:59],s81 sc1
s_mov_b64 exec,-1
Maav 144,188,120
DM 7,40,83,2304
Maav 144,192,140
M0 82,20192
Maav 148,188,124
Maav 148,192,144
s_nop 0
TR 180,5,34624
TR 182,5,35136
Maav 152,188,128
DM 7,40,83,3456
Maav 152,192,148
s_add_u32 s83,s70,147456
Maav 156,188,132
M0 82,37792
Maav 156,192,152
WL 6
DM 7,40,83,0
Maav 120,196,196
M0 82,37696
TR 184,5,38848
TR 186,5,39360
Maav 120,200,156
Maav 124,196,200
DM 7,40,83,1152
Maav 124,200,160
M0 82,37600
Maav 128,196,204
Maav 128,200,164
s_nop 0
TR 188,5,34656
TR 190,5,35168
Maav 132,196,208
DM 7,40,83,2304
Maav 132,200,168
M0 82,37504
Maav 136,196,212
Maav 136,200,172
WL 6
DM 7,40,83,3456
Maav 140,204,196
s_nop 0
TR 192,5,38880
TR 194,5,39392
Maav 140,208,156
CV 16,16,17
CV 17,18,19
Maav 144,204,200
CV 18,20,21
CV 19,22,23
Maav 144,208,160
v_permlane16_swap_b32 v16,v18
v_permlane16_swap_b32 v17,v19
Maav 148,204,204
buffer_store_dwordx4 v[16:19],v192,s[12:15],s100 offen
Maav 148,208,164
s_nop 0
TR 196,5,43072
TR 198,5,43584
Maav 152,204,208
v_mov_b32 v194,s88
s_mov_b64 exec,1
buffer_store_dwordx2 v[194:195],off,s[56:59],0 offset:256 sc1
s_mov_b64 exec,-1
Maav 152,208,168
Maav 156,204,212
Maav 156,208,172
WL 6
Maaa 120,180,0
s_nop 0
TR 200,5,47296
TR 202,5,47808
Maaa 120,184,20
Maaa 124,180,4
Maaa 124,184,24
Maaa 128,180,8
Maaa 128,184,28
s_nop 0
TR 204,5,43104
TR 206,5,43616
Maaa 132,180,12
Maaa 132,184,32
Maaa 136,180,16
Maaa 136,184,36
WL 6
Maaa 140,188,0
s_nop 0
TR 208,5,47328
TR 210,5,47840
Maaa 140,192,20
Maaa 144,188,4
Maaa 144,192,24
Maaa 148,188,8
Maaa 148,192,28
Maaa 152,188,12
Maaa 152,192,32
Maaa 156,188,16
Maaa 156,192,36
WL 4
Maaa 120,196,40
WL 0
s_barrier
Maaa 120,200,60
s_add_u32 s83,s70,294912
TR 180,4,0
TR 182,4,512
Maaa 124,196,44
M0 82,55104
TR 184,4,4224
TR 186,4,4736
Maaa 124,200,64
DM 7,40,83,0
Maaa 128,196,48
M0 82,55008
Maaa 128,200,68
s_nop 0
TR 188,4,32
TR 190,4,544
Maaa 132,196,52
DM 7,40,83,1152
Maaa 132,200,72
M0 82,54912
Maaa 136,196,56
Maaa 136,200,76
DM 7,40,83,2304
Maaa 140,204,40
M0 82,54816
TR 192,4,4256
TR 194,4,4768
Maaa 140,208,60
Maaa 144,204,44
DM 7,40,83,3456
Maaa 144,208,64
s_add_u32 s83,s70,442368
Maaa 148,204,48
M0 82,72416
Maaa 148,208,68
s_nop 0
TR 196,4,8448
TR 198,4,8960
Maaa 152,204,52
DM 7,40,83,0
Maaa 152,208,72
M0 82,72320
Maaa 156,204,56
Maaa 156,208,76
DM 7,40,83,1152
s_waitcnt vmcnt(21)
Mava 120,12,160
M0 82,72224
Mava 140,10,160
s_waitcnt vmcnt(20)
Mava 124,12,164
DM 7,40,83,2304
Mava 144,10,164
M0 82,72128
s_waitcnt vmcnt(20)
Mava 128,12,168
Mava 148,10,168
DM 7,40,83,3456
s_waitcnt vmcnt(20)
Mava 132,12,172
Mava 152,10,172
s_waitcnt vmcnt(19)
Mava 136,12,176
Mava 156,10,176
WL 6
Maavz 80,180,216
s_nop 0
TR 200,4,12672
TR 202,4,13184
Maav 80,184,236
buffer_load_dwordx2 v[24:25],off,s[56:59],s44 sc1
Maavz 84,180,220
Maav 84,184,240
AR 184,120
AR 185,121
Maavz 88,180,224
AR 186,122
AR 187,123
Maav 88,184,244
s_nop 0
TR 204,4,8480
TR 206,4,8992
Maavz 92,180,228
AR 188,140
AR 189,141
Maav 92,184,248
AR 190,142
AR 191,143
Maavz 96,180,232
CV 184,184,185
CV 185,186,187
Maav 96,184,252
WL 6
CV 186,188,189
CV 187,190,191
Maavz 100,188,216
s_add_u32 s23,s74,102400
TR 208,4,12704
TR 210,4,13216
Maav 100,192,236
buffer_store_dwordx4 v[184:187],v2,s[48:51],s23 offen sc1
Maavz 104,188,220
AR 176,124
AR 177,125
Maav 104,192,240
AR 178,126
AR 179,127
Maavz 108,188,224
AR 180,144
AR 181,145
Maav 108,192,244
s_nop 0
TR 180,6,0
TR 182,6,512
Maavz 112,188,228
AR 182,146
AR 183,147
Maav 112,192,248
CV 176,176,177
CV 177,178,179
Maavz 116,188,232
CV 178,180,181
CV 179,182,183
Maav 116,192,252
WL 6
Maaa 80,196,216
s_add_u32 s23,s74,103424
TR 184,6,4224
TR 186,6,4736
Maaa 80,200,236
buffer_store_dwordx4 v[176:179],v2,s[48:51],s23 offen sc1
Maaa 84,196,220
AR 184,128
AR 185,129
Maaa 84,200,240
AR 186,130
AR 187,131
Maaa 88,196,224
AR 188,148
AR 189,149
Maaa 88,200,244
s_nop 0
TR 188,6,32
TR 190,6,544
Maaa 92,196,228
AR 190,150
AR 191,151
Maaa 92,200,248
CV 184,184,185
CV 185,186,187
Maaa 96,196,232
CV 186,188,189
CV 187,190,191
Maaa 96,200,252
WL 6
Maaa 100,204,216
s_add_u32 s23,s74,104448
TR 192,6,4256
TR 194,6,4768
Maaa 100,208,236
buffer_store_dwordx4 v[184:187],v2,s[48:51],s23 offen sc1
Maaa 104,204,220
AR 176,132
AR 177,133
Maaa 104,208,240
AR 178,134
AR 179,135
Maaa 108,204,224
AR 180,152
AR 181,153
Maaa 108,208,244
s_nop 0
TR 196,6,8448
TR 198,6,8960
Maaa 112,204,228
AR 182,154
AR 183,155
Maaa 112,208,248
CV 176,176,177
CV 177,178,179
Maaa 116,204,232
CV 178,180,181
CV 179,182,183
Maaa 116,208,252
WL 6
Maav 80,180,36
s_add_u32 s23,s74,105472
TR 200,6,12672
TR 202,6,13184
Maav 80,184,56
buffer_store_dwordx4 v[176:179],v2,s[48:51],s23 offen sc1
Maav 84,180,40
AR 184,136
AR 185,137
Maav 84,184,60
AR 186,138
AR 187,139
Maav 88,180,44
AR 188,156
AR 189,157
Maav 88,184,64
s_nop 0
TR 204,6,8480
TR 206,6,8992
Maav 92,180,48
AR 190,158
AR 191,159
Maav 92,184,68
CV 184,184,185
CV 185,186,187
Maav 96,180,52
CV 186,188,189
CV 187,190,191
Maav 96,184,72
WL 6
Maav 100,188,36
s_add_u32 s23,s74,106496
TR 208,6,12704
TR 210,6,13216
Maav 100,192,56
buffer_store_dwordx4 v[184:187],v2,s[48:51],s23 offen sc1
Maav 104,188,40
Maav 104,192,60
Maav 108,188,44
Maav 108,192,64
Maav 112,188,48
Maav 112,192,68
Maav 116,188,52
Maav 116,192,72
WL 4
Maav 80,196,76
WL 0
s_barrier
Maav 80,200,96
s_nop 0
TR 180,4,34624
TR 182,4,35136
Maav 84,196,80
s_nop 0
TR 184,4,38848
TR 186,4,39360
Maav 84,200,100
s_add_u32 s16,s88,5
s_waitcnt vmcnt(5)
v_cmp_lt_u64 vcc,v[24:25],s[16:17]
v_cmp_gt_u64 s[20:21],v[24:25],s[18:19]
s_nop 0
s_or_b64 s[20:21],s[20:21],vcc
s_cbranch_scc1 LC_spin3
LC_sb3:
Maav 88,196,84
s_add_u32 s76,s72,81920
Maav 88,200,104
s_nop 0
TR 188,4,34656
TR 190,4,35168
Maav 92,196,88
buffer_load_dwordx4 a[160:163],v2,s[48:51],s76 offen sc1
Maav 92,200,108
Maav 96,196,92
Maav 96,200,112
buffer_load_dwordx4 a[164:167],v2,s[48:51],s76 offen offset:1024 sc1
Maav 100,204,76
s_nop 0
TR 192,4,38880
TR 194,4,39392
Maav 100,208,96
Maav 104,204,80
buffer_load_dwordx4 a[168:171],v2,s[48:51],s76 offen offset:2048 sc1
Maav 104,208,100
Maav 108,204,84
Maav 108,208,104
s_add_u32 s70,s68,8
TR 196,4,43072
TR 198,4,43584
Maav 112,204,88
buffer_load_dwordx4 a[172:175],v2,s[48:51],s76 offen offset:3072 sc1
Maav 112,208,108
s_add_u32 s76,s72,86016
Maav 116,204,92
s_and_b32 s70,s70,7
Maav 116,208,112
WL 6
buffer_load_dwordx4 a[176:179],v2,s[48:51],s76 offen sc1
Maav 80,180,116
s_lshl_b32 s70,s70,7
TR 200,4,47296
TR 202,4,47808
Maav 80,184,136
s_add_u32 s83,s70,0
M0 82,89728
Maav 84,180,120
DM 7,40,83,0
Maav 84,184,140
Maav 88,180,124
M0 82,89632
Maav 88,184,144
s_nop 0
TR 204,4,43104
TR 206,4,43616
Maav 92,180,128
v_add_u32_e64 v194,s88,6
Maav 92,184,148
DM 7,40,83,1152
Maav 96,180,132
Maav 96,184,152
WL 6
Maav 100,188,116
M0 82,89536
TR 208,4,47328
TR 210,4,47840
Maav 100,192,136
s_waitcnt vmcnt(7)
s_mov_b64 exec,1
buffer_store_dwordx2 v[194:195],off,s[56:59],s81 sc1
s_mov_b64 exec,-1
Maav 104,188,120
DM 7,40,83,2304
Maav 104,192,140
M0 82,89440
Maav 108,188,124
Maav 108,192,144
s_nop 0
TR 180,6,34624
TR 182,6,35136
Maav 112,188,128
DM 7,40,83,3456
Maav 112,192,148
s_add_u32 s83,s70,147456
Maav 116,188,132
M0 82,107040
Maav 116,192,152
WL 6
DM 7,40,83,0
Maav 80,196,196
M0 82,106944
TR 184,6,38848
TR 186,6,39360
Maav 80,200,156
Maav 84,196,200
DM 7,40,83,1152
Maav 84,200,160
M0 82,106848
Maav 88,196,204
Maav 88,200,164
s_nop 0
TR 188,6,34656
TR 190,6,35168
Maav 92,196,208
DM 7,40,83,2304
Maav 92,200,168
M0 82,106752
Maav 96,196,212
Maav 96,200,172
WL 6
DM 7,40,83,3456
Maav 100,204,196
s_nop 0
TR 192,6,38880
TR 194,6,39392
Maav 100,208,156
Maav 104,204,200
Maav 104,208,160
Maav 108,204,204
Maav 108,208,164
s_nop 0
TR 196,6,43072
TR 198,6,43584
Maav 112,204,208
Maav 112,208,168
Maav 116,204,212
Maav 116,208,172
WL 6
Maaa 80,180,0
s_nop 0
TR 200,6,47296
TR 202,6,47808
Maaa 80,184,20
Maaa 84,180,4
Maaa 84,184,24
Maaa 88,180,8
Maaa 88,184,28
s_nop 0
TR 204,6,43104
TR 206,6,43616
Maaa 92,180,12
Maaa 92,184,32
Maaa 96,180,16
Maaa 96,184,36
WL 6
Maaa 100,188,0
s_nop 0
TR 208,6,47328
TR 210,6,47840
Maaa 100,192,20
Maaa 104,188,4
Maaa 104,192,24
Maaa 108,188,8
Maaa 108,192,28
Maaa 112,188,12
Maaa 112,192,32
Maaa 116,188,16
Maaa 116,192,36
WL 4
Maaa 80,196,40
WL 0
s_barrier
Maaa 80,200,60
s_add_u32 s83,s70,294912
TR 180,3,0
TR 182,3,512
Maaa 84,196,44
M0 82,124352
TR 184,3,4224
TR 186,3,4736
Maaa 84,200,64
DM 7,40,83,0
Maaa 88,196,48
M0 82,124256
Maaa 88,200,68
s_nop 0
TR 188,3,32
TR 190,3,544
Maaa 92,196,52
DM 7,40,83,1152
Maaa 92,200,72
M0 82,124160
Maaa 96,196,56
Maaa 96,200,76
DM 7,40,83,2304
Maaa 100,204,40
M0 82,124064
TR 192,3,4256
TR 194,3,4768
Maaa 100,208,60
Maaa 104,204,44
DM 7,40,83,3456
Maaa 104,208,64
s_add_u32 s83,s70,442368
Maaa 108,204,48
M0 82,141664
Maaa 108,208,68
s_nop 0
TR 196,3,8448
TR 198,3,8960
Maaa 112,204,52
DM 7,40,83,0
Maaa 112,208,72
M0 82,141568
Maaa 116,204,56
Maaa 116,208,76
DM 7,40,83,1152
s_waitcnt vmcnt(19)
Mava 80,12,160
M0 82,141472
Mava 100,10,160
s_waitcnt vmcnt(18)
Mava 84,12,164
DM 7,40,83,2304
Mava 104,10,164
M0 82,141376
s_waitcnt vmcnt(18)
Mava 88,12,168
Mava 108,10,168
DM 7,40,83,3456
s_waitcnt vmcnt(18)
Mava 92,12,172
Mava 112,10,172
s_waitcnt vmcnt(17)
Mava 96,12,176
Mava 116,10,176
WL 6
Maavz 120,180,216
s_nop 0
TR 200,3,12672
TR 202,3,13184
Maav 120,184,236
buffer_load_dwordx2 v[24:25],off,s[56:59],s44 sc1
Maavz 124,180,220
Maav 124,184,240
AR 176,80
AR 177,81
Maavz 128,180,224
AR 178,82
AR 179,83
Maav 128,184,244
s_nop 0
TR 204,3,8480
TR 206,3,8992
Maavz 132,180,228
AR 180,100
AR 181,101
Maav 132,184,248
AR 182,102
AR 183,103
Maavz 136,180,232
CV 176,176,177
CV 177,178,179
Maav 136,184,252
WL 6
CV 178,180,181
CV 179,182,183
Maavz 140,188,216
s_add_u32 s23,s74,122880
TR 208,3,12704
TR 210,3,13216
Maav 140,192,236
buffer_store_dwordx4 v[176:179],v2,s[48:51],s23 offen sc1
Maavz 144,188,220
AR 184,84
AR 185,85
Maav 144,192,240
AR 186,86
AR 187,87
Maavz 148,188,224
AR 188,104
AR 189,105
Maav 148,192,244
s_nop 0
TR 180,5,0
TR 182,5,512
Maavz 152,188,228
AR 190,106
AR 191,107
Maav 152,192,248
CV 184,184,185
CV 185,186,187
Maavz 156,188,232
CV 186,188,189
CV 187,190,191
Maav 156,192,252
WL 6
Maaa 120,196,216
s_add_u32 s23,s74,123904
TR 184,5,4224
TR 186,5,4736
Maaa 120,200,236
buffer_store_dwordx4 v[184:187],v2,s[48:51],s23 offen sc1
Maaa 124,196,220
AR 176,88
AR 177,89
Maaa 124,200,240
AR 178,90
AR 179,91
Maaa 128,196,224
AR 180,108
AR 181,109
Maaa 128,200,244
s_nop 0
TR 188,5,32
TR 190,5,544
Maaa 132,196,228
AR 182,110
AR 183,111
Maaa 132,200,248
CV 176,176,177
CV 177,178,179
Maaa 136,196,232
CV 178,180,181
CV 179,182,183
Maaa 136,200,252
WL 6
Maaa 140,204,216
s_add_u32 s23,s74,124928
TR 192,5,4256
TR 194,5,4768
Maaa 140,208,236
buffer_store_dwordx4 v[176:179],v2,s[48:51],s23 offen sc1
Maaa 144,204,220
AR 184,92
AR 185,93
Maaa 144,208,240
AR 186,94
AR 187,95
Maaa 148,204,224
AR 188,112
AR 189,113
Maaa 148,208,244
s_nop 0
TR 196,5,8448
TR 198,5,8960
Maaa 152,204,228
AR 190,114
AR 191,115
Maaa 152,208,248
CV 184,184,185
CV 185,186,187
Maaa 156,204,232
CV 186,188,189
CV 187,190,191
Maaa 156,208,252
WL 6
Maav 120,180,36
s_add_u32 s23,s74,125952
TR 200,5,12672
TR 202,5,13184
Maav 120,184,56
buffer_store_dwordx4 v[184:187],v2,s[48:51],s23 offen sc1
Maav 124,180,40
AR 176,96
AR 177,97
Maav 124,184,60
AR 178,98
AR 179,99
Maav 128,180,44
AR 180,116
AR 181,117
Maav 128,184,64
s_nop 0
TR 204,5,8480
TR 206,5,8992
Maav 132,180,48
AR 182,118
AR 183,119
Maav 132,184,68
CV 176,176,177
CV 177,178,179
Maav 136,180,52
CV 178,180,181
CV 179,182,183
Maav 136,184,72
WL 6
Maav 140,188,36
s_add_u32 s23,s74,126976
TR 208,5,12704
TR 210,5,13216
Maav 140,192,56
buffer_store_dwordx4 v[176:179],v2,s[48:51],s23 offen sc1
Maav 144,188,40
Maav 144,192,60
Maav 148,188,44
Maav 148,192,64
Maav 152,188,48
Maav 152,192,68
Maav 156,188,52
Maav 156,192,72
WL 4
Maav 120,196,76
WL 0
s_barrier
Maav 120,200,96
s_nop 0
TR 180,3,34624
TR 182,3,35136
Maav 124,196,80
s_nop 0
TR 184,3,38848
TR 186,3,39360
Maav 124,200,100
s_add_u32 s16,s88,6
s_waitcnt vmcnt(5)
v_cmp_lt_u64 vcc,v[24:25],s[16:17]
v_cmp_gt_u64 s[20:21],v[24:25],s[18:19]
s_nop 0
s_or_b64 s[20:21],s[20:21],vcc
s_cbranch_scc1 LC_spin4
LC_sb4:
Maav 128,196,84
s_add_u32 s76,s72,102400
Maav 128,200,104
s_nop 0
TR 188,3,34656
TR 190,3,35168
Maav 132,196,88
buffer_load_dwordx4 a[160:163],v2,s[48:51],s76 offen sc1
Maav 132,200,108
Maav 136,196,92
Maav 136,200,112
buffer_load_dwordx4 a[164:167],v2,s[48:51],s76 offen offset:1024 sc1
Maav 140,204,76
s_nop 0
TR 192,3,38880
TR 194,3,39392
Maav 140,208,96
Maav 144,204,80
buffer_load_dwordx4 a[168:171],v2,s[48:51],s76 offen offset:2048 sc1
Maav 144,208,100
Maav 148,204,84
Maav 148,208,104
s_nop 0
TR 196,3,43072
TR 198,3,43584
Maav 152,204,88
buffer_load_dwordx4 a[172:175],v2,s[48:51],s76 offen offset:3072 sc1
Maav 152,208,108
s_add_u32 s76,s72,106496
Maav 156,204,92
Maav 156,208,112
WL 6
buffer_load_dwordx4 a[176:179],v2,s[48:51],s76 offen sc1
Maav 120,180,116
s_nop 0
TR 200,3,47296
TR 202,3,47808
Maav 120,184,136
Maav 124,180,120
Maav 124,184,140
Maav 128,180,124
Maav 128,184,144
s_nop 0
TR 204,3,43104
TR 206,3,43616
Maav 132,180,128
v_add_u32_e64 v194,s88,7
Maav 132,184,148
Maav 136,180,132
Maav 136,184,152
WL 6
Maav 140,188,116
s_nop 0
TR 208,3,47328
TR 210,3,47840
Maav 140,192,136
s_waitcnt vmcnt(5)
s_mov_b64 exec,1
buffer_store_dwordx2 v[194:195],off,s[56:59],s81 sc1
s_mov_b64 exec,-1
Maav 144,188,120
Maav 144,192,140
Maav 148,188,124
Maav 148,192,144
s_nop 0
TR 180,5,34624
TR 182,5,35136
Maav 152,188,128
Maav 152,192,148
Maav 156,188,132
Maav 156,192,152
WL 6
Maav 120,196,196
s_nop 0
TR 184,5,38848
TR 186,5,39360
Maav 120,200,156
Maav 124,196,200
Maav 124,200,160
Maav 128,196,204
Maav 128,200,164
s_nop 0
TR 188,5,34656
TR 190,5,35168
Maav 132,196,208
Maav 132,200,168
Maav 136,196,212
Maav 136,200,172
WL 6
Maav 140,204,196
s_nop 0
TR 192,5,38880
TR 194,5,39392
Maav 140,208,156
Maav 144,204,200
Maav 144,208,160
Maav 148,204,204
Maav 148,208,164
s_nop 0
TR 196,5,43072
TR 198,5,43584
Maav 152,204,208
Maav 152,208,168
Maav 156,204,212
Maav 156,208,172
WL 6
Maaa 120,180,0
s_nop 0
TR 200,5,47296
TR 202,5,47808
Maaa 120,184,20
Maaa 124,180,4
Maaa 124,184,24
Maaa 128,180,8
Maaa 128,184,28
s_nop 0
TR 204,5,43104
TR 206,5,43616
Maaa 132,180,12
Maaa 132,184,32
Maaa 136,180,16
Maaa 136,184,36
WL 6
Maaa 140,188,0
s_nop 0
TR 208,5,47328
TR 210,5,47840
Maaa 140,192,20
Maaa 144,188,4
Maaa 144,192,24
Maaa 148,188,8
Maaa 148,192,28
Maaa 152,188,12
Maaa 152,192,32
Maaa 156,188,16
Maaa 156,192,36
WL 4
Maaa 120,196,40
WL 0
s_barrier
Maaa 120,200,60
s_nop 0
TR 180,4,0
TR 182,4,512
Maaa 124,196,44
s_nop 0
TR 184,4,4224
TR 186,4,4736
Maaa 124,200,64
Maaa 128,196,48
Maaa 128,200,68
s_nop 0
TR 188,4,32
TR 190,4,544
Maaa 132,196,52
Maaa 132,200,72
Maaa 136,196,56
Maaa 136,200,76
Maaa 140,204,40
s_nop 0
TR 192,4,4256
TR 194,4,4768
Maaa 140,208,60
Maaa 144,204,44
Maaa 144,208,64
Maaa 148,204,48
Maaa 148,208,68
s_nop 0
TR 196,4,8448
TR 198,4,8960
Maaa 152,204,52
Maaa 152,208,72
Maaa 156,204,56
Maaa 156,208,76
s_waitcnt vmcnt(5)
Mava 120,12,160
Mava 140,10,160
s_waitcnt vmcnt(4)
Mava 124,12,164
Mava 144,10,164
s_waitcnt vmcnt(3)
Mava 128,12,168
Mava 148,10,168
s_waitcnt vmcnt(2)
Mava 132,12,172
Mava 152,10,172
s_waitcnt vmcnt(1)
Mava 136,12,176
Mava 156,10,176
WL 6
Maavz 80,180,216
s_nop 0
TR 200,4,12672
TR 202,4,13184
Maav 80,184,236
buffer_load_dwordx2 v[24:25],off,s[56:59],s44 sc1
Maavz 84,180,220
Maav 84,184,240
AR 184,120
AR 185,121
Maavz 88,180,224
AR 186,122
AR 187,123
Maav 88,184,244
s_nop 0
TR 204,4,8480
TR 206,4,8992
Maavz 92,180,228
AR 188,140
AR 189,141
Maav 92,184,248
AR 190,142
AR 191,143
Maavz 96,180,232
CV 184,184,185
CV 185,186,187
Maav 96,184,252
WL 6
CV 186,188,189
CV 187,190,191
Maavz 100,188,216
s_add_u32 s23,s74,143360
TR 208,4,12704
TR 210,4,13216
Maav 100,192,236
buffer_store_dwordx4 v[184:187],v2,s[48:51],s23 offen sc1
Maavz 104,188,220
AR 176,124
AR 177,125
Maav 104,192,240
AR 178,126
AR 179,127
Maavz 108,188,224
AR 180,144
AR 181,145
Maav 108,192,244
s_nop 0
TR 180,6,0
TR 182,6,512
Maavz 112,188,228
AR 182,146
AR 183,147
Maav 112,192,248
CV 176,176,177
CV 177,178,179
Maavz 116,188,232
CV 178,180,181
CV 179,182,183
Maav 116,192,252
WL 6
Maaa 80,196,216
s_add_u32 s23,s74,144384
TR 184,6,4224
TR 186,6,4736
Maaa 80,200,236
buffer_store_dwordx4 v[176:179],v2,s[48:51],s23 offen sc1
Maaa 84,196,220
AR 184,128
AR 185,129
Maaa 84,200,240
AR 186,130
AR 187,131
Maaa 88,196,224
AR 188,148
AR 189,149
Maaa 88,200,244
s_nop 0
TR 188,6,32
TR 190,6,544
Maaa 92,196,228
AR 190,150
AR 191,151
Maaa 92,200,248
CV 184,184,185
CV 185,186,187
Maaa 96,196,232
CV 186,188,189
CV 187,190,191
Maaa 96,200,252
WL 6
Maaa 100,204,216
s_add_u32 s23,s74,145408
TR 192,6,4256
TR 194,6,4768
Maaa 100,208,236
buffer_store_dwordx4 v[184:187],v2,s[48:51],s23 offen sc1
Maaa 104,204,220
AR 176,132
AR 177,133
Maaa 104,208,240
AR 178,134
AR 179,135
Maaa 108,204,224
AR 180,152
AR 181,153
Maaa 108,208,244
s_nop 0
TR 196,6,8448
TR 198,6,8960
Maaa 112,204,228
AR 182,154
AR 183,155
Maaa 112,208,248
CV 176,176,177
CV 177,178,179
Maaa 116,204,232
CV 178,180,181
CV 179,182,183
Maaa 116,208,252
WL 6
Maav 80,180,36
s_add_u32 s23,s74,146432
TR 200,6,12672
TR 202,6,13184
Maav 80,184,56
buffer_store_dwordx4 v[176:179],v2,s[48:51],s23 offen sc1
Maav 84,180,40
AR 184,136
AR 185,137
Maav 84,184,60
AR 186,138
AR 187,139
Maav 88,180,44
AR 188,156
AR 189,157
Maav 88,184,64
s_nop 0
TR 204,6,8480
TR 206,6,8992
Maav 92,180,48
AR 190,158
AR 191,159
Maav 92,184,68
CV 184,184,185
CV 185,186,187
Maav 96,180,52
CV 186,188,189
CV 187,190,191
Maav 96,184,72
WL 6
Maav 100,188,36
s_add_u32 s23,s74,147456
TR 208,6,12704
TR 210,6,13216
Maav 100,192,56
buffer_store_dwordx4 v[184:187],v2,s[48:51],s23 offen sc1
Maav 104,188,40
Maav 104,192,60
Maav 108,188,44
Maav 108,192,64
Maav 112,188,48
Maav 112,192,68
Maav 116,188,52
Maav 116,192,72
WL 4
Maav 80,196,76
WL 0
s_barrier
Maav 80,200,96
s_nop 0
TR 180,4,34624
TR 182,4,35136
Maav 84,196,80
s_nop 0
TR 184,4,38848
TR 186,4,39360
Maav 84,200,100
s_add_u32 s16,s88,7
s_waitcnt vmcnt(5)
v_cmp_lt_u64 vcc,v[24:25],s[16:17]
v_cmp_gt_u64 s[20:21],v[24:25],s[18:19]
s_nop 0
s_or_b64 s[20:21],s[20:21],vcc
s_cbranch_scc1 LC_spin5
LC_sb5:
Maav 88,196,84
s_add_u32 s76,s72,122880
Maav 88,200,104
s_nop 0
TR 188,4,34656
TR 190,4,35168
Maav 92,196,88
buffer_load_dwordx4 a[160:163],v2,s[48:51],s76 offen sc1
Maav 92,200,108
Maav 96,196,92
Maav 96,200,112
buffer_load_dwordx4 a[164:167],v2,s[48:51],s76 offen offset:1024 sc1
Maav 100,204,76
s_nop 0
TR 192,4,38880
TR 194,4,39392
Maav 100,208,96
Maav 104,204,80
buffer_load_dwordx4 a[168:171],v2,s[48:51],s76 offen offset:2048 sc1
Maav 104,208,100
Maav 108,204,84
Maav 108,208,104
s_nop 0
TR 196,4,43072
TR 198,4,43584
Maav 112,204,88
buffer_load_dwordx4 a[172:175],v2,s[48:51],s76 offen offset:3072 sc1
Maav 112,208,108
s_add_u32 s76,s72,126976
Maav 116,204,92
Maav 116,208,112
WL 6
buffer_load_dwordx4 a[176:179],v2,s[48:51],s76 offen sc1
Maav 80,180,116
s_nop 0
TR 200,4,47296
TR 202,4,47808
Maav 80,184,136
Maav 84,180,120
Maav 84,184,140
Maav 88,180,124
Maav 88,184,144
s_nop 0
TR 204,4,43104
TR 206,4,43616
Maav 92,180,128
v_add_u32_e64 v194,s88,8
Maav 92,184,148
Maav 96,180,132
Maav 96,184,152
WL 6
Maav 100,188,116
s_nop 0
TR 208,4,47328
TR 210,4,47840
Maav 100,192,136
s_waitcnt vmcnt(5)
s_mov_b64 exec,1
buffer_store_dwordx2 v[194:195],off,s[56:59],s81 sc1
s_mov_b64 exec,-1
Maav 104,188,120
Maav 104,192,140
Maav 108,188,124
Maav 108,192,144
s_nop 0
TR 180,6,34624
TR 182,6,35136
Maav 112,188,128
Maav 112,192,148
Maav 116,188,132
Maav 116,192,152
WL 6
Maav 80,196,196
s_nop 0
TR 184,6,38848
TR 186,6,39360
Maav 80,200,156
buffer_load_dwordx2 v[24:25],off,s[56:59],s45 sc1
Maav 84,196,200
Maav 84,200,160
Maav 88,196,204
Maav 88,200,164
s_nop 0
TR 188,6,34656
TR 190,6,35168
Maav 92,196,208
Maav 92,200,168
Maav 96,196,212
Maav 96,200,172
WL 6
Maav 100,204,196
s_nop 0
TR 192,6,38880
TR 194,6,39392
Maav 100,208,156
Maav 104,204,200
Maav 104,208,160
Maav 108,204,204
Maav 108,208,164
s_nop 0
TR 196,6,43072
TR 198,6,43584
Maav 112,204,208
Maav 112,208,168
Maav 116,204,212
Maav 116,208,172
WL 6
Maaa 80,180,0
s_nop 0
TR 200,6,47296
TR 202,6,47808
Maaa 80,184,20
Maaa 84,180,4
Maaa 84,184,24
Maaa 88,180,8
Maaa 88,184,28
s_nop 0
TR 204,6,43104
TR 206,6,43616
Maaa 92,180,12
Maaa 92,184,32
Maaa 96,180,16
Maaa 96,184,36
WL 6
Maaa 100,188,0
s_nop 0
TR 208,6,47328
TR 210,6,47840
Maaa 100,192,20
s_add_u32 s16,s88,8
s_waitcnt vmcnt(0)
v_cmp_lt_u64 vcc,v[24:25],s[16:17]
v_cmp_gt_u64 s[20:21],v[24:25],s[18:19]
s_nop 0
s_or_b64 s[20:21],s[20:21],vcc
s_cbranch_scc1 LC_spin6
LC_sb6:
Maaa 104,188,4
s_add_u32 s76,s73,143360
Maaa 104,192,24
buffer_load_dwordx4 v[216:219],v2,s[48:51],s76 offen sc1
Maaa 108,188,8
Maaa 108,192,28
Maaa 112,188,12
buffer_load_dwordx4 v[220:223],v2,s[48:51],s76 offen offset:1024 sc1
Maaa 112,192,32
Maaa 116,188,16
Maaa 116,192,36
WL 4
buffer_load_dwordx4 v[224:227],v2,s[48:51],s76 offen offset:2048 sc1
Maaa 80,196,40
Maaa 80,200,60
Maaa 84,196,44
buffer_load_dwordx4 v[228:231],v2,s[48:51],s76 offen offset:3072 sc1
Maaa 84,200,64
s_add_u32 s76,s73,147456
Maaa 88,196,48
Maaa 88,200,68
buffer_load_dwordx4 v[232:235],v2,s[48:51],s76 offen sc1
Maaa 92,196,52
Maaa 92,200,72
Maaa 96,196,56
Maaa 96,200,76
WL 0
Maaa 100,204,40
Maaa 100,208,60
Maaa 104,204,44
Maaa 104,208,64
Maaa 108,204,48
Maaa 108,208,68
Maaa 112,204,52
Maaa 112,208,72
Maaa 116,204,56
Maaa 116,208,76
Mava 80,12,160
Mava 100,10,160
Mava 84,12,164
Mava 104,10,164
Mava 88,12,168
Mava 108,10,168
Mava 92,12,172
Mava 112,10,172
Mava 96,12,176
Mava 116,10,176
s_waitcnt vmcnt(4)
Mavv 80,12,216
Mavv 100,10,216
s_waitcnt vmcnt(3)
Mavv 84,12,220
Mavv 104,10,220
s_waitcnt vmcnt(2)
Mavv 88,12,224
Mavv 108,10,224
s_waitcnt vmcnt(1)
Mavv 92,12,228
Mavv 112,10,228
s_waitcnt vmcnt(0)
Mavv 96,12,232
Mavv 116,10,232
AR 116,80
AR 117,81
AR 118,82
AR 119,83
AR 120,100
AR 121,101
AR 122,102
AR 123,103
CV 116,116,117
CV 117,118,119
CV 118,120,121
CV 119,122,123
s_add_u32 s23,s47,0
v_permlane16_swap_b32 v116,v118
v_permlane16_swap_b32 v117,v119
buffer_store_dwordx4 v[116:119],v192,s[52:55],s23 offen
AR 124,84
AR 125,85
AR 126,86
AR 127,87
AR 128,104
AR 129,105
AR 130,106
AR 131,107
CV 124,124,125
CV 125,126,127
CV 126,128,129
CV 127,130,131
s_add_u32 s23,s47,18432
v_permlane16_swap_b32 v124,v126
v_permlane16_swap_b32 v125,v127
buffer_store_dwordx4 v[124:127],v192,s[52:55],s23 offen
AR 132,88
AR 133,89
AR 134,90
AR 135,91
AR 136,108
AR 137,109
AR 138,110
AR 139,111
CV 132,132,133
CV 133,134,135
CV 134,136,137
CV 135,138,139
s_add_u32 s23,s47,36864
v_permlane16_swap_b32 v132,v134
v_permlane16_swap_b32 v133,v135
buffer_store_dwordx4 v[132:135],v192,s[52:55],s23 offen
AR 140,92
AR 141,93
AR 142,94
AR 143,95
AR 144,112
AR 145,113
AR 146,114
AR 147,115
CV 140,140,141
CV 141,142,143
CV 142,144,145
CV 143,146,147
s_add_u32 s23,s47,55296
v_permlane16_swap_b32 v140,v142
v_permlane16_swap_b32 v141,v143
buffer_store_dwordx4 v[140:143],v192,s[52:55],s23 offen
AR 148,96
AR 149,97
AR 150,98
AR 151,99
AR 152,116
AR 153,117
AR 154,118
AR 155,119
CV 148,148,149
CV 149,150,151
CV 150,152,153
CV 151,154,155
s_add_u32 s23,s47,73728
v_permlane16_swap_b32 v148,v150
v_permlane16_swap_b32 v149,v151
buffer_store_dwordx4 v[148:151],v192,s[52:55],s23 offen
s_endpgm
LC_safe0:
s_nop 7
s_mov_b32 s93,m0
s_waitcnt vmcnt(0) lgkmcnt(0)
v_mov_b32 v22,0xff800000
v_mov_b32 v23,0xff800000
v_mov_b32 v24,0xff800000
v_mov_b32 v25,0xff800000
v_mov_b32 v26,0xff800000
v_subrev_u32 v27,20480,v10
s_mov_b32 s90,0
s_mov_b32 s91,16
LC_sloop0:
s_barrier
s_add_u32 s92,s90,131072
M0 78,0
s_nop 0
DM 19,32,90,0
M0 78,4224
s_nop 0
DM 33,32,90,0
M0 78,8448
s_nop 0
DM 19,32,92,0
M0 78,12672
s_nop 0
DM 33,32,92,0
s_waitcnt vmcnt(0)
s_barrier
RA 40,27,0
RA 44,27,64
RA 48,27,128
RA 52,27,192
RA 56,27,512
RA 60,27,576
RA 64,27,640
RA 68,27,704
WL 0
Mvavz 156,40,36
Mvavz 160,40,52
Mvavz 164,40,68
Mvavz 168,40,84
Mvavz 172,40,100
Mvav 156,44,40
Mvav 160,44,56
Mvav 164,44,72
Mvav 168,44,88
Mvav 172,44,104
Mvav 156,48,44
Mvav 160,48,60
Mvav 164,48,76
Mvav 168,48,92
Mvav 172,48,108
Mvav 156,52,48
Mvav 160,52,64
Mvav 164,52,80
Mvav 168,52,96
Mvav 172,52,112
Mvavz 176,56,36
Mvavz 180,56,52
Mvavz 184,56,68
Mvavz 188,56,84
Mvavz 192,56,100
Mvav 176,60,40
Mvav 180,60,56
Mvav 184,60,72
Mvav 188,60,88
Mvav 192,60,104
Mvav 176,64,44
Mvav 180,64,60
Mvav 184,64,76
Mvav 188,64,92
Mvav 192,64,108
Mvav 176,68,48
Mvav 180,68,64
Mvav 184,68,80
Mvav 188,68,96
Mvav 192,68,112
s_nop 7
v_max3_f32 v22,v22,v156,v157
v_max3_f32 v22,v22,v158,v159
v_max3_f32 v23,v23,v160,v161
v_max3_f32 v23,v23,v162,v163
v_max3_f32 v24,v24,v164,v165
v_max3_f32 v24,v24,v166,v167
v_max3_f32 v25,v25,v168,v169
v_max3_f32 v25,v25,v170,v171
v_max3_f32 v26,v26,v172,v173
v_max3_f32 v26,v26,v174,v175
v_max3_f32 v22,v22,v176,v177
v_max3_f32 v22,v22,v178,v179
v_max3_f32 v23,v23,v180,v181
v_max3_f32 v23,v23,v182,v183
v_max3_f32 v24,v24,v184,v185
v_max3_f32 v24,v24,v186,v187
v_max3_f32 v25,v25,v188,v189
v_max3_f32 v25,v25,v190,v191
v_max3_f32 v26,v26,v192,v193
v_max3_f32 v26,v26,v194,v195
s_add_u32 s90,s90,8192
s_sub_u32 s91,s91,1
s_cmp_lg_u32 s91,0
s_cbranch_scc1 LC_sloop0
s_barrier
s_mov_b32 m0,s93
s_nop 3
s_branch LC_back0
LC_safe1:
s_nop 7
s_mov_b32 s93,m0
s_waitcnt vmcnt(0) lgkmcnt(0)
v_mov_b32 v22,0xff800000
v_mov_b32 v23,0xff800000
v_mov_b32 v24,0xff800000
v_mov_b32 v25,0xff800000
v_mov_b32 v26,0xff800000
v_subrev_u32 v27,20480,v10
s_mov_b32 s90,262144
s_mov_b32 s91,16
LC_sloop1:
s_barrier
s_add_u32 s92,s90,131072
M0 78,0
s_nop 0
DM 19,32,90,0
M0 78,4224
s_nop 0
DM 33,32,90,0
M0 78,8448
s_nop 0
DM 19,32,92,0
M0 78,12672
s_nop 0
DM 33,32,92,0
s_waitcnt vmcnt(0)
s_barrier
RA 40,27,0
RA 44,27,64
RA 48,27,128
RA 52,27,192
RA 56,27,512
RA 60,27,576
RA 64,27,640
RA 68,27,704
WL 0
Mvavz 156,40,36
Mvavz 160,40,52
Mvavz 164,40,68
Mvavz 168,40,84
Mvavz 172,40,100
Mvav 156,44,40
Mvav 160,44,56
Mvav 164,44,72
Mvav 168,44,88
Mvav 172,44,104
Mvav 156,48,44
Mvav 160,48,60
Mvav 164,48,76
Mvav 168,48,92
Mvav 172,48,108
Mvav 156,52,48
Mvav 160,52,64
Mvav 164,52,80
Mvav 168,52,96
Mvav 172,52,112
Mvavz 176,56,36
Mvavz 180,56,52
Mvavz 184,56,68
Mvavz 188,56,84
Mvavz 192,56,100
Mvav 176,60,40
Mvav 180,60,56
Mvav 184,60,72
Mvav 188,60,88
Mvav 192,60,104
Mvav 176,64,44
Mvav 180,64,60
Mvav 184,64,76
Mvav 188,64,92
Mvav 192,64,108
Mvav 176,68,48
Mvav 180,68,64
Mvav 184,68,80
Mvav 188,68,96
Mvav 192,68,112
s_nop 7
v_max3_f32 v22,v22,v156,v157
v_max3_f32 v22,v22,v158,v159
v_max3_f32 v23,v23,v160,v161
v_max3_f32 v23,v23,v162,v163
v_max3_f32 v24,v24,v164,v165
v_max3_f32 v24,v24,v166,v167
v_max3_f32 v25,v25,v168,v169
v_max3_f32 v25,v25,v170,v171
v_max3_f32 v26,v26,v172,v173
v_max3_f32 v26,v26,v174,v175
v_max3_f32 v22,v22,v176,v177
v_max3_f32 v22,v22,v178,v179
v_max3_f32 v23,v23,v180,v181
v_max3_f32 v23,v23,v182,v183
v_max3_f32 v24,v24,v184,v185
v_max3_f32 v24,v24,v186,v187
v_max3_f32 v25,v25,v188,v189
v_max3_f32 v25,v25,v190,v191
v_max3_f32 v26,v26,v192,v193
v_max3_f32 v26,v26,v194,v195
s_add_u32 s90,s90,8192
s_sub_u32 s91,s91,1
s_cmp_lg_u32 s91,0
s_cbranch_scc1 LC_sloop1
s_barrier
s_mov_b32 m0,s93
s_nop 3
s_branch LC_back1
LC_redo:
s_waitcnt vmcnt(0) lgkmcnt(0)
s_barrier
s_mov_b32 s85,-1
s_getpc_b64 s[90:91]
LC_rp:
s_sub_u32 s90,s90,LC_rp-L_restart
s_subb_u32 s91,s91,0
s_setpc_b64 s[90:91]
LC_spin0:
s_sub_u32 s99,s99,1
s_cbranch_scc1 LC_to0
s_sleep 1
buffer_load_dwordx2 v[24:25],off,s[56:59],s44 sc1
s_waitcnt vmcnt(0)
v_cmp_lt_u64 vcc,v[24:25],s[16:17]
v_cmp_gt_u64 s[20:21],v[24:25],s[18:19]
s_nop 0
s_or_b64 s[20:21],s[20:21],vcc
s_cbranch_scc1 LC_spin0
s_branch LC_sb0
LC_to0:
s_mov_b32 s99,0
s_branch LC_sb0
LC_spin1:
s_sub_u32 s99,s99,1
s_cbranch_scc1 LC_to1
s_sleep 1
buffer_load_dwordx2 v[24:25],off,s[56:59],s44 sc1
s_waitcnt vmcnt(0)
v_cmp_lt_u64 vcc,v[24:25],s[16:17]
v_cmp_gt_u64 s[20:21],v[24:25],s[18:19]
s_nop 0
s_or_b64 s[20:21],s[20:21],vcc
s_cbranch_scc1 LC_spin1
s_branch LC_sb1
LC_to1:
s_mov_b32 s99,0
s_branch LC_sb1
LC_spin2:
s_sub_u32 s99,s99,1
s_cbranch_scc1 LC_to2
s_sleep 1
buffer_load_dwordx2 v[24:25],off,s[56:59],s44 sc1
s_waitcnt vmcnt(0)
v_cmp_lt_u64 vcc,v[24:25],s[16:17]
v_cmp_gt_u64 s[20:21],v[24:25],s[18:19]
s_nop 0
s_or_b64 s[20:21],s[20:21],vcc
s_cbranch_scc1 LC_spin2
s_branch LC_sb2
LC_to2:
s_mov_b32 s99,0
s_branch LC_sb2
LC_spin3:
s_sub_u32 s99,s99,1
s_cbranch_scc1 LC_to3
s_sleep 1
buffer_load_dwordx2 v[24:25],off,s[56:59],s44 sc1
s_waitcnt vmcnt(0)
v_cmp_lt_u64 vcc,v[24:25],s[16:17]
v_cmp_gt_u64 s[20:21],v[24:25],s[18:19]
s_nop 0
s_or_b64 s[20:21],s[20:21],vcc
s_cbranch_scc1 LC_spin3
s_branch LC_sb3
LC_to3:
s_mov_b32 s99,0
s_branch LC_sb3
LC_spin4:
s_sub_u32 s99,s99,1
s_cbranch_scc1 LC_to4
s_sleep 1
buffer_load_dwordx2 v[24:25],off,s[56:59],s44 sc1
s_waitcnt vmcnt(0)
v_cmp_lt_u64 vcc,v[24:25],s[16:17]
v_cmp_gt_u64 s[20:21],v[24:25],s[18:19]
s_nop 0
s_or_b64 s[20:21],s[20:21],vcc
s_cbranch_scc1 LC_spin4
s_branch LC_sb4
LC_to4:
s_mov_b32 s99,0
s_branch LC_sb4
LC_spin5:
s_sub_u32 s99,s99,1
s_cbranch_scc1 LC_to5
s_sleep 1
buffer_load_dwordx2 v[24:25],off,s[56:59],s44 sc1
s_waitcnt vmcnt(0)
v_cmp_lt_u64 vcc,v[24:25],s[16:17]
v_cmp_gt_u64 s[20:21],v[24:25],s[18:19]
s_nop 0
s_or_b64 s[20:21],s[20:21],vcc
s_cbranch_scc1 LC_spin5
s_branch LC_sb5
LC_to5:
s_mov_b32 s99,0
s_branch LC_sb5
LC_spin6:
s_sub_u32 s99,s99,1
s_cbranch_scc1 LC_to6
s_sleep 1
buffer_load_dwordx2 v[24:25],off,s[56:59],s45 sc1
s_waitcnt vmcnt(0)
v_cmp_lt_u64 vcc,v[24:25],s[16:17]
v_cmp_gt_u64 s[20:21],v[24:25],s[18:19]
s_nop 0
s_or_b64 s[20:21],s[20:21],vcc
s_cbranch_scc1 LC_spin6
s_branch LC_sb6
LC_to6:
s_mov_b32 s99,0
s_branch LC_sb6
LC_spin7:
s_sub_u32 s99,s99,1
s_cbranch_scc1 LC_to7
s_sleep 1
buffer_load_dwordx2 v[24:25],v26,s[56:59],s46 offen sc1
s_waitcnt vmcnt(0)
v_cmp_lt_u64 vcc,v[24:25],s[16:17]
v_cmp_gt_u64 s[20:21],v[24:25],s[18:19]
s_nop 0
s_or_b64 s[20:21],s[20:21],vcc
s_cbranch_scc1 LC_spin7
s_branch LC_sb7
LC_to7:
s_mov_b32 s99,0
s_branch LC_sb7
LC_pc21:
s_memrealtime s[94:95]
WL 0
s_sub_u32 s95,s94,s96
s_cmp_lt_i32 s95,0
s_cbranch_scc1 LC_pc21
s_branch LC_pb21
LC_pc22:
s_memrealtime s[94:95]
WL 0
s_sub_u32 s95,s94,s96
s_cmp_lt_i32 s95,0
s_cbranch_scc1 LC_pc22
s_branch LC_pb22
LC_pc23:
s_memrealtime s[94:95]
WL 0
s_sub_u32 s95,s94,s96
s_cmp_lt_i32 s95,0
s_cbranch_scc1 LC_pc23
s_branch LC_pb23
LC_pc24:
s_memrealtime s[94:95]
WL 0
s_sub_u32 s95,s94,s96
s_cmp_lt_i32 s95,0
s_cbranch_scc1 LC_pc24
s_branch LC_pb24
LC_pc25:
s_memrealtime s[94:95]
WL 0
s_sub_u32 s95,s94,s96
s_cmp_lt_i32 s95,0
s_cbranch_scc1 LC_pc25
s_branch LC_pb25
LC_pc26:
s_memrealtime s[94:95]
WL 0
s_sub_u32 s95,s94,s96
s_cmp_lt_i32 s95,0
s_cbranch_scc1 LC_pc26
s_branch LC_pb26
LC_pc27:
s_memrealtime s[94:95]
WL 0
s_sub_u32 s95,s94,s96
s_cmp_lt_i32 s95,0
s_cbranch_scc1 LC_pc27
s_branch LC_pb27
LC_pc28:
s_memrealtime s[94:95]
WL 0
s_sub_u32 s95,s94,s96
s_cmp_lt_i32 s95,0
s_cbranch_scc1 LC_pc28
s_branch LC_pb28
LC_pc29:
s_memrealtime s[94:95]
WL 0
s_sub_u32 s95,s94,s96
s_cmp_lt_i32 s95,0
s_cbranch_scc1 LC_pc29
s_branch LC_pb29
LC_pc30:
s_memrealtime s[94:95]
WL 0
s_sub_u32 s95,s94,s96
s_cmp_lt_i32 s95,0
s_cbranch_scc1 LC_pc30
s_branch LC_pb30
LC_pc31:
s_memrealtime s[94:95]
WL 0
s_sub_u32 s95,s94,s96
s_cmp_lt_i32 s95,0
s_cbranch_scc1 LC_pc31
s_branch LC_pb31
LC_pc32:
s_memrealtime s[94:95]
WL 0
s_sub_u32 s95,s94,s96
s_cmp_lt_i32 s95,0
s_cbranch_scc1 LC_pc32
s_branch LC_pb32
LC_pc33:
s_memrealtime s[94:95]
WL 0
s_sub_u32 s95,s94,s96
s_cmp_lt_i32 s95,0
s_cbranch_scc1 LC_pc33
s_branch LC_pb33
LC_pc34:
s_memrealtime s[94:95]
WL 0
s_sub_u32 s95,s94,s96
s_cmp_lt_i32 s95,0
s_cbranch_scc1 LC_pc34
s_branch LC_pb34
LC_pc35:
s_memrealtime s[94:95]
WL 0
s_sub_u32 s95,s94,s96
s_cmp_lt_i32 s95,0
s_cbranch_scc1 LC_pc35
s_branch LC_pb35
LC_pc56:
s_memrealtime s[94:95]
WL 0
s_sub_u32 s95,s94,s96
s_cmp_lt_i32 s95,0
s_cbranch_scc1 LC_pc56
s_branch LC_pb56
LC_pc57:
s_memrealtime s[94:95]
WL 0
s_sub_u32 s95,s94,s96
s_cmp_lt_i32 s95,0
s_cbranch_scc1 LC_pc57
s_branch LC_pb57
LC_pc58:
s_memrealtime s[94:95]
WL 0
s_sub_u32 s95,s94,s96
s_cmp_lt_i32 s95,0
s_cbranch_scc1 LC_pc58
s_branch LC_pb58
LC_pc59:
s_memrealtime s[94:95]
WL 0
s_sub_u32 s95,s94,s96
s_cmp_lt_i32 s95,0
s_cbranch_scc1 LC_pc59
s_branch LC_pb59
LC_pc60:
s_memrealtime s[94:95]
WL 0
s_sub_u32 s95,s94,s96
s_cmp_lt_i32 s95,0
s_cbranch_scc1 LC_pc60
s_branch LC_pb60
LC_pc61:
s_memrealtime s[94:95]
WL 0
s_sub_u32 s95,s94,s96
s_cmp_lt_i32 s95,0
s_cbranch_scc1 LC_pc61
s_branch LC_pb61
LC_pc62:
s_memrealtime s[94:95]
WL 0
s_sub_u32 s95,s94,s96
s_cmp_lt_i32 s95,0
s_cbranch_scc1 LC_pc62
s_branch LC_pb62
LC_pc63:
s_memrealtime s[94:95]
WL 0
s_sub_u32 s95,s94,s96
s_cmp_lt_i32 s95,0
s_cbranch_scc1 LC_pc63
s_branch LC_pb63
LC_pc64:
s_memrealtime s[94:95]
WL 0
s_sub_u32 s95,s94,s96
s_cmp_lt_i32 s95,0
s_cbranch_scc1 LC_pc64
s_branch LC_pb64
LC_pc65:
s_memrealtime s[94:95]
WL 0
s_sub_u32 s95,s94,s96
s_cmp_lt_i32 s95,0
s_cbranch_scc1 LC_pc65
s_branch LC_pb65
LC_pc66:
s_memrealtime s[94:95]
WL 0
s_sub_u32 s95,s94,s96
s_cmp_lt_i32 s95,0
s_cbranch_scc1 LC_pc66
s_branch LC_pb66
LC_pc67:
s_memrealtime s[94:95]
WL 0
s_sub_u32 s95,s94,s96
s_cmp_lt_i32 s95,0
s_cbranch_scc1 LC_pc67
s_branch LC_pb67
LB_body:
s_waitcnt vmcnt(14) lgkmcnt(0)
s_barrier
TR 180,14,0
TR 182,14,256
RA 216,15,0
RA 220,15,2048
RA 224,15,4096
RA 228,15,6144
RA 232,15,8192
TR 184,14,8
TR 186,14,264
TR 188,14,64
TR 190,14,320
TR 192,14,72
TR 194,14,328
TR 196,14,128
TR 198,14,384
WL 12
Maaaz 0,180,216
s_nop 0
TR 200,14,136
TR 202,14,392
WL 13
Maaaz 4,180,220
WL 12
Maaaz 8,180,224
WL 11
Maaaz 12,180,228
WL 10
Maaaz 16,180,232
WL 8
Maaaz 20,184,216
s_nop 0
TR 204,14,192
TR 206,14,448
Maaaz 24,184,220
Maaaz 28,184,224
Maaaz 32,184,228
Maaaz 36,184,232
WL 6
Maaaz 40,188,216
s_nop 0
TR 208,14,200
TR 210,14,456
Maaaz 44,188,220
Maaaz 48,188,224
Maaaz 52,188,228
Maaaz 56,188,232
Maaaz 60,192,216
Maaaz 64,192,220
Maaaz 68,192,224
Maaaz 72,192,228
Maaaz 76,192,232
WL 4
Maaaz 80,196,216
s_waitcnt vmcnt(10) lgkmcnt(0)
s_barrier
Maaaz 84,196,220
s_nop 0
TR 180,14,17312
TR 182,14,17568
Maaaz 88,196,224
RA 236,16,0
Maaaz 92,196,228
RA 240,16,2048
Maaaz 96,196,232
RA 244,16,4096
Maaaz 100,200,216
s_mov_b32 s79,786944
RA 248,16,6144
Maaaz 104,200,220
M0 77,89728
RA 252,16,8192
Maaaz 108,200,224
s_nop 0
TR 184,14,17320
TR 186,14,17576
Maaaz 112,200,228
s_nop 0
TR 188,14,17376
TR 190,14,17632
Maaaz 116,200,232
DM 20,28,79,0
Maaaz 120,204,216
s_mov_b32 s79,795136
TR 192,14,17384
TR 194,14,17640
Maaaz 124,204,220
M0 77,90816
Maaaz 128,204,224
DM 20,28,79,0
Maaaz 132,204,228
s_mov_b32 s79,803328
Maaaz 136,204,232
M0 77,91904
Maaaz 140,208,216
s_nop 0
TR 196,14,17440
TR 198,14,17696
Maaaz 144,208,220
DM 20,28,79,0
Maaaz 148,208,224
s_mov_b32 s79,811520
Maaaz 152,208,228
M0 77,92992
Maaaz 156,208,232
WL 6
DM 20,28,79,0
Maaa 0,180,236
s_nop 0
TR 200,14,17448
TR 202,14,17704
Maaa 4,180,240
Maaa 8,180,244
Maaa 12,180,248
Maaa 16,180,252
Maaa 20,184,236
s_nop 0
TR 204,14,17504
TR 206,14,17760
Maaa 24,184,240
Maaa 28,184,244
Maaa 32,184,248
Maaa 36,184,252
WL 6
Maaa 40,188,236
s_nop 0
TR 208,14,17512
TR 210,14,17768
Maaa 44,188,240
Maaa 48,188,244
Maaa 52,188,248
Maaa 56,188,252
Maaa 60,192,236
Maaa 64,192,240
Maaa 68,192,244
Maaa 72,192,248
Maaa 76,192,252
WL 4
Maaa 80,196,236
s_waitcnt vmcnt(5) lgkmcnt(0)
s_barrier
Maaa 84,196,240
s_add_u32 s98,s71,16384
TR 180,14,34624
TR 182,14,34880
Maaa 88,196,244
RA 216,17,0
v_lshl_add_u32 v8,v2,1,s98
Maaa 92,196,248
RA 220,17,2048
Maaa 96,196,252
RA 224,17,4096
Maaa 100,200,236
s_mov_b32 s79,256
RA 228,17,6144
Maaa 104,200,240
M0 69,0
RA 232,17,8192
Maaa 108,200,244
s_nop 0
TR 184,14,34632
TR 186,14,34888
Maaa 112,200,248
s_nop 0
TR 188,14,34688
TR 190,14,34944
Maaa 116,200,252
DM 21,24,79,0
Maaa 120,204,236
s_mov_b32 s79,37120
TR 192,14,34696
TR 194,14,34952
Maaa 124,204,240
M0 69,4096
buffer_load_dword v8,v8,s[60:63],0 offen
Maaa 128,204,244
DM 21,24,79,0
Maaa 132,204,248
s_mov_b32 s79,73984
Maaa 136,204,252
M0 69,8192
Maaa 140,208,236
s_nop 0
TR 196,14,34752
TR 198,14,35008
Maaa 144,208,240
DM 21,24,79,0
Maaa 148,208,244
s_mov_b32 s79,110848
Maaa 152,208,248
M0 69,12288
Maaa 156,208,252
WL 6
DM 21,24,79,0
Maaa 0,180,216
s_mov_b32 s79,147712
TR 200,14,34760
TR 202,14,35016
Maaa 4,180,220
M0 69,16384
Maaa 8,180,224
DM 21,24,79,0
Maaa 12,180,228
s_mov_b32 s79,1049088
Maaa 16,180,232
M0 77,107040
Maaa 20,184,216
s_nop 0
TR 204,14,34816
TR 206,14,35072
Maaa 24,184,220
DM 20,28,79,0
Maaa 28,184,224
s_mov_b32 s79,1057280
Maaa 32,184,228
M0 77,108128
Maaa 36,184,232
WL 6
DM 20,28,79,0
Maaa 40,188,216
s_mov_b32 s79,1065472
TR 208,14,34824
TR 210,14,35080
Maaa 44,188,220
M0 77,109216
Maaa 48,188,224
DM 20,28,79,0
Maaa 52,188,228
s_mov_b32 s79,1073664
Maaa 56,188,232
M0 77,110304
Maaa 60,192,216
DM 20,28,79,0
Maaa 64,192,220
Maaa 68,192,224
Maaa 72,192,228
Maaa 76,192,232
WL 4
Maaa 80,196,216
s_waitcnt vmcnt(10) lgkmcnt(0)
s_barrier
Maaa 84,196,220
s_nop 0
TR 180,14,0
TR 182,14,256
Maaa 88,196,224
RA 236,18,0
Maaa 92,196,228
RA 240,18,2048
Maaa 96,196,232
RA 244,18,4096
Maaa 100,200,216
s_mov_b32 s79,1311232
RA 248,18,6144
Maaa 104,200,220
M0 77,124352
RA 252,18,8192
Maaa 108,200,224
s_nop 0
TR 184,14,8
TR 186,14,264
Maaa 112,200,228
s_nop 0
TR 188,14,64
TR 190,14,320
Maaa 116,200,232
DM 20,28,79,0
Maaa 120,204,216
s_mov_b32 s79,1319424
TR 192,14,72
TR 194,14,328
Maaa 124,204,220
M0 77,125440
Maaa 128,204,224
DM 20,28,79,0
Maaa 132,204,228
s_mov_b32 s79,1327616
Maaa 136,204,232
M0 77,126528
Maaa 140,208,216
s_nop 0
TR 196,14,128
TR 198,14,384
Maaa 144,208,220
DM 20,28,79,0
Maaa 148,208,224
s_mov_b32 s79,1335808
Maaa 152,208,228
M0 77,127616
Maaa 156,208,232
WL 6
DM 20,28,79,0
Maaa 0,180,236
s_nop 0
TR 200,14,136
TR 202,14,392
Maaa 4,180,240
Maaa 8,180,244
Maaa 12,180,248
Maaa 16,180,252
Maaa 20,184,236
s_nop 0
TR 204,14,192
TR 206,14,448
Maaa 24,184,240
Maaa 28,184,244
Maaa 32,184,248
Maaa 36,184,252
WL 6
Maaa 40,188,236
s_nop 0
TR 208,14,200
TR 210,14,456
Maaa 44,188,240
Maaa 48,188,244
Maaa 52,188,248
Maaa 56,188,252
Maaa 60,192,236
Maaa 64,192,240
Maaa 68,192,244
Maaa 72,192,248
Maaa 76,192,252
WL 4
Maaa 80,196,236
s_waitcnt vmcnt(4) lgkmcnt(0)
s_barrier
Maaa 84,196,240
s_nop 0
TR 180,14,17312
TR 182,14,17568
Maaa 88,196,244
RA 216,15,0
Maaa 92,196,248
RA 220,15,2048
Maaa 96,196,252
RA 224,15,4096
Maaa 100,200,236
s_mov_b32 s79,384
RA 228,15,6144
Maaa 104,200,240
M0 69,141664
RA 232,15,8192
Maaa 108,200,244
s_nop 0
TR 184,14,17320
TR 186,14,17576
Maaa 112,200,248
s_nop 0
TR 188,14,17376
TR 190,14,17632
Maaa 116,200,252
DM 21,24,79,0
Maaa 120,204,236
s_mov_b32 s79,37248
TR 192,14,17384
TR 194,14,17640
Maaa 124,204,240
M0 69,145760
Maaa 128,204,244
DM 21,24,79,0
Maaa 132,204,248
s_mov_b32 s79,74112
Maaa 136,204,252
M0 69,149856
Maaa 140,208,236
s_nop 0
TR 196,14,17440
TR 198,14,17696
Maaa 144,208,240
DM 21,24,79,0
Maaa 148,208,244
s_mov_b32 s79,110976
Maaa 152,208,248
M0 69,153952
Maaa 156,208,252
WL 6
DM 21,24,79,0
v_readfirstlane_b32 s90,v28
v_readfirstlane_b32 s91,v29
s_nop 0
s_add_u32 s90,s90,0x7f4a7c15
s_addc_u32 s91,s91,0x9e3779b9
s_and_b32 s90,s90,0xffffffe0
s_cmp_eq_u32 s85,0
s_cselect_b64 s[88:89],s[90:91],s[88:89]
Maaa 0,180,216
s_mov_b32 s79,147840
TR 200,14,17448
TR 202,14,17704
Maaa 4,180,220
M0 69,158048
Maaa 8,180,224
DM 21,24,79,0
Maaa 12,180,228
s_mov_b32 s79,1573376
Maaa 16,180,232
M0 77,89728
Maaa 20,184,216
s_nop 0
TR 204,14,17504
TR 206,14,17760
Maaa 24,184,220
DM 20,28,79,0
Maaa 28,184,224
s_mov_b32 s79,1581568
Maaa 32,184,228
M0 77,90816
Maaa 36,184,232
WL 6
DM 20,28,79,0
Maaa 40,188,216
s_mov_b32 s79,1589760
TR 208,14,17512
TR 210,14,17768
Maaa 44,188,220
M0 77,91904
Maaa 48,188,224
DM 20,28,79,0
Maaa 52,188,228
s_mov_b32 s79,1597952
Maaa 56,188,232
M0 77,92992
Maaa 60,192,216
DM 20,28,79,0
Maaa 64,192,220
Maaa 68,192,224
Maaa 72,192,228
Maaa 76,192,232
WL 4
Maaa 80,196,216
s_waitcnt vmcnt(9) lgkmcnt(0)
s_barrier
Maaa 84,196,220
s_nop 0
TR 180,14,34624
TR 182,14,34880
Maaa 88,196,224
RA 236,16,0
Maaa 92,196,228
RA 240,16,2048
Maaa 96,196,232
RA 244,16,4096
Maaa 100,200,216
s_mov_b32 s79,1835520
RA 248,16,6144
Maaa 104,200,220
M0 77,107040
RA 252,16,8192
Maaa 108,200,224
s_nop 0
TR 184,14,34632
TR 186,14,34888
Maaa 112,200,228
s_nop 0
TR 188,14,34688
TR 190,14,34944
Maaa 116,200,232
DM 20,28,79,0
Maaa 120,204,216
s_mov_b32 s79,1843712
TR 192,14,34696
TR 194,14,34952
Maaa 124,204,220
M0 77,108128
Maaa 128,204,224
DM 20,28,79,0
Maaa 132,204,228
s_mov_b32 s79,1851904
Maaa 136,204,232
M0 77,109216
Maaa 140,208,216
s_nop 0
TR 196,14,34752
TR 198,14,35008
Maaa 144,208,220
DM 20,28,79,0
Maaa 148,208,224
s_mov_b32 s79,1860096
Maaa 152,208,228
M0 77,110304
Maaa 156,208,232
WL 6
DM 20,28,79,0
Maaa 0,180,236
s_nop 0
TR 200,14,34760
TR 202,14,35016
Maaa 4,180,240
Maaa 8,180,244
Maaa 12,180,248
Maaa 16,180,252
Maaa 20,184,236
s_nop 0
TR 204,14,34816
TR 206,14,35072
Maaa 24,184,240
Maaa 28,184,244
Maaa 32,184,248
Maaa 36,184,252
WL 6
Maaa 40,188,236
s_nop 0
TR 208,14,34824
TR 210,14,35080
Maaa 44,188,240
Maaa 48,188,244
Maaa 52,188,248
Maaa 56,188,252
Maaa 60,192,236
Maaa 64,192,240
Maaa 68,192,244
Maaa 72,192,248
Maaa 76,192,252
WL 4
Maaa 80,196,236
s_waitcnt vmcnt(4) lgkmcnt(0)
s_barrier
Maaa 84,196,240
s_nop 0
TR 180,14,0
TR 182,14,256
Maaa 88,196,244
RA 216,17,0
Maaa 92,196,248
RA 220,17,2048
Maaa 96,196,252
RA 224,17,4096
Maaa 100,200,236
s_mov_b32 s79,512
RA 228,17,6144
Maaa 104,200,240
M0 69,0
RA 232,17,8192
Maaa 108,200,244
s_nop 0
TR 184,14,8
TR 186,14,264
Maaa 112,200,248
s_nop 0
TR 188,14,64
TR 190,14,320
Maaa 116,200,252
DM 21,24,79,0
Maaa 120,204,236
s_mov_b32 s79,37376
TR 192,14,72
TR 194,14,328
Maaa 124,204,240
M0 69,4096
Maaa 128,204,244
DM 21,24,79,0
Maaa 132,204,248
s_mov_b32 s79,74240
Maaa 136,204,252
M0 69,8192
Maaa 140,208,236
s_nop 0
TR 196,14,128
TR 198,14,384
Maaa 144,208,240
DM 21,24,79,0
Maaa 148,208,244
s_mov_b32 s79,111104
Maaa 152,208,248
M0 69,12288
Maaa 156,208,252
WL 6
DM 21,24,79,0
Maaa 0,180,216
s_mov_b32 s79,147968
TR 200,14,136
TR 202,14,392
Maaa 4,180,220
M0 69,16384
Maaa 8,180,224
DM 21,24,79,0
Maaa 12,180,228
s_mov_b32 s79,2097664
Maaa 16,180,232
M0 77,124352
Maaa 20,184,216
s_nop 0
TR 204,14,192
TR 206,14,448
Maaa 24,184,220
DM 20,28,79,0
Maaa 28,184,224
s_mov_b32 s79,2105856
Maaa 32,184,228
M0 77,125440
Maaa 36,184,232
WL 6
DM 20,28,79,0
Maaa 40,188,216
s_mov_b32 s79,2114048
TR 208,14,200
TR 210,14,456
Maaa 44,188,220
M0 77,126528
Maaa 48,188,224
DM 20,28,79,0
Maaa 52,188,228
s_mov_b32 s79,2122240
Maaa 56,188,232
M0 77,127616
Maaa 60,192,216
DM 20,28,79,0
Maaa 64,192,220
Maaa 68,192,224
Maaa 72,192,228
Maaa 76,192,232
WL 4
Maaa 80,196,216
s_waitcnt vmcnt(9) lgkmcnt(0)
s_barrier
Maaa 84,196,220
s_nop 0
TR 180,14,17312
TR 182,14,17568
Maaa 88,196,224
RA 236,18,0
Maaa 92,196,228
RA 240,18,2048
Maaa 96,196,232
RA 244,18,4096
Maaa 100,200,216
s_mov_b32 s79,2359808
RA 248,18,6144
Maaa 104,200,220
M0 77,89728
RA 252,18,8192
Maaa 108,200,224
s_nop 0
TR 184,14,17320
TR 186,14,17576
Maaa 112,200,228
s_nop 0
TR 188,14,17376
TR 190,14,17632
Maaa 116,200,232
DM 20,28,79,0
Maaa 120,204,216
s_mov_b32 s79,2368000
TR 192,14,17384
TR 194,14,17640
Maaa 124,204,220
M0 77,90816
Maaa 128,204,224
DM 20,28,79,0
Maaa 132,204,228
s_mov_b32 s79,2376192
Maaa 136,204,232
M0 77,91904
Maaa 140,208,216
s_nop 0
TR 196,14,17440
TR 198,14,17696
Maaa 144,208,220
DM 20,28,79,0
Maaa 148,208,224
s_mov_b32 s79,2384384
Maaa 152,208,228
M0 77,92992
Maaa 156,208,232
WL 6
DM 20,28,79,0
Maaa 0,180,236
s_nop 0
TR 200,14,17448
TR 202,14,17704
Maaa 4,180,240
Maaa 8,180,244
Maaa 12,180,248
Maaa 16,180,252
Maaa 20,184,236
s_nop 0
TR 204,14,17504
TR 206,14,17760
Maaa 24,184,240
Maaa 28,184,244
Maaa 32,184,248
Maaa 36,184,252
WL 6
Maaa 40,188,236
s_nop 0
TR 208,14,17512
TR 210,14,17768
Maaa 44,188,240
Maaa 48,188,244
Maaa 52,188,248
Maaa 56,188,252
Maaa 60,192,236
Maaa 64,192,240
Maaa 68,192,244
Maaa 72,192,248
Maaa 76,192,252
WL 4
Maaa 80,196,236
s_waitcnt vmcnt(4) lgkmcnt(0)
s_barrier
Maaa 84,196,240
s_nop 0
TR 180,14,34624
TR 182,14,34880
Maaa 88,196,244
RA 216,15,0
Maaa 92,196,248
RA 220,15,2048
Maaa 96,196,252
RA 224,15,4096
Maaa 100,200,236
s_mov_b32 s79,640
RA 228,15,6144
Maaa 104,200,240
M0 69,141664
RA 232,15,8192
Maaa 108,200,244
s_nop 0
TR 184,14,34632
TR 186,14,34888
Maaa 112,200,248
s_nop 0
TR 188,14,34688
TR 190,14,34944
Maaa 116,200,252
DM 21,24,79,0
Maaa 120,204,236
s_mov_b32 s79,37504
TR 192,14,34696
TR 194,14,34952
Maaa 124,204,240
M0 69,145760
Maaa 128,204,244
DM 21,24,79,0
Maaa 132,204,248
s_mov_b32 s79,74368
Maaa 136,204,252
M0 69,149856
Maaa 140,208,236
s_nop 0
TR 196,14,34752
TR 198,14,35008
Maaa 144,208,240
DM 21,24,79,0
Maaa 148,208,244
s_mov_b32 s79,111232
Maaa 152,208,248
M0 69,153952
Maaa 156,208,252
WL 6
DM 21,24,79,0
Maaa 0,180,216
s_mov_b32 s79,148096
TR 200,14,34760
TR 202,14,35016
Maaa 4,180,220
M0 69,158048
Maaa 8,180,224
DM 21,24,79,0
Maaa 12,180,228
s_mov_b32 s79,2621952
Maaa 16,180,232
M0 77,107040
Maaa 20,184,216
s_nop 0
TR 204,14,34816
TR 206,14,35072
Maaa 24,184,220
DM 20,28,79,0
Maaa 28,184,224
s_mov_b32 s79,2630144
Maaa 32,184,228
M0 77,108128
Maaa 36,184,232
WL 6
DM 20,28,79,0
Maaa 40,188,216
s_mov_b32 s79,2638336
TR 208,14,34824
TR 210,14,35080
Maaa 44,188,220
M0 77,109216
Maaa 48,188,224
DM 20,28,79,0
Maaa 52,188,228
s_mov_b32 s79,2646528
Maaa 56,188,232
M0 77,110304
Maaa 60,192,216
DM 20,28,79,0
Maaa 64,192,220
Maaa 68,192,224
Maaa 72,192,228
Maaa 76,192,232
WL 4
Maaa 80,196,216
s_waitcnt vmcnt(9) lgkmcnt(0)
s_barrier
Maaa 84,196,220
s_nop 0
TR 180,14,0
TR 182,14,256
Maaa 88,196,224
RA 236,16,0
Maaa 92,196,228
RA 240,16,2048
Maaa 96,196,232
RA 244,16,4096
Maaa 100,200,216
s_mov_b32 s79,2884096
RA 248,16,6144
Maaa 104,200,220
M0 77,124352
RA 252,16,8192
Maaa 108,200,224
s_nop 0
TR 184,14,8
TR 186,14,264
Maaa 112,200,228
s_nop 0
TR 188,14,64
TR 190,14,320
Maaa 116,200,232
DM 20,28,79,0
Maaa 120,204,216
s_mov_b32 s79,2892288
TR 192,14,72
TR 194,14,328
Maaa 124,204,220
M0 77,125440
Maaa 128,204,224
DM 20,28,79,0
Maaa 132,204,228
s_mov_b32 s79,2900480
Maaa 136,204,232
M0 77,126528
Maaa 140,208,216
s_nop 0
TR 196,14,128
TR 198,14,384
Maaa 144,208,220
DM 20,28,79,0
Maaa 148,208,224
s_mov_b32 s79,2908672
Maaa 152,208,228
M0 77,127616
Maaa 156,208,232
WL 6
DM 20,28,79,0
Maaa 0,180,236
s_nop 0
TR 200,14,136
TR 202,14,392
Maaa 4,180,240
Maaa 8,180,244
Maaa 12,180,248
Maaa 16,180,252
Maaa 20,184,236
s_nop 0
TR 204,14,192
TR 206,14,448
Maaa 24,184,240
Maaa 28,184,244
Maaa 32,184,248
Maaa 36,184,252
WL 6
Maaa 40,188,236
s_nop 0
TR 208,14,200
TR 210,14,456
Maaa 44,188,240
Maaa 48,188,244
Maaa 52,188,248
Maaa 56,188,252
Maaa 60,192,236
Maaa 64,192,240
Maaa 68,192,244
Maaa 72,192,248
Maaa 76,192,252
WL 4
Maaa 80,196,236
s_waitcnt vmcnt(4) lgkmcnt(0)
s_barrier
Maaa 84,196,240
s_nop 0
TR 180,14,17312
TR 182,14,17568
Maaa 88,196,244
RA 216,17,0
Maaa 92,196,248
RA 220,17,2048
Maaa 96,196,252
RA 224,17,4096
Maaa 100,200,236
s_mov_b32 s79,768
RA 228,17,6144
Maaa 104,200,240
M0 69,0
RA 232,17,8192
Maaa 108,200,244
s_nop 0
TR 184,14,17320
TR 186,14,17576
Maaa 112,200,248
s_nop 0
TR 188,14,17376
TR 190,14,17632
Maaa 116,200,252
DM 21,24,79,0
Maaa 120,204,236
s_mov_b32 s79,37632
TR 192,14,17384
TR 194,14,17640
Maaa 124,204,240
M0 69,4096
Maaa 128,204,244
DM 21,24,79,0
Maaa 132,204,248
s_mov_b32 s79,74496
Maaa 136,204,252
M0 69,8192
Maaa 140,208,236
s_nop 0
TR 196,14,17440
TR 198,14,17696
Maaa 144,208,240
DM 21,24,79,0
Maaa 148,208,244
s_mov_b32 s79,111360
Maaa 152,208,248
M0 69,12288
Maaa 156,208,252
WL 6
DM 21,24,79,0
Maaa 0,180,216
s_mov_b32 s79,148224
TR 200,14,17448
TR 202,14,17704
Maaa 4,180,220
M0 69,16384
s_add_u32 s98,s71,24576
v_lshl_add_u32 v8,v2,1,s98
Maaa 8,180,224
DM 21,24,79,0
Maaa 12,180,228
s_mov_b32 s79,3146240
Maaa 16,180,232
M0 77,89728
Maaa 20,184,216
s_nop 0
TR 204,14,17504
TR 206,14,17760
Maaa 24,184,220
DM 20,28,79,0
Maaa 28,184,224
s_mov_b32 s79,3154432
Maaa 32,184,228
M0 77,90816
Maaa 36,184,232
WL 6
DM 20,28,79,0
Maaa 40,188,216
s_mov_b32 s79,3162624
TR 208,14,17512
TR 210,14,17768
Maaa 44,188,220
M0 77,91904
Maaa 48,188,224
DM 20,28,79,0
Maaa 52,188,228
s_mov_b32 s79,3170816
Maaa 56,188,232
M0 77,92992
Maaa 60,192,216
DM 20,28,79,0
Maaa 64,192,220
Maaa 68,192,224
Maaa 72,192,228
buffer_load_dword v8,v8,s[60:63],0 offen
Maaa 76,192,232
WL 4
Maaa 80,196,216
s_waitcnt vmcnt(10) lgkmcnt(0)
s_barrier
Maaa 84,196,220
s_nop 0
TR 180,14,34624
TR 182,14,34880
Maaa 88,196,224
RA 236,18,0
Maaa 92,196,228
RA 240,18,2048
Maaa 96,196,232
RA 244,18,4096
Maaa 100,200,216
s_mov_b32 s79,3408384
RA 248,18,6144
Maaa 104,200,220
M0 77,107040
RA 252,18,8192
Maaa 108,200,224
s_nop 0
TR 184,14,34632
TR 186,14,34888
Maaa 112,200,228
s_nop 0
TR 188,14,34688
TR 190,14,34944
Maaa 116,200,232
DM 20,28,79,0
Maaa 120,204,216
s_mov_b32 s79,3416576
TR 192,14,34696
TR 194,14,34952
Maaa 124,204,220
M0 77,108128
Maaa 128,204,224
DM 20,28,79,0
Maaa 132,204,228
s_mov_b32 s79,3424768
Maaa 136,204,232
M0 77,109216
Maaa 140,208,216
s_nop 0
TR 196,14,34752
TR 198,14,35008
Maaa 144,208,220
DM 20,28,79,0
Maaa 148,208,224
s_mov_b32 s79,3432960
Maaa 152,208,228
M0 77,110304
Maaa 156,208,232
WL 6
DM 20,28,79,0
Maaa 0,180,236
s_nop 0
TR 200,14,34760
TR 202,14,35016
Maaa 4,180,240
Maaa 8,180,244
Maaa 12,180,248
Maaa 16,180,252
Maaa 20,184,236
s_nop 0
TR 204,14,34816
TR 206,14,35072
Maaa 24,184,240
Maaa 28,184,244
Maaa 32,184,248
Maaa 36,184,252
WL 6
Maaa 40,188,236
s_nop 0
TR 208,14,34824
TR 210,14,35080
Maaa 44,188,240
Maaa 48,188,244
Maaa 52,188,248
Maaa 56,188,252
Maaa 60,192,236
Maaa 64,192,240
Maaa 68,192,244
Maaa 72,192,248
Maaa 76,192,252
WL 4
Maaa 80,196,236
s_waitcnt vmcnt(5) lgkmcnt(0)
s_barrier
Maaa 84,196,240
s_nop 0
TR 180,14,0
TR 182,14,256
Maaa 88,196,244
RA 216,15,0
Maaa 92,196,248
RA 220,15,2048
Maaa 96,196,252
RA 224,15,4096
Maaa 100,200,236
s_mov_b32 s79,896
RA 228,15,6144
Maaa 104,200,240
M0 69,141664
RA 232,15,8192
Maaa 108,200,244
s_nop 0
TR 184,14,8
TR 186,14,264
Maaa 112,200,248
s_nop 0
TR 188,14,64
TR 190,14,320
Maaa 116,200,252
DM 21,24,79,0
Maaa 120,204,236
s_mov_b32 s79,37760
TR 192,14,72
TR 194,14,328
Maaa 124,204,240
M0 69,145760
Maaa 128,204,244
DM 21,24,79,0
Maaa 132,204,248
s_mov_b32 s79,74624
Maaa 136,204,252
M0 69,149856
Maaa 140,208,236
s_nop 0
TR 196,14,128
TR 198,14,384
Maaa 144,208,240
DM 21,24,79,0
Maaa 148,208,244
s_mov_b32 s79,111488
Maaa 152,208,248
M0 69,153952
Maaa 156,208,252
WL 6
DM 21,24,79,0
Maaa 0,180,216
s_mov_b32 s79,148352
TR 200,14,136
TR 202,14,392
Maaa 4,180,220
M0 69,158048
Maaa 8,180,224
DM 21,24,79,0
Maaa 12,180,228
s_mov_b32 s79,3670528
Maaa 16,180,232
M0 77,124352
Maaa 20,184,216
s_nop 0
TR 204,14,192
TR 206,14,448
Maaa 24,184,220
DM 20,28,79,0
Maaa 28,184,224
s_mov_b32 s79,3678720
Maaa 32,184,228
M0 77,125440
Maaa 36,184,232
WL 6
DM 20,28,79,0
Maaa 40,188,216
s_mov_b32 s79,3686912
TR 208,14,200
TR 210,14,456
Maaa 44,188,220
M0 77,126528
Maaa 48,188,224
DM 20,28,79,0
Maaa 52,188,228
s_mov_b32 s79,3695104
Maaa 56,188,232
M0 77,127616
Maaa 60,192,216
DM 20,28,79,0
Maaa 64,192,220
Maaa 68,192,224
Maaa 72,192,228
Maaa 76,192,232
WL 4
Maaa 80,196,216
s_waitcnt vmcnt(9) lgkmcnt(0)
s_barrier
Maaa 84,196,220
s_nop 0
TR 180,14,17312
TR 182,14,17568
Maaa 88,196,224
RA 236,16,0
Maaa 92,196,228
RA 240,16,2048
Maaa 96,196,232
RA 244,16,4096
Maaa 100,200,216
s_mov_b32 s79,3932672
RA 248,16,6144
Maaa 104,200,220
M0 77,89728
RA 252,16,8192
Maaa 108,200,224
s_nop 0
TR 184,14,17320
TR 186,14,17576
Maaa 112,200,228
s_nop 0
TR 188,14,17376
TR 190,14,17632
Maaa 116,200,232
DM 20,28,79,0
Maaa 120,204,216
s_mov_b32 s79,3940864
TR 192,14,17384
TR 194,14,17640
Maaa 124,204,220
M0 77,90816
Maaa 128,204,224
DM 20,28,79,0
Maaa 132,204,228
s_mov_b32 s79,3949056
Maaa 136,204,232
M0 77,91904
Maaa 140,208,216
s_nop 0
TR 196,14,17440
TR 198,14,17696
Maaa 144,208,220
DM 20,28,79,0
Maaa 148,208,224
s_mov_b32 s79,3957248
Maaa 152,208,228
M0 77,92992
Maaa 156,208,232
WL 6
DM 20,28,79,0
Maaa 0,180,236
s_nop 0
TR 200,14,17448
TR 202,14,17704
Maaa 4,180,240
Maaa 8,180,244
Maaa 12,180,248
Maaa 16,180,252
Maaa 20,184,236
s_nop 0
TR 204,14,17504
TR 206,14,17760
Maaa 24,184,240
Maaa 28,184,244
Maaa 32,184,248
Maaa 36,184,252
WL 6
Maaa 40,188,236
s_nop 0
TR 208,14,17512
TR 210,14,17768
Maaa 44,188,240
Maaa 48,188,244
Maaa 52,188,248
Maaa 56,188,252
Maaa 60,192,236
Maaa 64,192,240
Maaa 68,192,244
Maaa 72,192,248
Maaa 76,192,252
WL 4
Maaa 80,196,236
s_waitcnt vmcnt(4) lgkmcnt(0)
s_barrier
Maaa 84,196,240
s_nop 0
TR 180,14,34624
TR 182,14,34880
Maaa 88,196,244
RA 216,17,0
Maaa 92,196,248
RA 220,17,2048
Maaa 96,196,252
RA 224,17,4096
Maaa 100,200,236
s_mov_b32 s79,1024
RA 228,17,6144
Maaa 104,200,240
M0 69,0
RA 232,17,8192
Maaa 108,200,244
s_nop 0
TR 184,14,34632
TR 186,14,34888
Maaa 112,200,248
s_nop 0
TR 188,14,34688
TR 190,14,34944
Maaa 116,200,252
DM 21,24,79,0
Maaa 120,204,236
s_mov_b32 s79,37888
TR 192,14,34696
TR 194,14,34952
Maaa 124,204,240
M0 69,4096
Maaa 128,204,244
DM 21,24,79,0
Maaa 132,204,248
s_mov_b32 s79,74752
Maaa 136,204,252
M0 69,8192
Maaa 140,208,236
s_nop 0
TR 196,14,34752
TR 198,14,35008
Maaa 144,208,240
DM 21,24,79,0
Maaa 148,208,244
s_mov_b32 s79,111616
Maaa 152,208,248
M0 69,12288
Maaa 156,208,252
WL 6
DM 21,24,79,0
Maaa 0,180,216
s_mov_b32 s79,148480
TR 200,14,34760
TR 202,14,35016
Maaa 4,180,220
M0 69,16384
Maaa 8,180,224
DM 21,24,79,0
Maaa 12,180,228
s_mov_b32 s79,4194816
Maaa 16,180,232
M0 77,107040
Maaa 20,184,216
s_nop 0
TR 204,14,34816
TR 206,14,35072
Maaa 24,184,220
DM 20,28,79,0
Maaa 28,184,224
s_mov_b32 s79,4203008
Maaa 32,184,228
M0 77,108128
Maaa 36,184,232
WL 6
DM 20,28,79,0
Maaa 40,188,216
s_mov_b32 s79,4211200
TR 208,14,34824
TR 210,14,35080
Maaa 44,188,220
M0 77,109216
Maaa 48,188,224
DM 20,28,79,0
Maaa 52,188,228
s_mov_b32 s79,4219392
Maaa 56,188,232
M0 77,110304
Maaa 60,192,216
DM 20,28,79,0
Maaa 64,192,220
Maaa 68,192,224
Maaa 72,192,228
Maaa 76,192,232
WL 4
Maaa 80,196,216
s_waitcnt vmcnt(9) lgkmcnt(0)
s_barrier
Maaa 84,196,220
s_nop 0
TR 180,14,0
TR 182,14,256
Maaa 88,196,224
RA 236,18,0
Maaa 92,196,228
RA 240,18,2048
Maaa 96,196,232
RA 244,18,4096
Maaa 100,200,216
s_mov_b32 s79,4456960
RA 248,18,6144
Maaa 104,200,220
M0 77,124352
RA 252,18,8192
Maaa 108,200,224
s_nop 0
TR 184,14,8
TR 186,14,264
Maaa 112,200,228
s_nop 0
TR 188,14,64
TR 190,14,320
Maaa 116,200,232
DM 20,28,79,0
Maaa 120,204,216
s_mov_b32 s79,4465152
TR 192,14,72
TR 194,14,328
Maaa 124,204,220
M0 77,125440
Maaa 128,204,224
DM 20,28,79,0
Maaa 132,204,228
s_mov_b32 s79,4473344
Maaa 136,204,232
M0 77,126528
Maaa 140,208,216
s_nop 0
TR 196,14,128
TR 198,14,384
Maaa 144,208,220
DM 20,28,79,0
Maaa 148,208,224
s_mov_b32 s79,4481536
Maaa 152,208,228
M0 77,127616
Maaa 156,208,232
WL 6
DM 20,28,79,0
Maaa 0,180,236
s_nop 0
TR 200,14,136
TR 202,14,392
Maaa 4,180,240
Maaa 8,180,244
Maaa 12,180,248
Maaa 16,180,252
Maaa 20,184,236
s_nop 0
TR 204,14,192
TR 206,14,448
Maaa 24,184,240
Maaa 28,184,244
Maaa 32,184,248
Maaa 36,184,252
WL 6
Maaa 40,188,236
s_nop 0
TR 208,14,200
TR 210,14,456
Maaa 44,188,240
Maaa 48,188,244
Maaa 52,188,248
Maaa 56,188,252
Maaa 60,192,236
Maaa 64,192,240
Maaa 68,192,244
Maaa 72,192,248
Maaa 76,192,252
WL 4
Maaa 80,196,236
s_waitcnt vmcnt(4) lgkmcnt(0)
s_barrier
Maaa 84,196,240
s_nop 0
TR 180,14,17312
TR 182,14,17568
Maaa 88,196,244
RA 216,15,0
Maaa 92,196,248
RA 220,15,2048
Maaa 96,196,252
RA 224,15,4096
Maaa 100,200,236
s_mov_b32 s79,0
RA 228,15,6144
Maaa 104,200,240
M0 77,89728
RA 232,15,8192
Maaa 108,200,244
s_nop 0
TR 184,14,17320
TR 186,14,17576
Maaa 112,200,248
s_nop 0
TR 188,14,17376
TR 190,14,17632
Maaa 116,200,252
DM 20,28,79,0
Maaa 120,204,236
s_mov_b32 s79,8192
TR 192,14,17384
TR 194,14,17640
Maaa 124,204,240
M0 77,90816
Maaa 128,204,244
DM 20,28,79,0
Maaa 132,204,248
s_mov_b32 s79,16384
Maaa 136,204,252
M0 77,91904
Maaa 140,208,236
s_nop 0
TR 196,14,17440
TR 198,14,17696
Maaa 144,208,240
DM 20,28,79,0
Maaa 148,208,244
s_mov_b32 s79,24576
Maaa 152,208,248
M0 77,92992
Maaa 156,208,252
WL 6
DM 20,28,79,0
Maaa 0,180,216
s_mov_b32 s79,0
TR 200,14,17448
TR 202,14,17704
Maaa 4,180,220
M0 69,141664
Maaa 8,180,224
DM 21,24,79,0
Maaa 12,180,228
s_mov_b32 s79,36864
Maaa 16,180,232
M0 69,145760
Maaa 20,184,216
s_nop 0
TR 204,14,17504
TR 206,14,17760
Maaa 24,184,220
DM 21,24,79,0
Maaa 28,184,224
s_mov_b32 s79,73728
Maaa 32,184,228
M0 69,149856
Maaa 36,184,232
WL 6
DM 21,24,79,0
Maaa 40,188,216
s_mov_b32 s79,110592
TR 208,14,17512
TR 210,14,17768
Maaa 44,188,220
M0 69,153952
Maaa 48,188,224
DM 21,24,79,0
Maaa 52,188,228
s_mov_b32 s79,147456
Maaa 56,188,232
M0 69,158048
Maaa 60,192,216
DM 21,24,79,0
Maaa 64,192,220
Maaa 68,192,224
Maaa 72,192,228
Maaa 76,192,232
WL 4
Maaa 80,196,216
s_waitcnt vmcnt(9) lgkmcnt(0)
s_barrier
Maaa 84,196,220
s_nop 0
TR 180,14,34624
TR 182,14,34880
Maaa 88,196,224
RA 236,16,0
Maaa 92,196,228
RA 240,16,2048
Maaa 96,196,232
RA 244,16,4096
Maaa 100,200,216
s_mov_b32 s79,262144
RA 248,16,6144
Maaa 104,200,220
M0 77,107040
RA 252,16,8192
Maaa 108,200,224
s_nop 0
TR 184,14,34632
TR 186,14,34888
Maaa 112,200,228
s_nop 0
TR 188,14,34688
TR 190,14,34944
Maaa 116,200,232
DM 20,28,79,0
Maaa 120,204,216
s_mov_b32 s79,270336
TR 192,14,34696
TR 194,14,34952
Maaa 124,204,220
M0 77,108128
Maaa 128,204,224
DM 20,28,79,0
Maaa 132,204,228
s_mov_b32 s79,278528
Maaa 136,204,232
M0 77,109216
Maaa 140,208,216
s_nop 0
TR 196,14,34752
TR 198,14,35008
Maaa 144,208,220
DM 20,28,79,0
Maaa 148,208,224
s_mov_b32 s79,286720
Maaa 152,208,228
M0 77,110304
Maaa 156,208,232
WL 6
DM 20,28,79,0
Maaa 0,180,236
s_nop 0
TR 200,14,34760
TR 202,14,35016
Maaa 4,180,240
Maaa 8,180,244
Maaa 12,180,248
Maaa 16,180,252
Maaa 20,184,236
s_nop 0
TR 204,14,34816
TR 206,14,35072
Maaa 24,184,240
Maaa 28,184,244
Maaa 32,184,248
Maaa 36,184,252
WL 6
Maaa 40,188,236
s_nop 0
TR 208,14,34824
TR 210,14,35080
Maaa 44,188,240
Maaa 48,188,244
Maaa 52,188,248
Maaa 56,188,252
Maaa 60,192,236
Maaa 64,192,240
Maaa 68,192,244
Maaa 72,192,248
Maaa 76,192,252
WL 4
Maaa 80,196,236
s_waitcnt vmcnt(4) lgkmcnt(0)
s_barrier
Maaa 84,196,240
s_nop 0
TR 180,14,0
TR 182,14,256
Maaa 88,196,244
RA 216,17,0
Maaa 92,196,248
RA 220,17,2048
Maaa 96,196,252
RA 224,17,4096
Maaa 100,200,236
s_mov_b32 s79,128
RA 228,17,6144
Maaa 104,200,240
M0 69,0
RA 232,17,8192
Maaa 108,200,244
s_nop 0
TR 184,14,8
TR 186,14,264
Maaa 112,200,248
s_nop 0
TR 188,14,64
TR 190,14,320
Maaa 116,200,252
DM 21,24,79,0
Maaa 120,204,236
s_mov_b32 s79,36992
TR 192,14,72
TR 194,14,328
Maaa 124,204,240
M0 69,4096
Maaa 128,204,244
DM 21,24,79,0
Maaa 132,204,248
s_mov_b32 s79,73856
Maaa 136,204,252
M0 69,8192
Maaa 140,208,236
s_nop 0
TR 196,14,128
TR 198,14,384
Maaa 144,208,240
DM 21,24,79,0
Maaa 148,208,244
s_mov_b32 s79,110720
Maaa 152,208,248
M0 69,12288
Maaa 156,208,252
WL 6
DM 21,24,79,0
Mvaaz 36,180,216
s_mov_b32 s79,147584
TR 200,14,136
TR 202,14,392
Mvaaz 52,180,220
M0 69,16384
AR 196,0
AR 197,1
Mvaaz 68,180,224
DM 21,24,79,0
Mvaaz 84,180,228
s_mov_b32 s79,524288
AR 198,2
AR 199,3
Mvaaz 100,180,232
M0 77,124352
AR 200,20
AR 201,21
Mvaaz 116,184,216
s_nop 0
TR 204,14,192
TR 206,14,448
Mvaaz 120,184,220
DM 20,28,79,0
Mvaaz 124,184,224
s_mov_b32 s79,532480
AR 202,22
AR 203,23
Mvaaz 128,184,228
M0 77,125440
v_mul_f32 v196,s84,v196
v_mul_f32 v197,s84,v197
Mvaaz 132,184,232
WL 6
DM 20,28,79,0
Mvaaz 40,188,216
s_mov_b32 s79,540672
TR 208,14,200
TR 210,14,456
Mvaaz 56,188,220
M0 77,126528
v_mul_f32 v198,s84,v198
v_mul_f32 v199,s84,v199
Mvaaz 72,188,224
DM 20,28,79,0
Mvaaz 88,188,228
s_mov_b32 s79,548864
v_mul_f32 v200,s84,v200
v_mul_f32 v201,s84,v201
Mvaaz 104,188,232
M0 77,127616
v_mul_f32 v202,s84,v202
v_mul_f32 v203,s84,v203
Mvaaz 136,192,216
DM 20,28,79,0
Mvaaz 140,192,220
CV 216,196,197
CV 217,198,199
Mvaaz 144,192,224
CV 218,200,201
CV 219,202,203
Mvaaz 148,192,228
AR 204,4
AR 205,5
Mvaaz 152,192,232
WL 4
AR 206,6
AR 207,7
Mvaaz 44,196,216
s_waitcnt vmcnt(9) lgkmcnt(0)
s_barrier
Mvaaz 60,196,220
s_add_u32 s98,s71,32768
TR 180,14,17312
TR 182,14,17568
Mvaaz 76,196,224
RA 236,18,0
v_lshl_add_u32 v8,v2,1,s98
Mvaaz 92,196,228
RA 240,18,2048
AR 208,24
Mvaaz 108,196,232
RA 244,18,4096
AR 209,25
Mvaaz 156,200,216
s_mov_b32 s79,786432
RA 248,18,6144
AR 210,26
Mvaaz 160,200,220
M0 77,89728
RA 252,18,8192
AR 211,27
Mvaaz 164,200,224
s_nop 0
TR 184,14,17320
TR 186,14,17576
Mvaaz 168,200,228
s_nop 0
TR 188,14,17376
TR 190,14,17632
Mvaaz 172,200,232
DM 20,28,79,0
Mvaaz 48,204,216
s_mov_b32 s79,794624
TR 192,14,17384
TR 194,14,17640
Mvaaz 64,204,220
M0 77,90816
v_mul_f32 v204,s84,v204
v_mul_f32 v205,s84,v205
Mvaaz 80,204,224
DM 20,28,79,0
Mvaaz 96,204,228
s_mov_b32 s79,802816
v_mul_f32 v206,s84,v206
v_mul_f32 v207,s84,v207
Mvaaz 112,204,232
M0 77,91904
v_mul_f32 v208,s84,v208
v_mul_f32 v209,s84,v209
Mvaaz 176,208,216
s_nop 0
TR 196,14,17440
TR 198,14,17696
Mvaaz 180,208,220
DM 20,28,79,0
Mvaaz 184,208,224
s_mov_b32 s79,811008
v_mul_f32 v210,s84,v210
v_mul_f32 v211,s84,v211
Mvaaz 188,208,228
M0 77,92992
CV 220,204,205
CV 221,206,207
Mvaaz 192,208,232
WL 6
DM 20,28,79,0
Mvaa 36,180,236
s_nop 0
TR 200,14,17448
TR 202,14,17704
Mvaa 52,180,240
CV 222,208,209
CV 223,210,211
Mvaa 68,180,244
buffer_load_dword v8,v8,s[60:63],0 offen
Mvaa 84,180,248
AR 196,8
AR 197,9
Mvaa 100,180,252
AR 198,10
AR 199,11
Mvaa 116,184,236
s_nop 0
TR 204,14,17504
TR 206,14,17760
Mvaa 120,184,240
AR 200,28
AR 201,29
Mvaa 124,184,244
AR 202,30
AR 203,31
Mvaa 128,184,248
v_mul_f32 v196,s84,v196
v_mul_f32 v197,s84,v197
Mvaa 132,184,252
WL 6
v_mul_f32 v198,s84,v198
v_mul_f32 v199,s84,v199
Mvaa 40,188,236
s_nop 0
TR 208,14,17512
TR 210,14,17768
Mvaa 56,188,240
v_mul_f32 v200,s84,v200
v_mul_f32 v201,s84,v201
Mvaa 72,188,244
v_mul_f32 v202,s84,v202
v_mul_f32 v203,s84,v203
Mvaa 88,188,248
CV 224,196,197
CV 225,198,199
Mvaa 104,188,252
CV 226,200,201
CV 227,202,203
Mvaa 136,192,236
AR 204,12
AR 205,13
Mvaa 140,192,240
AR 206,14
AR 207,15
Mvaa 144,192,244
AR 208,32
AR 209,33
Mvaa 148,192,248
AR 210,34
AR 211,35
Mvaa 152,192,252
WL 4
v_mul_f32 v204,s84,v204
v_mul_f32 v205,s84,v205
Mvaa 44,196,236
s_waitcnt vmcnt(5) lgkmcnt(0)
s_barrier
Mvaa 60,196,240
s_nop 0
TR 180,14,34624
TR 182,14,34880
Mvaa 76,196,244
RA 216,15,0
v_mul_f32 v206,s84,v206
Mvaa 92,196,248
RA 220,15,2048
v_mul_f32 v207,s84,v207
Mvaa 108,196,252
RA 224,15,4096
v_mul_f32 v208,s84,v208
Mvaa 156,200,236
s_mov_b32 s79,256
RA 228,15,6144
v_mul_f32 v209,s84,v209
Mvaa 160,200,240
M0 69,141664
RA 232,15,8192
v_mul_f32 v210,s84,v210
Mvaa 164,200,244
s_nop 0
TR 184,14,34632
TR 186,14,34888
Mvaa 168,200,248
s_nop 0
TR 188,14,34688
TR 190,14,34944
Mvaa 172,200,252
DM 21,24,79,0
Mvaa 48,204,236
s_mov_b32 s79,37120
TR 192,14,34696
TR 194,14,34952
Mvaa 64,204,240
M0 69,145760
v_mul_f32 v211,s84,v211
CV 228,204,205
Mvaa 80,204,244
DM 21,24,79,0
Mvaa 96,204,248
s_mov_b32 s79,73984
CV 229,206,207
CV 230,208,209
Mvaa 112,204,252
M0 69,149856
CV 231,210,211
AR 196,16
Mvaa 176,208,236
s_nop 0
TR 196,14,34752
TR 198,14,35008
Mvaa 180,208,240
DM 21,24,79,0
Mvaa 184,208,244
s_mov_b32 s79,110848
AR 197,17
AR 198,18
Mvaa 188,208,248
M0 69,153952
AR 199,19
AR 200,36
Mvaa 192,208,252
WL 6
DM 21,24,79,0
Mvaa 36,180,216
s_mov_b32 s79,147712
TR 200,14,34760
TR 202,14,35016
Mvaa 52,180,220
M0 69,158048
AR 201,37
AR 202,38
Mvaa 68,180,224
DM 21,24,79,0
Mvaa 84,180,228
s_mov_b32 s79,1048576
AR 203,39
v_mul_f32 v196,s84,v196
Mvaa 100,180,232
M0 77,107040
v_mul_f32 v197,s84,v197
v_mul_f32 v198,s84,v198
Mvaa 116,184,216
s_nop 0
TR 204,14,34816
TR 206,14,35072
Mvaa 120,184,220
DM 20,28,79,0
Mvaa 124,184,224
s_mov_b32 s79,1056768
v_mul_f32 v199,s84,v199
v_mul_f32 v200,s84,v200
Mvaa 128,184,228
M0 77,108128
v_mul_f32 v201,s84,v201
v_mul_f32 v202,s84,v202
Mvaa 132,184,232
WL 6
DM 20,28,79,0
Mvaa 40,188,216
s_mov_b32 s79,1064960
TR 208,14,34824
TR 210,14,35080
Mvaa 56,188,220
M0 77,109216
v_mul_f32 v203,s84,v203
CV 232,196,197
Mvaa 72,188,224
DM 20,28,79,0
Mvaa 88,188,228
s_mov_b32 s79,1073152
CV 233,198,199
CV 234,200,201
Mvaa 104,188,232
M0 77,110304
CV 235,202,203
AR 204,40
Mvaa 136,192,216
DM 20,28,79,0
Mvaa 140,192,220
AR 205,41
AR 206,42
Mvaa 144,192,224
AR 207,43
AR 208,60
Mvaa 148,192,228
AR 209,61
AR 210,62
Mvaa 152,192,232
WL 4
AR 211,63
v_mul_f32 v204,s84,v204
Mvaa 44,196,216
s_waitcnt vmcnt(10) lgkmcnt(0)
s_barrier
Mvaa 60,196,220
s_nop 0
TR 180,14,0
TR 182,14,256
Mvaa 76,196,224
RA 236,16,0
v_mul_f32 v205,s84,v205
Mvaa 92,196,228
s_add_u32 s4,s80,0
RA 240,16,2048
v_mul_f32 v206,s84,v206
Mvaa 108,196,232
s_and_b32 s4,s4,0x1e000
RA 244,16,4096
v_mul_f32 v207,s84,v207
Mvaa 156,200,216
s_xor_b32 s5,s4,0x30000
RA 248,16,6144
v_mul_f32 v208,s84,v208
Mvaa 160,200,220
M0 78,20480
RA 252,16,8192
v_mul_f32 v209,s84,v209
Mvaa 164,200,224
s_nop 0
TR 184,14,8
TR 186,14,264
Mvaa 168,200,228
s_nop 0
TR 188,14,64
TR 190,14,320
Mvaa 172,200,232
DN 19,32,4,0
Mvaa 48,204,216
s_mov_b32 s79,1310720
TR 192,14,72
TR 194,14,328
Mvaa 64,204,220
M0 77,124352
v_mul_f32 v210,s84,v210
v_mul_f32 v211,s84,v211
Mvaa 80,204,224
DM 20,28,79,0
Mvaa 96,204,228
s_mov_b32 s79,1318912
CV 236,204,205
CV 237,206,207
Mvaa 112,204,232
M0 77,125440
CV 238,208,209
CV 239,210,211
Mvaa 176,208,216
s_nop 0
TR 196,14,128
TR 198,14,384
Mvaa 180,208,220
DM 20,28,79,0
Mvaa 184,208,224
s_mov_b32 s79,1327104
AR 196,44
AR 197,45
Mvaa 188,208,228
M0 77,126528
AR 198,46
AR 199,47
Mvaa 192,208,232
WL 6
DM 20,28,79,0
Mvaa 36,180,236
s_mov_b32 s79,1335296
TR 200,14,136
TR 202,14,392
Mvaa 52,180,240
M0 77,127616
AR 200,64
AR 201,65
Mvaa 68,180,244
DM 20,28,79,0
Mvaa 84,180,248
AR 202,66
AR 203,67
Mvaa 100,180,252
v_mul_f32 v196,s84,v196
v_mul_f32 v197,s84,v197
Mvaa 116,184,236
s_nop 0
TR 204,14,192
TR 206,14,448
Mvaa 120,184,240
v_mul_f32 v198,s84,v198
v_mul_f32 v199,s84,v199
Mvaa 124,184,244
v_mul_f32 v200,s84,v200
v_mul_f32 v201,s84,v201
Mvaa 128,184,248
M0 78,24704
v_mul_f32 v202,s84,v202
v_mul_f32 v203,s84,v203
Mvaa 132,184,252
WL 6
DN 33,32,4,0
Mvaa 40,188,236
s_nop 0
TR 208,14,200
TR 210,14,456
Mvaa 56,188,240
CV 240,196,197
CV 241,198,199
Mvaa 72,188,244
CV 242,200,201
CV 243,202,203
Mvaa 88,188,248
AR 204,48
AR 205,49
Mvaa 104,188,252
AR 206,50
AR 207,51
Mvaa 136,192,236
AR 208,68
AR 209,69
Mvaa 140,192,240
AR 210,70
AR 211,71
Mvaa 144,192,244
v_mul_f32 v204,s84,v204
v_mul_f32 v205,s84,v205
Mvaa 148,192,248
v_mul_f32 v206,s84,v206
v_mul_f32 v207,s84,v207
Mvaa 152,192,252
WL 4
v_mul_f32 v208,s84,v208
v_mul_f32 v209,s84,v209
Mvaa 44,196,236
s_waitcnt vmcnt(6) lgkmcnt(0)
s_barrier
Mvaa 60,196,240
s_nop 0
TR 180,14,17312
TR 182,14,17568
Mvaa 76,196,244
RA 216,17,0
v_mul_f32 v210,s84,v210
Mvaa 92,196,248
RA 220,17,2048
v_mul_f32 v211,s84,v211
Mvaa 108,196,252
RA 224,17,4096
CV 244,204,205
Mvaa 156,200,236
s_mov_b32 s79,384
RA 228,17,6144
CV 245,206,207
Mvaa 160,200,240
M0 69,0
RA 232,17,8192
CV 246,208,209
Mvaa 164,200,244
s_nop 0
TR 184,14,17320
TR 186,14,17576
Mvaa 168,200,248
s_nop 0
TR 188,14,17376
TR 190,14,17632
Mvaa 172,200,252
DM 21,24,79,0
Mvaa 48,204,236
s_mov_b32 s79,37248
TR 192,14,17384
TR 194,14,17640
Mvaa 64,204,240
M0 69,4096
CV 247,210,211
AR 196,52
Mvaa 80,204,244
DM 21,24,79,0
Mvaa 96,204,248
s_mov_b32 s79,74112
AR 197,53
AR 198,54
Mvaa 112,204,252
M0 69,8192
AR 199,55
AR 200,72
Mvaa 176,208,236
s_nop 0
TR 196,14,17440
TR 198,14,17696
Mvaa 180,208,240
DM 21,24,79,0
Mvaa 184,208,244
s_mov_b32 s79,110976
AR 201,73
AR 202,74
Mvaa 188,208,248
M0 69,12288
AR 203,75
v_mul_f32 v196,s84,v196
Mvaa 192,208,252
WL 6
DM 21,24,79,0
Mvaa 36,180,216
s_mov_b32 s79,147840
TR 200,14,17448
TR 202,14,17704
Mvaa 52,180,220
M0 69,16384
v_mul_f32 v197,s84,v197
v_mul_f32 v198,s84,v198
Mvaa 68,180,224
DM 21,24,79,0
Mvaa 84,180,228
s_mov_b32 s79,1572864
v_mul_f32 v199,s84,v199
v_mul_f32 v200,s84,v200
Mvaa 100,180,232
M0 77,89728
v_mul_f32 v201,s84,v201
v_mul_f32 v202,s84,v202
Mvaa 116,184,216
s_nop 0
TR 204,14,17504
TR 206,14,17760
Mvaa 120,184,220
DM 20,28,79,0
Mvaa 124,184,224
s_mov_b32 s79,1581056
v_mul_f32 v203,s84,v203
CV 248,196,197
Mvaa 128,184,228
M0 77,90816
CV 249,198,199
CV 250,200,201
Mvaa 132,184,232
WL 6
DM 20,28,79,0
Mvaa 40,188,216
s_mov_b32 s79,1589248
TR 208,14,17512
TR 210,14,17768
Mvaa 56,188,220
M0 77,91904
CV 251,202,203
AR 204,56
Mvaa 72,188,224
DM 20,28,79,0
Mvaa 88,188,228
s_mov_b32 s79,1597440
AR 205,57
AR 206,58
Mvaa 104,188,232
M0 77,92992
AR 207,59
AR 208,76
Mvaa 136,192,216
DM 20,28,79,0
Mvaa 140,192,220
M0 78,28928
AR 209,77
AR 210,78
Mvaa 144,192,224
AR 211,79
v_mul_f32 v204,s84,v204
Mvaa 148,192,228
DN 19,32,5,0
Mvaa 152,192,232
WL 4
v_mul_f32 v205,s84,v205
v_mul_f32 v206,s84,v206
Mvaa 44,196,216
s_waitcnt vmcnt(11) lgkmcnt(0)
s_barrier
Mvaa 60,196,220
s_nop 0
TR 180,14,34624
TR 182,14,34880
Mvaa 76,196,224
RA 20,18,0
v_mul_f32 v207,s84,v207
Mvaa 92,196,228
RA 24,18,2048
v_mul_f32 v208,s84,v208
Mvaa 108,196,232
RA 28,18,4096
v_mul_f32 v209,s84,v209
Mvaa 156,200,216
s_mov_b32 s79,1835008
RA 32,18,6144
v_mul_f32 v210,s84,v210
Mvaa 160,200,220
M0 77,107040
RA 36,18,8192
v_mul_f32 v211,s84,v211
Mvaa 164,200,224
s_nop 0
TR 184,14,34632
TR 186,14,34888
Mvaa 168,200,228
s_nop 0
TR 188,14,34688
TR 190,14,34944
Mvaa 172,200,232
DM 20,28,79,0
Mvaa 48,204,216
s_mov_b32 s79,1843200
TR 192,14,34696
TR 194,14,34952
Mvaa 64,204,220
M0 77,108128
CV 252,204,205
CV 253,206,207
Mvaa 80,204,224
DM 20,28,79,0
Mvaa 96,204,228
s_mov_b32 s79,1851392
CV 254,208,209
CV 255,210,211
Mvaa 112,204,232
M0 77,109216
Mvaa 176,208,216
s_nop 0
TR 196,14,34752
TR 198,14,35008
Mvaa 180,208,220
DM 20,28,79,0
Mvaa 184,208,224
s_mov_b32 s79,1859584
Mvaa 188,208,228
M0 77,110304
Mvaa 192,208,232
WL 6
DM 20,28,79,0
Mvaa 36,180,20
s_nop 0
TR 200,14,34760
TR 202,14,35016
Mvaa 52,180,24
AR 196,80
AR 197,81
Mvaa 68,180,28
AR 198,82
AR 199,83
Mvaa 84,180,32
AR 200,100
AR 201,101
Mvaa 100,180,36
AR 202,102
AR 203,103
Mvaa 116,184,20
s_nop 0
TR 204,14,34816
TR 206,14,35072
Mvaa 120,184,24
v_mul_f32 v196,s84,v196
v_mul_f32 v197,s84,v197
Mvaa 124,184,28
v_mul_f32 v198,s84,v198
v_mul_f32 v199,s84,v199
Mvaa 128,184,32
M0 78,33152
v_mul_f32 v200,s84,v200
v_mul_f32 v201,s84,v201
Mvaa 132,184,36
WL 6
DN 33,32,5,0
Mvaa 40,188,20
s_nop 0
TR 208,14,34824
TR 210,14,35080
Mvaa 56,188,24
v_mul_f32 v202,s84,v202
v_mul_f32 v203,s84,v203
Mvaa 72,188,28
CV 196,196,197
CV 197,198,199
Mvaa 88,188,32
CV 198,200,201
CV 199,202,203
Mvaa 104,188,36
v_accvgpr_write_b32 a216,v196
v_accvgpr_write_b32 a217,v197
Mvaa 136,192,20
v_accvgpr_write_b32 a218,v198
v_accvgpr_write_b32 a219,v199
Mvaa 140,192,24
AR 204,84
AR 205,85
Mvaa 144,192,28
AR 206,86
AR 207,87
Mvaa 148,192,32
AR 208,104
AR 209,105
Mvaa 152,192,36
WL 4
AR 210,106
AR 211,107
Mvaa 44,196,20
s_waitcnt vmcnt(6) lgkmcnt(0)
s_barrier
Mvaa 60,196,24
s_nop 0
TR 180,14,0
TR 182,14,256
Mvaa 76,196,28
RA 0,15,0
v_mul_f32 v204,s84,v204
Mvaa 92,196,32
RA 4,15,2048
v_mul_f32 v205,s84,v205
Mvaa 108,196,36
RA 8,15,4096
v_mul_f32 v206,s84,v206
Mvaa 156,200,20
s_mov_b32 s79,512
RA 12,15,6144
v_mul_f32 v207,s84,v207
Mvaa 160,200,24
M0 69,141664
RA 16,15,8192
v_mul_f32 v208,s84,v208
Mvaa 164,200,28
s_nop 0
TR 184,14,8
TR 186,14,264
Mvaa 168,200,32
s_nop 0
TR 188,14,64
TR 190,14,320
Mvaa 172,200,36
DM 21,24,79,0
Mvaa 48,204,20
s_mov_b32 s79,37376
TR 192,14,72
TR 194,14,328
Mvaa 64,204,24
M0 69,145760
v_mul_f32 v209,s84,v209
v_mul_f32 v210,s84,v210
Mvaa 80,204,28
DM 21,24,79,0
Mvaa 96,204,32
s_mov_b32 s79,74240
v_mul_f32 v211,s84,v211
CV 204,204,205
Mvaa 112,204,36
M0 69,149856
CV 205,206,207
CV 206,208,209
Mvaa 176,208,20
s_nop 0
TR 196,14,128
TR 198,14,384
Mvaa 180,208,24
DM 21,24,79,0
Mvaa 184,208,28
s_mov_b32 s79,111104
CV 207,210,211
v_accvgpr_write_b32 a220,v204
Mvaa 188,208,32
M0 69,153952
v_accvgpr_write_b32 a221,v205
v_accvgpr_write_b32 a222,v206
Mvaa 192,208,36
WL 6
DM 21,24,79,0
Mvaa 36,180,0
s_mov_b32 s79,147968
TR 200,14,136
TR 202,14,392
Mvaa 52,180,4
M0 69,158048
v_accvgpr_write_b32 a223,v207
AR 196,88
Mvaa 68,180,8
DM 21,24,79,0
Mvaa 84,180,12
s_mov_b32 s79,2097152
AR 197,89
AR 198,90
Mvaa 100,180,16
M0 77,124352
AR 199,91
AR 200,108
Mvaa 116,184,0
s_nop 0
TR 204,14,192
TR 206,14,448
Mvaa 120,184,4
DM 20,28,79,0
Mvaa 124,184,8
s_mov_b32 s79,2105344
AR 201,109
AR 202,110
Mvaa 128,184,12
M0 77,125440
AR 203,111
v_mul_f32 v196,s84,v196
Mvaa 132,184,16
WL 6
DM 20,28,79,0
Mvaa 40,188,0
s_mov_b32 s79,2113536
TR 208,14,200
TR 210,14,456
Mvaa 56,188,4
M0 77,126528
v_mul_f32 v197,s84,v197
v_mul_f32 v198,s84,v198
Mvaa 72,188,8
DM 20,28,79,0
Mvaa 88,188,12
s_mov_b32 s79,2121728
v_mul_f32 v199,s84,v199
v_mul_f32 v200,s84,v200
Mvaa 104,188,16
M0 77,127616
v_mul_f32 v201,s84,v201
v_mul_f32 v202,s84,v202
Mvaa 136,192,0
DM 20,28,79,0
Mvaa 140,192,4
v_mul_f32 v203,s84,v203
CV 196,196,197
Mvaa 144,192,8
CV 197,198,199
CV 198,200,201
Mvaa 148,192,12
CV 199,202,203
v_accvgpr_write_b32 a224,v196
Mvaa 152,192,16
WL 4
v_accvgpr_write_b32 a225,v197
v_accvgpr_write_b32 a226,v198
Mvaa 44,196,0
s_waitcnt vmcnt(10) lgkmcnt(0)
s_barrier
Mvaa 60,196,4
s_nop 0
TR 180,14,17312
TR 182,14,17568
Mvaa 76,196,8
RA 20,16,0
v_accvgpr_write_b32 a227,v199
Mvaa 92,196,12
s_add_u32 s6,s80,8192
RA 24,16,2048
AR 204,92
Mvaa 108,196,16
s_and_b32 s6,s6,0x1e000
RA 28,16,4096
AR 205,93
Mvaa 156,200,0
s_xor_b32 s7,s6,0x30000
RA 32,16,6144
AR 206,94
Mvaa 160,200,4
M0 78,37792
RA 36,16,8192
AR 207,95
Mvaa 164,200,8
s_nop 0
TR 184,14,17320
TR 186,14,17576
Mvaa 168,200,12
s_nop 0
TR 188,14,17376
TR 190,14,17632
Mvaa 172,200,16
DN 19,32,6,0
Mvaa 48,204,0
s_mov_b32 s79,2359296
TR 192,14,17384
TR 194,14,17640
Mvaa 64,204,4
M0 77,89728
AR 208,112
AR 209,113
Mvaa 80,204,8
DM 20,28,79,0
Mvaa 96,204,12
s_mov_b32 s79,2367488
AR 210,114
AR 211,115
Mvaa 112,204,16
M0 77,90816
v_mul_f32 v204,s84,v204
v_mul_f32 v205,s84,v205
Mvaa 176,208,0
s_nop 0
TR 196,14,17440
TR 198,14,17696
Mvaa 180,208,4
DM 20,28,79,0
Mvaa 184,208,8
s_mov_b32 s79,2375680
v_mul_f32 v206,s84,v206
v_mul_f32 v207,s84,v207
Mvaa 188,208,12
M0 77,91904
v_mul_f32 v208,s84,v208
v_mul_f32 v209,s84,v209
Mvaa 192,208,16
WL 6
DM 20,28,79,0
Mvaa 36,180,20
s_mov_b32 s79,2383872
TR 200,14,17448
TR 202,14,17704
Mvaa 52,180,24
M0 77,92992
v_mul_f32 v210,s84,v210
v_mul_f32 v211,s84,v211
Mvaa 68,180,28
DM 20,28,79,0
Mvaa 84,180,32
CV 204,204,205
CV 205,206,207
Mvaa 100,180,36
CV 206,208,209
CV 207,210,211
Mvaa 116,184,20
s_nop 0
TR 204,14,17504
TR 206,14,17760
Mvaa 120,184,24
v_accvgpr_write_b32 a228,v204
v_accvgpr_write_b32 a229,v205
Mvaa 124,184,28
v_accvgpr_write_b32 a230,v206
v_accvgpr_write_b32 a231,v207
Mvaa 128,184,32
M0 78,42016
AR 196,96
AR 197,97
Mvaa 132,184,36
WL 6
DN 33,32,6,0
Mvaa 40,188,20
s_nop 0
TR 208,14,17512
TR 210,14,17768
Mvaa 56,188,24
AR 198,98
AR 199,99
Mvaa 72,188,28
AR 200,116
AR 201,117
Mvaa 88,188,32
AR 202,118
AR 203,119
Mvaa 104,188,36
v_mul_f32 v196,s84,v196
v_mul_f32 v197,s84,v197
Mvaa 136,192,20
v_mul_f32 v198,s84,v198
v_mul_f32 v199,s84,v199
Mvaa 140,192,24
v_mul_f32 v200,s84,v200
v_mul_f32 v201,s84,v201
Mvaa 144,192,28
v_mul_f32 v202,s84,v202
v_mul_f32 v203,s84,v203
Mvaa 148,192,32
CV 196,196,197
CV 197,198,199
Mvaa 152,192,36
WL 4
CV 198,200,201
CV 199,202,203
Mvaa 44,196,20
s_waitcnt vmcnt(6) lgkmcnt(0)
s_barrier
Mvaa 60,196,24
s_nop 0
TR 180,14,34624
TR 182,14,34880
Mvaa 76,196,28
RA 0,17,0
v_accvgpr_write_b32 a232,v196
Mvaa 92,196,32
RA 4,17,2048
v_accvgpr_write_b32 a233,v197
Mvaa 108,196,36
RA 8,17,4096
v_accvgpr_write_b32 a234,v198
Mvaa 156,200,20
s_mov_b32 s79,640
RA 12,17,6144
v_accvgpr_write_b32 a235,v199
Mvaa 160,200,24
M0 69,0
RA 16,17,8192
AR 204,120
Mvaa 164,200,28
s_nop 0
TR 184,14,34632
TR 186,14,34888
Mvaa 168,200,32
s_nop 0
TR 188,14,34688
TR 190,14,34944
Mvaa 172,200,36
DM 21,24,79,0
Mvaa 48,204,20
s_mov_b32 s79,37504
TR 192,14,34696
TR 194,14,34952
Mvaa 64,204,24
M0 69,4096
AR 205,121
AR 206,122
Mvaa 80,204,28
DM 21,24,79,0
Mvaa 96,204,32
s_mov_b32 s79,74368
AR 207,123
AR 208,140
Mvaa 112,204,36
M0 69,8192
AR 209,141
AR 210,142
Mvaa 176,208,20
s_nop 0
TR 196,14,34752
TR 198,14,35008
Mvaa 180,208,24
DM 21,24,79,0
Mvaa 184,208,28
s_mov_b32 s79,111232
AR 211,143
v_mul_f32 v204,s84,v204
Mvaa 188,208,32
M0 69,12288
v_mul_f32 v205,s84,v205
v_mul_f32 v206,s84,v206
Mvaa 192,208,36
WL 6
DM 21,24,79,0
Mvaa 36,180,0
s_mov_b32 s79,148096
TR 200,14,34760
TR 202,14,35016
Mvaa 52,180,4
M0 69,16384
v_mul_f32 v207,s84,v207
v_mul_f32 v208,s84,v208
Mvaa 68,180,8
DM 21,24,79,0
Mvaa 84,180,12
s_mov_b32 s79,2621440
v_mul_f32 v209,s84,v209
v_mul_f32 v210,s84,v210
Mvaa 100,180,16
M0 77,107040
v_mul_f32 v211,s84,v211
CV 204,204,205
Mvaa 116,184,0
s_nop 0
TR 204,14,34816
TR 206,14,35072
Mvaa 120,184,4
DM 20,28,79,0
Mvaa 124,184,8
s_mov_b32 s79,2629632
CV 205,206,207
CV 206,208,209
Mvaa 128,184,12
M0 77,108128
CV 207,210,211
v_accvgpr_write_b32 a236,v204
Mvaa 132,184,16
WL 6
DM 20,28,79,0
Mvaa 40,188,0
s_mov_b32 s79,2637824
TR 208,14,34824
TR 210,14,35080
Mvaa 56,188,4
M0 77,109216
v_accvgpr_write_b32 a237,v205
v_accvgpr_write_b32 a238,v206
Mvaa 72,188,8
DM 20,28,79,0
Mvaa 88,188,12
s_mov_b32 s79,2646016
v_accvgpr_write_b32 a239,v207
AR 196,124
Mvaa 104,188,16
M0 77,110304
AR 197,125
AR 198,126
Mvaa 136,192,0
DM 20,28,79,0
Mvaa 140,192,4
M0 78,46240
AR 199,127
AR 200,144
Mvaa 144,192,8
AR 201,145
AR 202,146
Mvaa 148,192,12
DN 19,32,7,0
Mvaa 152,192,16
WL 4
AR 203,147
v_mul_f32 v196,s84,v196
Mvaa 44,196,0
s_waitcnt vmcnt(11) lgkmcnt(0)
s_barrier
Mvaa 60,196,4
s_nop 0
TR 180,14,0
TR 182,14,256
Mvaa 76,196,8
RA 20,18,0
v_mul_f32 v197,s84,v197
Mvaa 92,196,12
RA 24,18,2048
v_mul_f32 v198,s84,v198
Mvaa 108,196,16
RA 28,18,4096
v_mul_f32 v199,s84,v199
Mvaa 156,200,0
s_mov_b32 s79,2883584
RA 32,18,6144
v_mul_f32 v200,s84,v200
Mvaa 160,200,4
M0 77,124352
RA 36,18,8192
v_mul_f32 v201,s84,v201
Mvaa 164,200,8
s_nop 0
TR 184,14,8
TR 186,14,264
Mvaa 168,200,12
s_nop 0
TR 188,14,64
TR 190,14,320
Mvaa 172,200,16
DM 20,28,79,0
Mvaa 48,204,0
s_mov_b32 s79,2891776
TR 192,14,72
TR 194,14,328
Mvaa 64,204,4
M0 77,125440
v_mul_f32 v202,s84,v202
v_mul_f32 v203,s84,v203
Mvaa 80,204,8
DM 20,28,79,0
Mvaa 96,204,12
s_mov_b32 s79,2899968
CV 196,196,197
CV 197,198,199
Mvaa 112,204,16
M0 77,126528
CV 198,200,201
CV 199,202,203
Mvaa 176,208,0
s_nop 0
TR 196,14,128
TR 198,14,384
Mvaa 180,208,4
DM 20,28,79,0
Mvaa 184,208,8
s_mov_b32 s79,2908160
v_accvgpr_write_b32 a240,v196
v_accvgpr_write_b32 a241,v197
Mvaa 188,208,12
M0 77,127616
v_accvgpr_write_b32 a242,v198
v_accvgpr_write_b32 a243,v199
Mvaa 192,208,16
WL 6
DM 20,28,79,0
Mvaa 36,180,20
s_nop 0
TR 200,14,136
TR 202,14,392
Mvaa 52,180,24
s_add_u32 s98,s71,40960
v_lshl_add_u32 v8,v2,1,s98
AR 204,128
Mvaa 68,180,28
buffer_load_dword v8,v8,s[60:63],0 offen
Mvaa 84,180,32
AR 205,129
AR 206,130
Mvaa 100,180,36
AR 207,131
AR 208,148
Mvaa 116,184,20
s_nop 0
TR 204,14,192
TR 206,14,448
Mvaa 120,184,24
AR 209,149
AR 210,150
Mvaa 124,184,28
AR 211,151
v_mul_f32 v204,s84,v204
Mvaa 128,184,32
M0 78,50464
v_mul_f32 v205,s84,v205
v_mul_f32 v206,s84,v206
Mvaa 132,184,36
WL 6
DN 33,32,7,0
Mvaa 40,188,20
s_nop 0
TR 208,14,200
TR 210,14,456
Mvaa 56,188,24
v_mul_f32 v207,s84,v207
v_mul_f32 v208,s84,v208
Mvaa 72,188,28
v_mul_f32 v209,s84,v209
v_mul_f32 v210,s84,v210
Mvaa 88,188,32
v_mul_f32 v211,s84,v211
CV 204,204,205
Mvaa 104,188,36
CV 205,206,207
CV 206,208,209
Mvaa 136,192,20
CV 207,210,211
v_accvgpr_write_b32 a244,v204
Mvaa 140,192,24
v_accvgpr_write_b32 a245,v205
v_accvgpr_write_b32 a246,v206
Mvaa 144,192,28
v_accvgpr_write_b32 a247,v207
AR 196,132
Mvaa 148,192,32
AR 197,133
AR 198,134
Mvaa 152,192,36
WL 4
AR 199,135
AR 200,152
Mvaa 44,196,20
s_waitcnt vmcnt(7) lgkmcnt(0)
s_barrier
Mvaa 60,196,24
s_nop 0
TR 180,14,17312
TR 182,14,17568
Mvaa 76,196,28
RA 0,15,0
AR 201,153
Mvaa 92,196,32
RA 4,15,2048
AR 202,154
Mvaa 108,196,36
RA 8,15,4096
AR 203,155
Mvaa 156,200,20
s_mov_b32 s79,768
RA 12,15,6144
v_mul_f32 v196,s84,v196
Mvaa 160,200,24
M0 69,141664
RA 16,15,8192
v_mul_f32 v197,s84,v197
Mvaa 164,200,28
s_nop 0
TR 184,14,17320
TR 186,14,17576
Mvaa 168,200,32
s_nop 0
TR 188,14,17376
TR 190,14,17632
Mvaa 172,200,36
DM 21,24,79,0
Mvaa 48,204,20
s_mov_b32 s79,37632
TR 192,14,17384
TR 194,14,17640
Mvaa 64,204,24
M0 69,145760
v_mul_f32 v198,s84,v198
v_mul_f32 v199,s84,v199
Mvaa 80,204,28
DM 21,24,79,0
Mvaa 96,204,32
s_mov_b32 s79,74496
v_mul_f32 v200,s84,v200
v_mul_f32 v201,s84,v201
Mvaa 112,204,36
M0 69,149856
v_mul_f32 v202,s84,v202
v_mul_f32 v203,s84,v203
Mvaa 176,208,20
s_nop 0
TR 196,14,17440
TR 198,14,17696
Mvaa 180,208,24
DM 21,24,79,0
Mvaa 184,208,28
s_mov_b32 s79,111360
CV 196,196,197
CV 197,198,199
Mvaa 188,208,32
M0 69,153952
CV 198,200,201
CV 199,202,203
Mvaa 192,208,36
WL 6
DM 21,24,79,0
Mvaa 36,180,0
s_mov_b32 s79,148224
TR 200,14,17448
TR 202,14,17704
Mvaa 52,180,4
M0 69,158048
v_accvgpr_write_b32 a248,v196
v_accvgpr_write_b32 a249,v197
Mvaa 68,180,8
DM 21,24,79,0
Mvaa 84,180,12
s_mov_b32 s79,3145728
v_accvgpr_write_b32 a250,v198
v_accvgpr_write_b32 a251,v199
Mvaa 100,180,16
M0 77,89728
AR 204,136
AR 205,137
Mvaa 116,184,0
s_nop 0
TR 204,14,17504
TR 206,14,17760
Mvaa 120,184,4
DM 20,28,79,0
Mvaa 124,184,8
s_mov_b32 s79,3153920
AR 206,138
AR 207,139
Mvaa 128,184,12
M0 77,90816
AR 208,156
AR 209,157
Mvaa 132,184,16
WL 6
DM 20,28,79,0
Mvaa 40,188,0
s_mov_b32 s79,3162112
TR 208,14,17512
TR 210,14,17768
Mvaa 56,188,4
M0 77,91904
AR 210,158
AR 211,159
Mvaa 72,188,8
DM 20,28,79,0
Mvaa 88,188,12
s_mov_b32 s79,3170304
v_mul_f32 v204,s84,v204
v_mul_f32 v205,s84,v205
Mvaa 104,188,16
M0 77,92992
v_mul_f32 v206,s84,v206
v_mul_f32 v207,s84,v207
Mvaa 136,192,0
DM 20,28,79,0
Mvaa 140,192,4
M0 78,55104
v_mul_f32 v208,s84,v208
v_mul_f32 v209,s84,v209
Mvaa 144,192,8
v_mul_f32 v210,s84,v210
v_mul_f32 v211,s84,v211
Mvaa 148,192,12
DN 19,36,4,0
Mvaa 152,192,16
WL 4
CV 204,204,205
CV 205,206,207
Mvaa 44,196,0
s_waitcnt vmcnt(12) lgkmcnt(0)
s_barrier
Mvaa 60,196,4
s_nop 0
TR 180,14,34624
TR 182,14,34880
Mvaa 76,196,8
RA 20,16,0
CV 206,208,209
Mvaa 92,196,12
RA 24,16,2048
CV 207,210,211
Mvaa 108,196,16
RA 28,16,4096
v_accvgpr_write_b32 a252,v204
Mvaa 156,200,0
s_mov_b32 s79,3407872
RA 32,16,6144
v_accvgpr_write_b32 a253,v205
Mvaa 160,200,4
M0 77,107040
RA 36,16,8192
v_accvgpr_write_b32 a254,v206
Mvaa 164,200,8
s_nop 0
TR 184,14,34632
TR 186,14,34888
Mvaa 168,200,12
s_nop 0
TR 188,14,34688
TR 190,14,34944
Mvaa 172,200,16
DM 20,28,79,0
Mvaa 48,204,0
s_mov_b32 s79,3416064
TR 192,14,34696
TR 194,14,34952
Mvaa 64,204,4
M0 77,108128
v_accvgpr_write_b32 a255,v207
Mvaa 80,204,8
DM 20,28,79,0
Mvaa 96,204,12
s_mov_b32 s79,3424256
Mvaa 112,204,16
M0 77,109216
Mvaa 176,208,0
s_nop 0
TR 196,14,34752
TR 198,14,35008
Mvaa 180,208,4
DM 20,28,79,0
Mvaa 184,208,8
s_mov_b32 s79,3432448
Mvaa 188,208,12
M0 77,110304
Mvaa 192,208,16
WL 6
DM 20,28,79,0
Mvaa 36,180,20
s_nop 0
TR 200,14,34760
TR 202,14,35016
Mvaa 52,180,24
Mvaa 68,180,28
Mvaa 84,180,32
Mvaa 100,180,36
Mvaa 116,184,20
s_nop 0
TR 204,14,34816
TR 206,14,35072
Mvaa 120,184,24
Mvaa 124,184,28
Mvaa 128,184,32
M0 78,59328
Mvaa 132,184,36
WL 6
DN 33,36,4,0
Mvaa 40,188,20
s_nop 0
TR 208,14,34824
TR 210,14,35080
Mvaa 56,188,24
Mvaa 72,188,28
Mvaa 88,188,32
Mvaa 104,188,36
Mvaa 136,192,20
Mvaa 140,192,24
Mvaa 144,192,28
Mvaa 148,192,32
Mvaa 152,192,36
WL 4
Mvaa 44,196,20
s_waitcnt vmcnt(6) lgkmcnt(0)
s_barrier
Mvaa 60,196,24
s_nop 0
TR 180,14,0
TR 182,14,256
Mvaa 76,196,28
RA 0,17,0
Mvaa 92,196,32
RA 4,17,2048
Mvaa 108,196,36
RA 8,17,4096
Mvaa 156,200,20
s_mov_b32 s79,896
RA 12,17,6144
Mvaa 160,200,24
M0 69,0
RA 16,17,8192
Mvaa 164,200,28
s_nop 0
TR 184,14,8
TR 186,14,264
Mvaa 168,200,32
s_nop 0
TR 188,14,64
TR 190,14,320
Mvaa 172,200,36
DM 21,24,79,0
Mvaa 48,204,20
s_mov_b32 s79,37760
TR 192,14,72
TR 194,14,328
Mvaa 64,204,24
M0 69,4096
Mvaa 80,204,28
DM 21,24,79,0
Mvaa 96,204,32
s_mov_b32 s79,74624
Mvaa 112,204,36
M0 69,8192
Mvaa 176,208,20
s_nop 0
TR 196,14,128
TR 198,14,384
Mvaa 180,208,24
DM 21,24,79,0
Mvaa 184,208,28
s_mov_b32 s79,111488
Mvaa 188,208,32
M0 69,12288
Mvaa 192,208,36
WL 6
DM 21,24,79,0
Mvaa 36,180,0
s_mov_b32 s79,148352
TR 200,14,136
TR 202,14,392
Mvaa 52,180,4
M0 69,16384
Mvaa 68,180,8
DM 21,24,79,0
Mvaa 84,180,12
s_mov_b32 s79,3670016
Mvaa 100,180,16
M0 77,124352
Mvaa 116,184,0
s_nop 0
TR 204,14,192
TR 206,14,448
Mvaa 120,184,4
DM 20,28,79,0
Mvaa 124,184,8
s_mov_b32 s79,3678208
Mvaa 128,184,12
M0 77,125440
Mvaa 132,184,16
WL 6
DM 20,28,79,0
Mvaa 40,188,0
s_mov_b32 s79,3686400
TR 208,14,200
TR 210,14,456
Mvaa 56,188,4
M0 77,126528
Mvaa 72,188,8
DM 20,28,79,0
Mvaa 88,188,12
s_mov_b32 s79,3694592
Mvaa 104,188,16
M0 77,127616
Mvaa 136,192,0
DM 20,28,79,0
Mvaa 140,192,4
M0 78,63552
Mvaa 144,192,8
Mvaa 148,192,12
DN 19,36,5,0
Mvaa 152,192,16
WL 4
Mvaa 44,196,0
s_waitcnt vmcnt(11) lgkmcnt(0)
s_barrier
Mvaa 60,196,4
s_nop 0
TR 180,14,17312
TR 182,14,17568
Mvaa 76,196,8
RA 20,18,0
Mvaa 92,196,12
RA 24,18,2048
Mvaa 108,196,16
RA 28,18,4096
Mvaa 156,200,0
s_mov_b32 s79,3932160
RA 32,18,6144
Mvaa 160,200,4
M0 77,89728
RA 36,18,8192
Mvaa 164,200,8
s_nop 0
TR 184,14,17320
TR 186,14,17576
Mvaa 168,200,12
s_nop 0
TR 188,14,17376
TR 190,14,17632
Mvaa 172,200,16
DM 20,28,79,0
Mvaa 48,204,0
s_mov_b32 s79,3940352
TR 192,14,17384
TR 194,14,17640
Mvaa 64,204,4
M0 77,90816
Mvaa 80,204,8
DM 20,28,79,0
Mvaa 96,204,12
s_mov_b32 s79,3948544
Mvaa 112,204,16
M0 77,91904
Mvaa 176,208,0
s_nop 0
TR 196,14,17440
TR 198,14,17696
Mvaa 180,208,4
DM 20,28,79,0
Mvaa 184,208,8
s_mov_b32 s79,3956736
Mvaa 188,208,12
M0 77,92992
Mvaa 192,208,16
WL 6
DM 20,28,79,0
Mvaa 36,180,20
s_nop 0
TR 200,14,17448
TR 202,14,17704
Mvaa 52,180,24
Mvaa 68,180,28
Mvaa 84,180,32
Mvaa 100,180,36
Mvaa 116,184,20
s_nop 0
TR 204,14,17504
TR 206,14,17760
Mvaa 120,184,24
Mvaa 124,184,28
Mvaa 128,184,32
M0 78,67776
Mvaa 132,184,36
WL 6
DN 33,36,5,0
Mvaa 40,188,20
s_nop 0
TR 208,14,17512
TR 210,14,17768
Mvaa 56,188,24
Mvaa 72,188,28
Mvaa 88,188,32
Mvaa 104,188,36
Mvaa 136,192,20
Mvaa 140,192,24
Mvaa 144,192,28
Mvaa 148,192,32
Mvaa 152,192,36
WL 4
Mvaa 44,196,20
s_waitcnt vmcnt(6) lgkmcnt(0)
s_barrier
Mvaa 60,196,24
s_nop 0
TR 180,14,34624
TR 182,14,34880
Mvaa 76,196,28
RA 0,15,0
Mvaa 92,196,32
RA 4,15,2048
Mvaa 108,196,36
RA 8,15,4096
Mvaa 156,200,20
s_mov_b32 s79,1024
RA 12,15,6144
Mvaa 160,200,24
M0 69,141664
RA 16,15,8192
Mvaa 164,200,28
s_nop 0
TR 184,14,34632
TR 186,14,34888
Mvaa 168,200,32
s_nop 0
TR 188,14,34688
TR 190,14,34944
Mvaa 172,200,36
DM 21,24,79,0
Mvaa 48,204,20
s_mov_b32 s79,37888
TR 192,14,34696
TR 194,14,34952
Mvaa 64,204,24
M0 69,145760
Mvaa 80,204,28
DM 21,24,79,0
Mvaa 96,204,32
s_mov_b32 s79,74752
Mvaa 112,204,36
M0 69,149856
Mvaa 176,208,20
s_nop 0
TR 196,14,34752
TR 198,14,35008
Mvaa 180,208,24
DM 21,24,79,0
Mvaa 184,208,28
s_mov_b32 s79,111616
Mvaa 188,208,32
M0 69,153952
Mvaa 192,208,36
WL 6
DM 21,24,79,0
Mvaa 36,180,0
s_mov_b32 s79,148480
TR 200,14,34760
TR 202,14,35016
Mvaa 52,180,4
M0 69,158048
Mvaa 68,180,8
DM 21,24,79,0
Mvaa 84,180,12
s_mov_b32 s79,4194304
Mvaa 100,180,16
M0 77,107040
Mvaa 116,184,0
s_nop 0
TR 204,14,34816
TR 206,14,35072
Mvaa 120,184,4
DM 20,28,79,0
Mvaa 124,184,8
s_mov_b32 s79,4202496
Mvaa 128,184,12
M0 77,108128
Mvaa 132,184,16
WL 6
DM 20,28,79,0
Mvaa 40,188,0
s_mov_b32 s79,4210688
TR 208,14,34824
TR 210,14,35080
Mvaa 56,188,4
M0 77,109216
Mvaa 72,188,8
DM 20,28,79,0
Mvaa 88,188,12
s_mov_b32 s79,4218880
Mvaa 104,188,16
M0 77,110304
Mvaa 136,192,0
DM 20,28,79,0
Mvaa 140,192,4
Mvaa 144,192,8
Mvaa 148,192,12
Mvaa 152,192,16
WL 4
Mvaa 44,196,0
s_waitcnt vmcnt(10) lgkmcnt(0)
s_barrier
Mvaa 60,196,4
s_nop 0
TR 180,14,0
TR 182,14,256
Mvaa 76,196,8
RA 20,16,0
Mvaa 92,196,12
s_add_u32 s4,s80,16384
RA 24,16,2048
Mvaa 108,196,16
s_and_b32 s4,s4,0x1e000
RA 28,16,4096
Mvaa 156,200,0
s_xor_b32 s5,s4,0x30000
RA 32,16,6144
Mvaa 160,200,4
M0 78,72416
RA 36,16,8192
Mvaa 164,200,8
s_nop 0
TR 184,14,8
TR 186,14,264
Mvaa 168,200,12
s_nop 0
TR 188,14,64
TR 190,14,320
Mvaa 172,200,16
DN 19,32,4,0
Mvaa 48,204,0
s_mov_b32 s79,4456448
TR 192,14,72
TR 194,14,328
Mvaa 64,204,4
M0 77,124352
Mvaa 80,204,8
DM 20,28,79,0
Mvaa 96,204,12
s_mov_b32 s79,4464640
Mvaa 112,204,16
M0 77,125440
Mvaa 176,208,0
s_nop 0
TR 196,14,128
TR 198,14,384
Mvaa 180,208,4
DM 20,28,79,0
Mvaa 184,208,8
s_mov_b32 s79,4472832
Mvaa 188,208,12
M0 77,126528
Mvaa 192,208,16
WL 6
DM 20,28,79,0
Mvaa 36,180,20
s_mov_b32 s79,4481024
TR 200,14,136
TR 202,14,392
Mvaa 52,180,24
M0 77,127616
Mvaa 68,180,28
DM 20,28,79,0
Mvaa 84,180,32
Mvaa 100,180,36
Mvaa 116,184,20
s_nop 0
TR 204,14,192
TR 206,14,448
Mvaa 120,184,24
Mvaa 124,184,28
Mvaa 128,184,32
M0 78,76640
Mvaa 132,184,36
WL 6
DN 33,32,4,0
Mvaa 40,188,20
s_nop 0
TR 208,14,200
TR 210,14,456
Mvaa 56,188,24
Mvaa 72,188,28
Mvaa 88,188,32
Mvaa 104,188,36
Mvaa 136,192,20
Mvaa 140,192,24
Mvaa 144,192,28
Mvaa 148,192,32
Mvaa 152,192,36
WL 4
Mvaa 44,196,20
s_waitcnt vmcnt(6) lgkmcnt(0)
s_barrier
Mvaa 60,196,24
s_nop 0
TR 180,14,17312
TR 182,14,17568
Mvaa 76,196,28
RA 0,17,0
Mvaa 92,196,32
RA 4,17,2048
Mvaa 108,196,36
RA 8,17,4096
Mvaa 156,200,20
RA 12,17,6144
Mvaa 160,200,24
M0 78,89728
RA 16,17,8192
Mvaa 164,200,28
s_nop 0
TR 184,14,17320
TR 186,14,17576
Mvaa 168,200,32
s_nop 0
TR 188,14,17376
TR 190,14,17632
Mvaa 172,200,36
DN 19,36,6,0
Mvaa 48,204,20
M0 78,93952
TR 192,14,17384
TR 194,14,17640
Mvaa 64,204,24
Mvaa 80,204,28
DN 33,36,6,0
Mvaa 96,204,32
M0 78,98176
Mvaa 112,204,36
Mvaa 176,208,20
s_nop 0
TR 196,14,17440
TR 198,14,17696
Mvaa 180,208,24
DN 19,36,7,0
Mvaa 184,208,28
M0 78,102400
Mvaa 188,208,32
Mvaa 192,208,36
WL 6
DN 33,36,7,0
Mvaa 36,180,0
s_nop 0
TR 200,14,17448
TR 202,14,17704
Mvaa 52,180,4
Mvaa 68,180,8
Mvaa 84,180,12
Mvaa 100,180,16
Mvaa 116,184,0
s_nop 0
TR 204,14,17504
TR 206,14,17760
Mvaa 120,184,4
Mvaa 124,184,8
Mvaa 128,184,12
M0 78,80864
Mvaa 132,184,16
WL 6
DN 19,32,5,0
Mvaa 40,188,0
s_nop 0
TR 208,14,17512
TR 210,14,17768
Mvaa 56,188,4
Mvaa 72,188,8
Mvaa 88,188,12
Mvaa 104,188,16
Mvaa 136,192,0
Mvaa 140,192,4
Mvaa 144,192,8
Mvaa 148,192,12
Mvaa 152,192,16
WL 4
Mvaa 44,196,0
s_waitcnt vmcnt(6) lgkmcnt(0)
s_barrier
Mvaa 60,196,4
s_nop 0
TR 180,14,34624
TR 182,14,34880
Mvaa 76,196,8
RA 20,18,0
Mvaa 92,196,12
RA 24,18,2048
Mvaa 108,196,16
RA 28,18,4096
Mvaa 156,200,0
RA 32,18,6144
Mvaa 160,200,4
RA 36,18,8192
Mvaa 164,200,8
s_nop 0
TR 184,14,34632
TR 186,14,34888
Mvaa 168,200,12
s_nop 0
TR 188,14,34688
TR 190,14,34944
Mvaa 172,200,16
Mvaa 48,204,0
s_nop 0
TR 192,14,34696
TR 194,14,34952
Mvaa 64,204,4
Mvaa 80,204,8
Mvaa 96,204,12
Mvaa 112,204,16
Mvaa 176,208,0
s_nop 0
TR 196,14,34752
TR 198,14,35008
Mvaa 180,208,4
Mvaa 184,208,8
Mvaa 188,208,12
Mvaa 192,208,16
WL 6
Mvaa 36,180,20
s_nop 0
TR 200,14,34760
TR 202,14,35016
Mvaa 52,180,24
Mvaa 68,180,28
Mvaa 84,180,32
Mvaa 100,180,36
Mvaa 116,184,20
s_nop 0
TR 204,14,34816
TR 206,14,35072
Mvaa 120,184,24
Mvaa 124,184,28
Mvaa 128,184,32
M0 78,85088
v_mul_f32 v36,s84,v36
v_mul_f32 v37,s84,v37
Mvaa 132,184,36
WL 6
DN 33,32,5,0
Mvaa 40,188,20
s_add_u32 s6,s80,24576
TR 208,14,34824
TR 210,14,35080
Mvaa 56,188,24
s_and_b32 s6,s6,0x1e000
v_mul_f32 v38,s84,v38
v_mul_f32 v39,s84,v39
Mvaa 72,188,28
s_xor_b32 s7,s6,0x30000
CV 36,36,37
CV 37,38,39
Mvaa 88,188,32
M0 78,107040
v_mul_f32 v116,s84,v116
v_mul_f32 v117,s84,v117
Mvaa 104,188,36
DN 19,32,6,0
Mvaa 136,192,20
M0 78,111264
v_mul_f32 v118,s84,v118
v_mul_f32 v119,s84,v119
Mvaa 140,192,24
CV 38,116,117
CV 39,118,119
Mvaa 144,192,28
DN 33,32,6,0
Mvaa 148,192,32
v_mul_f32 v52,s84,v52
v_mul_f32 v53,s84,v53
Mvaa 152,192,36
WL 4
v_mul_f32 v54,s84,v54
v_mul_f32 v55,s84,v55
v_mul_f32 v56,s84,v56
Mvaa 44,196,20
WL 0
s_barrier
v_mul_f32 v57,s84,v57
Mvaa 60,196,24
M0 78,115488
RA 180,10,0
CV 52,52,53
v_mul_f32 v58,s84,v58
Mvaa 76,196,28
s_add_u32 s98,s71,49152
RA 184,10,64
v_lshl_add_u32 v8,v2,1,s98
v_mul_f32 v59,s84,v59
Mvaa 92,196,32
DN 19,32,7,0
CV 56,56,57
Mvaa 108,196,36
M0 78,119712
CV 53,54,55
v_mul_f32 v120,s84,v120
CV 57,58,59
Mvaa 156,200,20
RA 188,10,128
v_mul_f32 v121,s84,v121
v_mul_f32 v140,s84,v140
Mvaa 160,200,24
DN 33,32,7,0
v_mul_f32 v141,s84,v141
Mvaa 164,200,28
M0 78,124352
v_mul_f32 v122,s84,v122
v_mul_f32 v123,s84,v123
v_mul_f32 v142,s84,v142
Mvaa 168,200,32
CV 54,120,121
CV 55,122,123
v_mul_f32 v143,s84,v143
Mvaa 172,200,36
DN 19,36,4,0
CV 58,140,141
Mvaa 48,204,20
M0 78,128576
RA 192,10,192
v_mul_f32 v68,s84,v68
CV 59,142,143
Mvaa 64,204,24
v_mul_f32 v69,s84,v69
v_mul_f32 v70,s84,v70
v_mul_f32 v104,s84,v104
Mvaa 80,204,28
DN 33,36,4,0
v_mul_f32 v105,s84,v105
Mvaa 96,204,32
M0 78,132800
v_mul_f32 v71,s84,v71
CV 68,68,69
v_mul_f32 v106,s84,v106
Mvaa 112,204,36
buffer_load_dword v8,v8,s[60:63],0 offen
v_mul_f32 v107,s84,v107
Mvaa 176,208,20
RA 196,10,512
CV 69,70,71
CV 104,104,105
Mvaa 180,208,24
DN 19,36,5,0
CV 105,106,107
Mvaa 184,208,28
M0 78,137024
v_mul_f32 v124,s84,v124
v_mul_f32 v125,s84,v125
v_mul_f32 v152,s84,v152
Mvaa 188,208,32
v_mul_f32 v126,s84,v126
v_mul_f32 v127,s84,v127
v_mul_f32 v153,s84,v153
Mvaa 192,208,36
WL 3
DN 33,36,5,0
v_mul_f32 v154,s84,v154
Mvavz 116,180,36
s_add_u32 s4,s80,32768
RA 200,10,576
CV 70,124,125
CV 71,126,127
Mvavz 120,180,52
s_and_b32 s4,s4,0x1e000
v_mul_f32 v84,s84,v84
v_mul_f32 v85,s84,v85
v_mul_f32 v86,s84,v86
v_mul_f32 v87,s84,v87
CV 84,84,85
CV 85,86,87
v_mul_f32 v128,s84,v128
v_mul_f32 v129,s84,v129
v_mul_f32 v130,s84,v130
v_mul_f32 v131,s84,v131
CV 86,128,129
CV 87,130,131
Mvavz 124,180,68
s_xor_b32 s5,s4,0x30000
v_mul_f32 v100,s84,v100
v_mul_f32 v101,s84,v101
v_mul_f32 v102,s84,v102
v_mul_f32 v103,s84,v103
CV 100,100,101
CV 101,102,103
v_mul_f32 v132,s84,v132
v_mul_f32 v133,s84,v133
v_mul_f32 v134,s84,v134
v_mul_f32 v135,s84,v135
CV 102,132,133
CV 103,134,135
Mvavz 128,180,84
M0 78,141664
v_mul_f32 v40,s84,v40
v_mul_f32 v41,s84,v41
v_mul_f32 v42,s84,v42
v_mul_f32 v43,s84,v43
CV 40,40,41
CV 41,42,43
v_mul_f32 v136,s84,v136
v_mul_f32 v137,s84,v137
v_mul_f32 v138,s84,v138
v_mul_f32 v139,s84,v139
CV 42,136,137
CV 43,138,139
Mvavz 132,180,100
DN 19,32,4,0
v_mul_f32 v155,s84,v155
Mvav 116,184,40
M0 78,145888
RA 204,10,640
v_mul_f32 v72,s84,v72
v_mul_f32 v73,s84,v73
v_mul_f32 v74,s84,v74
v_mul_f32 v75,s84,v75
CV 72,72,73
CV 73,74,75
v_mul_f32 v144,s84,v144
v_mul_f32 v145,s84,v145
v_mul_f32 v146,s84,v146
v_mul_f32 v147,s84,v147
CV 74,144,145
CV 75,146,147
Mvav 120,184,56
v_mul_f32 v88,s84,v88
v_mul_f32 v89,s84,v89
v_mul_f32 v90,s84,v90
v_mul_f32 v91,s84,v91
CV 88,88,89
CV 89,90,91
v_mul_f32 v148,s84,v148
v_mul_f32 v149,s84,v149
v_mul_f32 v150,s84,v150
v_mul_f32 v151,s84,v151
CV 90,148,149
CV 91,150,151
Mvav 124,184,72
DN 33,32,4,0
CV 106,152,153
CV 107,154,155
Mvav 128,184,88
M0 78,150112
v_mul_f32 v44,s84,v44
v_mul_f32 v45,s84,v45
v_mul_f32 v46,s84,v46
v_mul_f32 v47,s84,v47
CV 44,44,45
CV 45,46,47
v_mul_f32 v156,s84,v156
v_mul_f32 v157,s84,v157
v_mul_f32 v158,s84,v158
v_mul_f32 v159,s84,v159
CV 46,156,157
CV 47,158,159
Mvav 132,184,104
WL 3
v_mul_f32 v60,s84,v60
v_mul_f32 v61,s84,v61
v_mul_f32 v62,s84,v62
v_mul_f32 v63,s84,v63
CV 60,60,61
CV 61,62,63
v_mul_f32 v160,s84,v160
v_mul_f32 v161,s84,v161
v_mul_f32 v162,s84,v162
v_mul_f32 v163,s84,v163
CV 62,160,161
CV 63,162,163
Mvav 116,188,44
RA 208,10,704
v_mul_f32 v76,s84,v76
v_mul_f32 v77,s84,v77
v_mul_f32 v78,s84,v78
v_mul_f32 v79,s84,v79
CV 76,76,77
CV 77,78,79
v_mul_f32 v164,s84,v164
v_mul_f32 v165,s84,v165
v_mul_f32 v166,s84,v166
v_mul_f32 v167,s84,v167
CV 78,164,165
CV 79,166,167
Mvav 120,188,60
DN 19,32,5,0
v_mul_f32 v92,s84,v92
v_mul_f32 v93,s84,v93
v_mul_f32 v94,s84,v94
v_mul_f32 v95,s84,v95
CV 92,92,93
CV 93,94,95
v_mul_f32 v168,s84,v168
v_mul_f32 v169,s84,v169
v_mul_f32 v170,s84,v170
v_mul_f32 v171,s84,v171
CV 94,168,169
CV 95,170,171
Mvav 124,188,76
M0 78,154336
v_mul_f32 v108,s84,v108
v_mul_f32 v109,s84,v109
v_mul_f32 v110,s84,v110
v_mul_f32 v111,s84,v111
CV 108,108,109
CV 109,110,111
v_mul_f32 v172,s84,v172
v_mul_f32 v173,s84,v173
v_mul_f32 v174,s84,v174
v_mul_f32 v175,s84,v175
CV 110,172,173
CV 111,174,175
Mvav 128,188,92
v_mul_f32 v48,s84,v48
v_mul_f32 v49,s84,v49
v_mul_f32 v50,s84,v50
v_mul_f32 v51,s84,v51
CV 48,48,49
CV 49,50,51
v_mul_f32 v176,s84,v176
v_mul_f32 v177,s84,v177
v_mul_f32 v178,s84,v178
v_mul_f32 v179,s84,v179
CV 50,176,177
CV 51,178,179
Mvav 132,188,108
DN 33,32,5,0
v_mul_f32 v64,s84,v64
v_mul_f32 v65,s84,v65
v_mul_f32 v66,s84,v66
v_mul_f32 v67,s84,v67
CV 64,64,65
CV 65,66,67
v_mul_f32 v180,s84,v180
v_mul_f32 v181,s84,v181
v_mul_f32 v182,s84,v182
v_mul_f32 v183,s84,v183
CV 66,180,181
CV 67,182,183
Mvav 116,192,48
v_mul_f32 v80,s84,v80
v_mul_f32 v81,s84,v81
v_mul_f32 v82,s84,v82
v_mul_f32 v83,s84,v83
CV 80,80,81
CV 81,82,83
v_mul_f32 v184,s84,v184
v_mul_f32 v185,s84,v185
v_mul_f32 v186,s84,v186
v_mul_f32 v187,s84,v187
CV 82,184,185
CV 83,186,187
Mvav 120,192,64
v_mul_f32 v96,s84,v96
v_mul_f32 v97,s84,v97
v_mul_f32 v98,s84,v98
v_mul_f32 v99,s84,v99
CV 96,96,97
CV 97,98,99
v_mul_f32 v188,s84,v188
v_mul_f32 v189,s84,v189
v_mul_f32 v190,s84,v190
v_mul_f32 v191,s84,v191
CV 98,188,189
CV 99,190,191
Mvav 124,192,80
v_mul_f32 v112,s84,v112
v_mul_f32 v113,s84,v113
v_mul_f32 v114,s84,v114
v_mul_f32 v115,s84,v115
CV 112,112,113
CV 113,114,115
v_mul_f32 v192,s84,v192
v_mul_f32 v193,s84,v193
v_mul_f32 v194,s84,v194
v_mul_f32 v195,s84,v195
CV 114,192,193
CV 115,194,195
Mvav 128,192,96
s_nop 0
Mvav 132,192,112
WL 2
s_cmp_lg_u32 s85,0
s_cbranch_scc1 LB_safe0
LB_back0:
v_max3_f32 v196,v116,v117,v118
v_max3_f32 v200,v120,v121,v122
v_max3_f32 v204,v124,v125,v126
v_max3_f32 v208,v128,v129,v130
s_nop 0
v_max3_f32 v212,v132,v133,v134
v_max3_f32 v196,v196,v119,v22
v_max3_f32 v200,v200,v123,v23
v_max3_f32 v204,v204,v127,v24
v_max3_f32 v208,v208,v131,v25
v_max3_f32 v212,v212,v135,v26
v_mov_b32 v197,v196
v_mov_b32 v201,v200
v_mov_b32 v205,v204
v_mov_b32 v209,v208
v_mov_b32 v213,v212
v_permlane16_swap_b32 v196,v197
v_permlane16_swap_b32 v200,v201
v_permlane16_swap_b32 v204,v205
v_permlane16_swap_b32 v208,v209
v_permlane16_swap_b32 v212,v213
v_max_f32 v196,v196,v197
v_max_f32 v200,v200,v201
v_max_f32 v204,v204,v205
v_max_f32 v208,v208,v209
v_max_f32 v212,v212,v213
v_mov_b32 v197,v196
v_mov_b32 v201,v200
v_mov_b32 v205,v204
v_mov_b32 v209,v208
v_mov_b32 v213,v212
v_permlane32_swap_b32 v196,v197
v_permlane32_swap_b32 v200,v201
v_permlane32_swap_b32 v204,v205
v_permlane32_swap_b32 v208,v209
v_permlane32_swap_b32 v212,v213
v_max_f32 v196,v196,v197
v_max_f32 v200,v200,v201
v_max_f32 v204,v204,v205
v_max_f32 v208,v208,v209
v_max_f32 v212,v212,v213
v_sub_f32 v199,0,v196
v_sub_f32 v198,0,v196
v_sub_f32 v197,0,v196
v_sub_f32 v196,0,v196
v_sub_f32 v203,0,v200
v_sub_f32 v202,0,v200
v_sub_f32 v201,0,v200
v_sub_f32 v200,0,v200
v_sub_f32 v207,0,v204
v_sub_f32 v206,0,v204
v_sub_f32 v205,0,v204
v_sub_f32 v204,0,v204
v_sub_f32 v211,0,v208
v_sub_f32 v210,0,v208
v_sub_f32 v209,0,v208
v_sub_f32 v208,0,v208
v_sub_f32 v215,0,v212
v_sub_f32 v214,0,v212
v_sub_f32 v213,0,v212
v_sub_f32 v212,0,v212
Mvavc 136,196,36,196
s_memrealtime s[94:95]
WL 0
s_barrier
s_add_u32 s96,s94,115
Mvav 136,200,40
M0 78,20480
RA 180,10,17312
v_add_f32 v116,v116,v196
Mvavc 140,196,52,200
RA 184,10,17376
v_add_f32 v120,v120,v200
Mvav 140,200,56
DN 19,36,6,0
Mvavc 144,196,68,204
EX 116
Mvav 144,200,72
RA 188,10,17440
v_add_f32 v117,v117,v196
Mvavc 148,196,84,208
EX 117
Mvav 148,200,88
v_add_f32 v118,v118,v196
v_add_f32 v124,v124,v204
CV 156,116,117
Mvavc 152,196,100,212
EX 118
Mvav 152,200,104
EX 120
Mvav 136,204,44
M0 78,24704
RA 192,10,17504
v_add_f32 v119,v119,v196
Mvav 136,208,48
DN 33,36,6,0
Mvav 140,204,60
EX 119
Mvav 140,208,64
v_add_f32 v121,v121,v200
v_add_f32 v128,v128,v208
CV 157,118,119
Mvav 144,204,76
EX 121
Mvav 144,208,80
RA 196,10,17824
v_add_f32 v122,v122,v200
Mvav 148,204,92
EX 122
Mvav 148,208,96
v_add_f32 v123,v123,v200
v_add_f32 v132,v132,v212
Mvav 152,204,108
M0 78,28928
EX 123
Mvav 152,208,112
WL 3
DN 19,36,7,0
CV 161,122,123
Mvavc 116,180,36,196
RA 200,10,17888
CV 160,120,121
Mvav 116,184,40
EX 124
Mvavc 120,180,52,200
v_add_f32 v125,v125,v204
EX 125
v_add_f32 v126,v126,v204
EX 126
v_add_f32 v127,v127,v204
EX 127
Mvav 120,184,56
EX 128
CV 164,124,125
CV 165,126,127
Mvavc 124,180,68,204
v_add_f32 v129,v129,v208
EX 129
v_add_f32 v130,v130,v208
EX 130
v_add_f32 v131,v131,v208
EX 131
Mvav 124,184,72
RA 204,10,17952
CV 168,128,129
CV 169,130,131
Mvavc 128,180,84,208
M0 78,33152
EX 132
v_add_f32 v133,v133,v212
EX 133
v_add_f32 v134,v134,v212
EX 134
v_add_f32 v135,v135,v212
EX 135
Mvav 128,184,88
DN 33,36,7,0
CV 172,132,133
CV 173,134,135
Mvavc 132,180,100,212
EX 136
Mvav 132,184,104
WL 3
EX 137
Mvav 116,188,44
RA 208,10,18016
CV 158,136,137
Mvav 116,192,48
EX 138
Mvav 120,188,60
EX 139
Mvav 120,192,64
EX 140
CV 159,138,139
Mvav 124,188,76
EX 141
Mvav 124,192,80
s_nop 0
TR 180,12,34624
TR 182,12,35136
CV 162,140,141
Mvav 128,188,92
EX 142
Mvav 128,192,96
EX 143
Mvav 132,188,108
EX 144
CV 163,142,143
Mvav 132,192,112
WL 4
EX 145
Mvavc 136,196,36,196
s_nop 0
TR 184,12,34656
TR 186,12,35168
CV 166,144,145
Mvav 136,200,40
EX 146
Mvavc 140,196,52,200
EX 147
Mvav 140,200,56
EX 148
CV 167,146,147
Mvavc 144,196,68,204
EX 149
EX 150
EX 151
Mvav 144,200,72
s_nop 0
TR 188,12,34688
TR 190,12,35200
CV 170,148,149
CV 171,150,151
Mvavc 148,196,84,208
EX 152
EX 153
EX 154
EX 155
Mvav 148,200,88
CV 174,152,153
CV 175,154,155
Mvavc 152,196,100,212
EX 116
Mvav 152,200,104
WL 6
EX 117
Mvav 136,204,44
s_nop 0
TR 192,12,34720
TR 194,12,35232
Mvav 136,208,48
EX 118
Mvav 140,204,60
EX 119
Mvav 140,208,64
EX 120
Mvav 144,204,76
EX 121
Mvav 144,208,80
s_nop 0
TR 196,12,34752
TR 198,12,35264
Mvav 148,204,92
EX 122
Mvav 148,208,96
EX 123
Mvav 152,204,108
EX 124
Mvav 152,208,112
WL 6
EX 125
Maavz 0,180,156
s_nop 0
TR 200,12,34784
TR 202,12,35296
Maavz 4,180,160
EX 126
Maavz 8,180,164
EX 127
Maavz 12,180,168
EX 128
Maavz 16,180,172
EX 129
Maavz 20,184,156
s_nop 0
TR 204,12,34816
TR 206,12,35328
Maavz 24,184,160
EX 130
Maavz 28,184,164
EX 131
Maavz 32,184,168
EX 132
Maavz 36,184,172
WL 6
EX 133
Maavz 40,188,156
s_nop 0
TR 208,12,34848
TR 210,12,35360
Maavz 44,188,160
EX 134
Maavz 48,188,164
EX 135
Maavz 52,188,168
EX 136
Maavz 56,188,172
EX 137
Maavz 60,192,156
EX 138
Maavz 64,192,160
EX 139
Maavz 68,192,164
EX 140
Maavz 72,192,168
EX 141
Maavz 76,192,172
WL 4
EX 142
Maavz 80,196,156
s_memrealtime s[94:95]
s_waitcnt vmcnt(17) lgkmcnt(0)
s_barrier
s_sub_u32 s95,s94,s96
s_cmp_lt_i32 s95,0
s_cbranch_scc1 LB_pc39
LB_pb39:
s_add_u32 s96,s94,115
Maavz 84,196,160
s_add_u32 s6,s80,40960
RA 180,10,51936
CV 176,116,117
Maavz 88,196,164
s_and_b32 s6,s6,0x1e000
RA 184,10,52000
CV 177,118,119
Maavz 92,196,168
s_xor_b32 s7,s6,0x30000
EX 143
Maavz 96,196,172
M0 78,37792
EX 144
Maavz 100,200,156
RA 188,10,52064
CV 180,120,121
Maavz 104,200,160
DN 19,32,6,0
Maavz 108,200,164
EX 145
Maavz 112,200,168
EX 146
Maavz 116,200,172
EX 147
Maavz 120,204,156
RA 192,10,52128
CV 181,122,123
Maavz 124,204,160
EX 148
Maavz 128,204,164
EX 149
Maavz 132,204,168
M0 78,42016
EX 150
Maavz 136,204,172
DN 33,32,6,0
Maavz 140,208,156
RA 196,10,52448
CV 184,124,125
Maavz 144,208,160
EX 151
Maavz 148,208,164
EX 152
Maavz 152,208,168
EX 153
Maavz 156,208,172
EX 154
Maavz 160,212,156
EX 155
Maavz 164,212,160
M0 78,46240
CV 185,126,127
CV 188,128,129
Maavz 168,212,164
DN 19,32,7,0
Maavz 172,212,168
CV 189,130,131
CV 192,132,133
Maavz 176,212,172
WL 3
CV 193,134,135
CV 178,136,137
Mvavc 116,180,36,196
RA 200,10,52512
CV 179,138,139
Mvav 116,184,40
CV 182,140,141
CV 183,142,143
Mvavc 120,180,52,200
CV 186,144,145
CV 187,146,147
Mvav 120,184,56
CV 190,148,149
CV 191,150,151
Mvavc 124,180,68,204
M0 78,50464
CV 195,154,155
Mvav 124,184,72
RA 204,10,52576
CV 194,152,153
Mvavc 128,180,84,208
DN 33,32,7,0
Mvav 128,184,88
Mvavc 132,180,100,212
Mvav 132,184,104
WL 3
Mvav 116,188,44
RA 208,10,52640
Mvav 116,192,48
Mvav 120,188,60
Mvav 120,192,64
M0 78,55104
Mvav 124,188,76
DN 19,36,4,0
Mvav 124,192,80
s_nop 0
TR 180,13,0
TR 182,13,512
Mvav 128,188,92
EX 116
Mvav 128,192,96
EX 117
Mvav 132,188,108
EX 118
Mvav 132,192,112
WL 4
EX 119
Mvavc 136,196,36,196
s_nop 0
TR 184,13,32
TR 186,13,544
Mvav 136,200,40
M0 78,59328
EX 120
Mvavc 140,196,52,200
DN 33,36,4,0
Mvav 140,200,56
EX 121
Mvavc 144,196,68,204
EX 122
Mvav 144,200,72
s_nop 0
TR 188,13,64
TR 190,13,576
Mvavc 148,196,84,208
EX 123
Mvav 148,200,88
EX 124
Mvavc 152,196,100,212
EX 125
Mvav 152,200,104
WL 6
EX 126
Mvav 136,204,44
M0 78,63552
TR 192,13,96
TR 194,13,608
Mvav 136,208,48
DN 19,36,5,0
Mvav 140,204,60
EX 127
Mvav 140,208,64
EX 128
Mvav 144,204,76
EX 129
Mvav 144,208,80
s_nop 0
TR 196,13,128
TR 198,13,640
Mvav 148,204,92
EX 130
Mvav 148,208,96
EX 131
Mvav 152,204,108
M0 78,67776
EX 132
Mvav 152,208,112
WL 6
DN 33,36,5,0
Maav 0,180,176
s_nop 0
TR 200,13,160
TR 202,13,672
Maav 4,180,180
EX 133
Maav 8,180,184
EX 134
Maav 12,180,188
EX 135
Maav 16,180,192
EX 136
Maav 20,184,176
s_nop 0
TR 204,13,192
TR 206,13,704
Maav 24,184,180
EX 137
Maav 28,184,184
EX 138
Maav 32,184,188
EX 139
Maav 36,184,192
WL 6
EX 140
Maav 40,188,176
s_nop 0
TR 208,13,224
TR 210,13,736
Maav 44,188,180
EX 141
Maav 48,188,184
EX 142
Maav 52,188,188
EX 143
Maav 56,188,192
EX 144
Maav 60,192,176
EX 145
Maav 64,192,180
EX 146
Maav 68,192,184
EX 147
Maav 72,192,188
EX 148
Maav 76,192,192
WL 4
EX 149
Maav 80,196,176
s_memrealtime s[94:95]
s_waitcnt vmcnt(16) lgkmcnt(0)
s_barrier
s_sub_u32 s95,s94,s96
s_cmp_lt_i32 s95,0
s_cbranch_scc1 LB_pc40
LB_pb40:
s_add_u32 s96,s94,115
Maav 84,196,180
s_add_u32 s4,s80,49152
RA 180,11,17312
CV 156,116,117
Maav 88,196,184
s_and_b32 s4,s4,0x1e000
RA 184,11,17376
CV 157,118,119
Maav 92,196,188
s_xor_b32 s5,s4,0x30000
EX 150
Maav 96,196,192
M0 78,72416
EX 151
Maav 100,200,176
RA 188,11,17440
CV 160,120,121
Maav 104,200,180
DN 19,32,4,0
Maav 108,200,184
EX 152
Maav 112,200,188
EX 153
Maav 116,200,192
EX 154
Maav 120,204,176
RA 192,11,17504
CV 161,122,123
Maav 124,204,180
EX 155
Maav 128,204,184
CV 164,124,125
CV 165,126,127
Maav 132,204,188
M0 78,76640
CV 168,128,129
CV 169,130,131
Maav 136,204,192
DN 33,32,4,0
Maav 140,208,176
RA 196,11,17824
CV 172,132,133
Maav 144,208,180
CV 173,134,135
CV 158,136,137
Maav 148,208,184
CV 159,138,139
CV 162,140,141
Maav 152,208,188
CV 163,142,143
CV 166,144,145
Maav 156,208,192
CV 167,146,147
CV 170,148,149
Maav 160,212,176
CV 171,150,151
CV 174,152,153
Maav 164,212,180
M0 78,80864
CV 175,154,155
Maav 168,212,184
DN 19,32,5,0
Maav 172,212,188
Maav 176,212,192
WL 3
Mvavc 116,180,36,196
RA 200,11,17888
Mvav 116,184,40
Mvavc 120,180,52,200
Mvav 120,184,56
Mvavc 124,180,68,204
M0 78,85088
Mvav 124,184,72
RA 204,11,17952
Mvavc 128,180,84,208
DN 33,32,5,0
Mvav 128,184,88
Mvavc 132,180,100,212
Mvav 132,184,104
WL 3
Mvav 116,188,44
RA 208,11,18016
Mvav 116,192,48
Mvav 120,188,60
Mvav 120,192,64
M0 78,89728
Mvav 124,188,76
DN 19,36,6,0
Mvav 124,192,80
s_nop 0
TR 180,13,34624
TR 182,13,35136
Mvav 128,188,92
EX 116
Mvav 128,192,96
EX 117
Mvav 132,188,108
EX 118
Mvav 132,192,112
WL 4
EX 119
Mvavc 136,196,36,196
s_nop 0
TR 184,13,34656
TR 186,13,35168
Mvav 136,200,40
M0 78,93952
EX 120
Mvavc 140,196,52,200
DN 33,36,6,0
Mvav 140,200,56
EX 121
Mvavc 144,196,68,204
EX 122
Mvav 144,200,72
s_nop 0
TR 188,13,34688
TR 190,13,35200
Mvavc 148,196,84,208
EX 123
Mvav 148,200,88
EX 124
Mvavc 152,196,100,212
EX 125
Mvav 152,200,104
WL 6
EX 126
Mvav 136,204,44
M0 78,98176
TR 192,13,34720
TR 194,13,35232
Mvav 136,208,48
DN 19,36,7,0
Mvav 140,204,60
EX 127
Mvav 140,208,64
EX 128
Mvav 144,204,76
EX 129
Mvav 144,208,80
s_nop 0
TR 196,13,34752
TR 198,13,35264
Mvav 148,204,92
EX 130
Mvav 148,208,96
EX 131
Mvav 152,204,108
M0 78,102400
EX 132
Mvav 152,208,112
WL 6
DN 33,36,7,0
Maav 0,180,156
s_nop 0
TR 200,13,34784
TR 202,13,35296
Maav 4,180,160
EX 133
Maav 8,180,164
EX 134
Maav 12,180,168
EX 135
Maav 16,180,172
EX 136
Maav 20,184,156
s_nop 0
TR 204,13,34816
TR 206,13,35328
Maav 24,184,160
EX 137
Maav 28,184,164
EX 138
Maav 32,184,168
EX 139
Maav 36,184,172
WL 6
EX 140
Maav 40,188,156
s_nop 0
TR 208,13,34848
TR 210,13,35360
Maav 44,188,160
EX 141
Maav 48,188,164
EX 142
Maav 52,188,168
EX 143
Maav 56,188,172
EX 144
Maav 60,192,156
EX 145
Maav 64,192,160
EX 146
Maav 68,192,164
EX 147
Maav 72,192,168
EX 148
Maav 76,192,172
WL 4
EX 149
Maav 80,196,156
s_memrealtime s[94:95]
s_waitcnt vmcnt(16) lgkmcnt(0)
s_barrier
s_sub_u32 s95,s94,s96
s_cmp_lt_i32 s95,0
s_cbranch_scc1 LB_pc41
LB_pb41:
s_add_u32 s96,s94,115
Maav 84,196,160
s_add_u32 s6,s80,57344
RA 180,11,51936
CV 176,116,117
Maav 88,196,164
s_and_b32 s6,s6,0x1e000
RA 184,11,52000
CV 177,118,119
Maav 92,196,168
s_xor_b32 s7,s6,0x30000
EX 150
Maav 96,196,172
M0 78,107040
EX 151
Maav 100,200,156
RA 188,11,52064
CV 180,120,121
Maav 104,200,160
DN 19,32,6,0
Maav 108,200,164
EX 152
Maav 112,200,168
EX 153
Maav 116,200,172
EX 154
Maav 120,204,156
RA 192,11,52128
CV 181,122,123
Maav 124,204,160
EX 155
Maav 128,204,164
CV 184,124,125
CV 185,126,127
Maav 132,204,168
M0 78,111264
CV 188,128,129
CV 189,130,131
Maav 136,204,172
DN 33,32,6,0
Maav 140,208,156
RA 196,11,52448
CV 192,132,133
Maav 144,208,160
CV 193,134,135
CV 178,136,137
Maav 148,208,164
CV 179,138,139
CV 182,140,141
Maav 152,208,168
CV 183,142,143
CV 186,144,145
Maav 156,208,172
CV 187,146,147
CV 190,148,149
Maav 160,212,156
CV 191,150,151
CV 194,152,153
Maav 164,212,160
M0 78,115488
CV 195,154,155
Maav 168,212,164
DN 19,32,7,0
Maav 172,212,168
Maav 176,212,172
WL 3
Mvavc 116,180,36,196
RA 200,11,52512
Mvav 116,184,40
Mvavc 120,180,52,200
Mvav 120,184,56
Mvavc 124,180,68,204
M0 78,119712
Mvav 124,184,72
RA 204,11,52576
Mvavc 128,180,84,208
DN 33,32,7,0
Mvav 128,184,88
Mvavc 132,180,100,212
Mvav 132,184,104
WL 3
Mvav 116,188,44
RA 208,11,52640
Mvav 116,192,48
Mvav 120,188,60
Mvav 120,192,64
M0 78,124352
Mvav 124,188,76
DN 19,36,4,0
Mvav 124,192,80
s_nop 0
TR 180,12,0
TR 182,12,512
Mvav 128,188,92
EX 116
Mvav 128,192,96
EX 117
Mvav 132,188,108
EX 118
Mvav 132,192,112
WL 4
EX 119
Mvavc 136,196,36,196
s_nop 0
TR 184,12,32
TR 186,12,544
Mvav 136,200,40
M0 78,128576
EX 120
Mvavc 140,196,52,200
DN 33,36,4,0
Mvav 140,200,56
EX 121
Mvavc 144,196,68,204
EX 122
Mvav 144,200,72
s_nop 0
TR 188,12,64
TR 190,12,576
Mvavc 148,196,84,208
s_add_u32 s98,s71,57344
v_lshl_add_u32 v8,v2,1,s98
CV 156,116,117
Mvav 148,200,88
buffer_load_dword v8,v8,s[60:63],0 offen
Mvavc 152,196,100,212
EX 123
Mvav 152,200,104
WL 6
EX 124
Mvav 136,204,44
M0 78,132800
TR 192,12,96
TR 194,12,608
Mvav 136,208,48
DN 19,36,5,0
Mvav 140,204,60
EX 125
Mvav 140,208,64
EX 126
Mvav 144,204,76
EX 127
Mvav 144,208,80
s_nop 0
TR 196,12,128
TR 198,12,640
Mvav 148,204,92
EX 128
Mvav 148,208,96
EX 129
Mvav 152,204,108
M0 78,137024
EX 130
Mvav 152,208,112
WL 6
DN 33,36,5,0
Maav 0,180,176
s_nop 0
TR 200,12,160
TR 202,12,672
Maav 4,180,180
EX 131
Maav 8,180,184
EX 132
Maav 12,180,188
EX 133
Maav 16,180,192
EX 134
Maav 20,184,176
s_nop 0
TR 204,12,192
TR 206,12,704
Maav 24,184,180
EX 135
Maav 28,184,184
EX 136
Maav 32,184,188
EX 137
Maav 36,184,192
WL 6
EX 138
Maav 40,188,176
s_nop 0
TR 208,12,224
TR 210,12,736
Maav 44,188,180
EX 139
Maav 48,188,184
EX 140
Maav 52,188,188
EX 141
Maav 56,188,192
EX 142
Maav 60,192,176
EX 143
Maav 64,192,180
EX 144
Maav 68,192,184
EX 145
Maav 72,192,188
EX 146
Maav 76,192,192
WL 4
EX 147
Maav 80,196,176
s_memrealtime s[94:95]
s_waitcnt vmcnt(17) lgkmcnt(0)
s_barrier
s_sub_u32 s95,s94,s96
s_cmp_lt_i32 s95,0
s_cbranch_scc1 LB_pc42
LB_pb42:
s_add_u32 s96,s94,115
Maav 84,196,180
s_add_u32 s4,s80,65536
RA 180,10,17312
CV 157,118,119
Maav 88,196,184
s_and_b32 s4,s4,0x1e000
RA 184,10,17376
CV 160,120,121
Maav 92,196,188
s_xor_b32 s5,s4,0x30000
EX 148
Maav 96,196,192
M0 78,141664
EX 149
Maav 100,200,176
RA 188,10,17440
CV 161,122,123
Maav 104,200,180
DN 19,32,4,0
Maav 108,200,184
EX 150
Maav 112,200,188
EX 151
Maav 116,200,192
EX 152
Maav 120,204,176
RA 192,10,17504
CV 164,124,125
Maav 124,204,180
EX 153
Maav 128,204,184
EX 154
Maav 132,204,188
M0 78,145888
EX 155
Maav 136,204,192
DN 33,32,4,0
Maav 140,208,176
RA 196,10,17824
CV 165,126,127
Maav 144,208,180
CV 168,128,129
CV 169,130,131
Maav 148,208,184
CV 172,132,133
CV 173,134,135
Maav 152,208,188
CV 158,136,137
CV 159,138,139
Maav 156,208,192
CV 162,140,141
CV 163,142,143
Maav 160,212,176
CV 166,144,145
CV 167,146,147
Maav 164,212,180
M0 78,150112
CV 170,148,149
CV 171,150,151
Maav 168,212,184
DN 19,32,5,0
Maav 172,212,188
CV 174,152,153
CV 175,154,155
Maav 176,212,192
WL 3
Mvavc 116,180,36,196
RA 200,10,17888
Mvav 116,184,40
Mvavc 120,180,52,200
Mvav 120,184,56
Mvavc 124,180,68,204
M0 78,154336
Mvav 124,184,72
RA 204,10,17952
Mvavc 128,180,84,208
DN 33,32,5,0
Mvav 128,184,88
Mvavc 132,180,100,212
Mvav 132,184,104
WL 3
Mvav 116,188,44
RA 208,10,18016
Mvav 116,192,48
Mvav 120,188,60
Mvav 120,192,64
M0 78,20480
Mvav 124,188,76
DN 19,36,6,0
Mvav 124,192,80
s_nop 0
TR 180,12,34624
TR 182,12,35136
Mvav 128,188,92
EX 116
Mvav 128,192,96
EX 117
Mvav 132,188,108
EX 118
Mvav 132,192,112
WL 4
EX 119
Mvavc 136,196,36,196
s_nop 0
TR 184,12,34656
TR 186,12,35168
Mvav 136,200,40
M0 78,24704
EX 120
Mvavc 140,196,52,200
DN 33,36,6,0
Mvav 140,200,56
EX 121
Mvavc 144,196,68,204
EX 122
Mvav 144,200,72
s_nop 0
TR 188,12,34688
TR 190,12,35200
Mvavc 148,196,84,208
EX 123
Mvav 148,200,88
EX 124
Mvavc 152,196,100,212
EX 125
Mvav 152,200,104
WL 6
EX 126
Mvav 136,204,44
M0 78,28928
TR 192,12,34720
TR 194,12,35232
Mvav 136,208,48
DN 19,36,7,0
Mvav 140,204,60
EX 127
Mvav 140,208,64
EX 128
Mvav 144,204,76
EX 129
Mvav 144,208,80
s_nop 0
TR 196,12,34752
TR 198,12,35264
Mvav 148,204,92
EX 130
Mvav 148,208,96
EX 131
Mvav 152,204,108
M0 78,33152
EX 132
Mvav 152,208,112
WL 6
DN 33,36,7,0
Maav 0,180,156
s_nop 0
TR 200,12,34784
TR 202,12,35296
Maav 4,180,160
EX 133
Maav 8,180,164
EX 134
Maav 12,180,168
EX 135
Maav 16,180,172
EX 136
Maav 20,184,156
s_nop 0
TR 204,12,34816
TR 206,12,35328
Maav 24,184,160
EX 137
Maav 28,184,164
EX 138
Maav 32,184,168
EX 139
Maav 36,184,172
WL 6
EX 140
Maav 40,188,156
s_nop 0
TR 208,12,34848
TR 210,12,35360
Maav 44,188,160
EX 141
Maav 48,188,164
EX 142
Maav 52,188,168
EX 143
Maav 56,188,172
EX 144
Maav 60,192,156
EX 145
Maav 64,192,160
EX 146
Maav 68,192,164
EX 147
Maav 72,192,168
EX 148
Maav 76,192,172
WL 4
EX 149
Maav 80,196,156
s_memrealtime s[94:95]
s_waitcnt vmcnt(17) lgkmcnt(0)
s_barrier
s_sub_u32 s95,s94,s96
s_cmp_lt_i32 s95,0
s_cbranch_scc1 LB_pc43
LB_pb43:
s_add_u32 s96,s94,115
Maav 84,196,160
s_add_u32 s6,s80,73728
RA 180,10,51936
CV 176,116,117
Maav 88,196,164
s_and_b32 s6,s6,0x1e000
RA 184,10,52000
CV 177,118,119
Maav 92,196,168
s_xor_b32 s7,s6,0x30000
EX 150
Maav 96,196,172
M0 78,37792
EX 151
Maav 100,200,156
RA 188,10,52064
CV 180,120,121
Maav 104,200,160
DN 19,32,6,0
Maav 108,200,164
EX 152
Maav 112,200,168
EX 153
Maav 116,200,172
EX 154
Maav 120,204,156
RA 192,10,52128
CV 181,122,123
Maav 124,204,160
EX 155
Maav 128,204,164
CV 184,124,125
CV 185,126,127
Maav 132,204,168
M0 78,42016
CV 188,128,129
CV 189,130,131
Maav 136,204,172
DN 33,32,6,0
Maav 140,208,156
RA 196,10,52448
CV 192,132,133
Maav 144,208,160
CV 193,134,135
CV 178,136,137
Maav 148,208,164
CV 179,138,139
CV 182,140,141
Maav 152,208,168
CV 183,142,143
CV 186,144,145
Maav 156,208,172
CV 187,146,147
CV 190,148,149
Maav 160,212,156
CV 191,150,151
CV 194,152,153
Maav 164,212,160
M0 78,46240
CV 195,154,155
Maav 168,212,164
DN 19,32,7,0
Maav 172,212,168
Maav 176,212,172
WL 3
Mvavc 116,180,36,196
RA 200,10,52512
Mvav 116,184,40
Mvavc 120,180,52,200
Mvav 120,184,56
Mvavc 124,180,68,204
M0 78,50464
Mvav 124,184,72
RA 204,10,52576
Mvavc 128,180,84,208
DN 33,32,7,0
Mvav 128,184,88
Mvavc 132,180,100,212
Mvav 132,184,104
WL 3
Mvav 116,188,44
RA 208,10,52640
Mvav 116,192,48
Mvav 120,188,60
Mvav 120,192,64
M0 78,55104
Mvav 124,188,76
DN 19,36,4,0
Mvav 124,192,80
s_nop 0
TR 180,13,0
TR 182,13,512
Mvav 128,188,92
EX 116
Mvav 128,192,96
EX 117
Mvav 132,188,108
EX 118
Mvav 132,192,112
WL 4
EX 119
Mvavc 136,196,36,196
s_nop 0
TR 184,13,32
TR 186,13,544
Mvav 136,200,40
M0 78,59328
EX 120
Mvavc 140,196,52,200
DN 33,36,4,0
Mvav 140,200,56
EX 121
Mvavc 144,196,68,204
EX 122
Mvav 144,200,72
s_nop 0
TR 188,13,64
TR 190,13,576
Mvavc 148,196,84,208
EX 123
Mvav 148,200,88
EX 124
Mvavc 152,196,100,212
EX 125
Mvav 152,200,104
WL 6
EX 126
Mvav 136,204,44
M0 78,63552
TR 192,13,96
TR 194,13,608
Mvav 136,208,48
DN 19,36,5,0
Mvav 140,204,60
EX 127
Mvav 140,208,64
EX 128
Mvav 144,204,76
EX 129
Mvav 144,208,80
s_nop 0
TR 196,13,128
TR 198,13,640
Mvav 148,204,92
EX 130
Mvav 148,208,96
EX 131
Mvav 152,204,108
M0 78,67776
EX 132
Mvav 152,208,112
WL 6
DN 33,36,5,0
Maav 0,180,176
s_nop 0
TR 200,13,160
TR 202,13,672
Maav 4,180,180
EX 133
Maav 8,180,184
EX 134
Maav 12,180,188
EX 135
Maav 16,180,192
EX 136
Maav 20,184,176
s_nop 0
TR 204,13,192
TR 206,13,704
Maav 24,184,180
EX 137
Maav 28,184,184
EX 138
Maav 32,184,188
EX 139
Maav 36,184,192
WL 6
EX 140
Maav 40,188,176
s_nop 0
TR 208,13,224
TR 210,13,736
Maav 44,188,180
EX 141
Maav 48,188,184
EX 142
Maav 52,188,188
EX 143
Maav 56,188,192
EX 144
Maav 60,192,176
EX 145
Maav 64,192,180
EX 146
Maav 68,192,184
EX 147
Maav 72,192,188
EX 148
Maav 76,192,192
WL 4
EX 149
Maav 80,196,176
s_memrealtime s[94:95]
s_waitcnt vmcnt(16) lgkmcnt(0)
s_barrier
s_sub_u32 s95,s94,s96
s_cmp_lt_i32 s95,0
s_cbranch_scc1 LB_pc44
LB_pb44:
s_add_u32 s96,s94,115
Maav 84,196,180
s_add_u32 s4,s80,81920
RA 180,11,17312
CV 156,116,117
Maav 88,196,184
s_and_b32 s4,s4,0x1e000
RA 184,11,17376
CV 157,118,119
Maav 92,196,188
s_xor_b32 s5,s4,0x30000
EX 150
Maav 96,196,192
M0 78,72416
EX 151
Maav 100,200,176
RA 188,11,17440
CV 160,120,121
Maav 104,200,180
DN 19,32,4,0
Maav 108,200,184
EX 152
Maav 112,200,188
EX 153
Maav 116,200,192
EX 154
Maav 120,204,176
RA 192,11,17504
CV 161,122,123
Maav 124,204,180
EX 155
Maav 128,204,184
CV 164,124,125
CV 165,126,127
Maav 132,204,188
M0 78,76640
CV 168,128,129
CV 169,130,131
Maav 136,204,192
DN 33,32,4,0
Maav 140,208,176
RA 196,11,17824
CV 172,132,133
Maav 144,208,180
CV 173,134,135
CV 158,136,137
Maav 148,208,184
CV 159,138,139
CV 162,140,141
Maav 152,208,188
CV 163,142,143
CV 166,144,145
Maav 156,208,192
CV 167,146,147
CV 170,148,149
Maav 160,212,176
CV 171,150,151
CV 174,152,153
Maav 164,212,180
M0 78,80864
CV 175,154,155
Maav 168,212,184
DN 19,32,5,0
Maav 172,212,188
Maav 176,212,192
WL 3
Mvavc 116,180,36,196
RA 200,11,17888
Mvav 116,184,40
Mvavc 120,180,52,200
Mvav 120,184,56
Mvavc 124,180,68,204
M0 78,85088
Mvav 124,184,72
RA 204,11,17952
Mvavc 128,180,84,208
DN 33,32,5,0
Mvav 128,184,88
Mvavc 132,180,100,212
Mvav 132,184,104
WL 3
Mvav 116,188,44
RA 208,11,18016
Mvav 116,192,48
Mvav 120,188,60
Mvav 120,192,64
M0 78,89728
Mvav 124,188,76
DN 19,36,6,0
Mvav 124,192,80
s_nop 0
TR 180,13,34624
TR 182,13,35136
Mvav 128,188,92
EX 116
Mvav 128,192,96
EX 117
Mvav 132,188,108
EX 118
Mvav 132,192,112
WL 4
EX 119
Mvavc 136,196,36,196
s_nop 0
TR 184,13,34656
TR 186,13,35168
Mvav 136,200,40
M0 78,93952
EX 120
Mvavc 140,196,52,200
DN 33,36,6,0
Mvav 140,200,56
EX 121
Mvavc 144,196,68,204
EX 122
Mvav 144,200,72
s_nop 0
TR 188,13,34688
TR 190,13,35200
Mvavc 148,196,84,208
EX 123
Mvav 148,200,88
EX 124
Mvavc 152,196,100,212
EX 125
Mvav 152,200,104
WL 6
EX 126
Mvav 136,204,44
M0 78,98176
TR 192,13,34720
TR 194,13,35232
Mvav 136,208,48
DN 19,36,7,0
Mvav 140,204,60
EX 127
Mvav 140,208,64
EX 128
Mvav 144,204,76
EX 129
Mvav 144,208,80
s_nop 0
TR 196,13,34752
TR 198,13,35264
Mvav 148,204,92
EX 130
Mvav 148,208,96
EX 131
Mvav 152,204,108
M0 78,102400
EX 132
Mvav 152,208,112
WL 6
DN 33,36,7,0
Maav 0,180,156
s_nop 0
TR 200,13,34784
TR 202,13,35296
Maav 4,180,160
EX 133
Maav 8,180,164
EX 134
Maav 12,180,168
EX 135
Maav 16,180,172
EX 136
Maav 20,184,156
s_nop 0
TR 204,13,34816
TR 206,13,35328
Maav 24,184,160
EX 137
Maav 28,184,164
EX 138
Maav 32,184,168
EX 139
Maav 36,184,172
WL 6
EX 140
Maav 40,188,156
s_nop 0
TR 208,13,34848
TR 210,13,35360
Maav 44,188,160
EX 141
Maav 48,188,164
EX 142
Maav 52,188,168
EX 143
Maav 56,188,172
EX 144
Maav 60,192,156
EX 145
Maav 64,192,160
EX 146
Maav 68,192,164
EX 147
Maav 72,192,168
EX 148
Maav 76,192,172
WL 4
EX 149
Maav 80,196,156
s_memrealtime s[94:95]
s_waitcnt vmcnt(16) lgkmcnt(0)
s_barrier
s_sub_u32 s95,s94,s96
s_cmp_lt_i32 s95,0
s_cbranch_scc1 LB_pc45
LB_pb45:
s_add_u32 s96,s94,90
Maav 84,196,160
s_add_u32 s6,s80,90112
RA 180,11,51936
CV 176,116,117
Maav 88,196,164
s_and_b32 s6,s6,0x1e000
RA 184,11,52000
CV 177,118,119
Maav 92,196,168
s_xor_b32 s7,s6,0x30000
EX 150
Maav 96,196,172
M0 78,107040
EX 151
Maav 100,200,156
RA 188,11,52064
CV 180,120,121
Maav 104,200,160
DN 19,32,6,0
Maav 108,200,164
EX 152
Maav 112,200,168
EX 153
Maav 116,200,172
EX 154
Maav 120,204,156
RA 192,11,52128
CV 181,122,123
Maav 124,204,160
EX 155
Maav 128,204,164
CV 184,124,125
CV 185,126,127
Maav 132,204,168
M0 78,111264
CV 188,128,129
CV 189,130,131
Maav 136,204,172
DN 33,32,6,0
Maav 140,208,156
RA 196,11,52448
CV 192,132,133
Maav 144,208,160
CV 193,134,135
CV 178,136,137
Maav 148,208,164
CV 179,138,139
CV 182,140,141
Maav 152,208,168
CV 183,142,143
CV 186,144,145
Maav 156,208,172
CV 187,146,147
CV 190,148,149
Maav 160,212,156
CV 191,150,151
CV 194,152,153
Maav 164,212,160
M0 78,115488
CV 195,154,155
Maav 168,212,164
DN 19,32,7,0
Maav 172,212,168
Maav 176,212,172
WL 3
Mvavc 116,180,36,196
RA 200,11,52512
Mvav 116,184,40
Mvavc 120,180,52,200
Mvav 120,184,56
Mvavc 124,180,68,204
M0 78,119712
Mvav 124,184,72
RA 204,11,52576
Mvavc 128,180,84,208
DN 33,32,7,0
Mvav 128,184,88
Mvavc 132,180,100,212
Mvav 132,184,104
WL 3
Mvav 116,188,44
RA 208,11,52640
Mvav 116,192,48
Mvav 120,188,60
Mvav 120,192,64
M0 78,124352
Mvav 124,188,76
DN 19,36,4,0
Mvav 124,192,80
s_nop 0
TR 180,12,0
TR 182,12,512
Mvav 128,188,92
EX 116
Mvav 128,192,96
EX 117
Mvav 132,188,108
EX 118
Mvav 132,192,112
WL 4
EX 119
Mvavc 136,196,36,196
s_nop 0
TR 184,12,32
TR 186,12,544
Mvav 136,200,40
M0 78,128576
EX 120
Mvavc 140,196,52,200
DN 33,36,4,0
Mvav 140,200,56
EX 121
Mvavc 144,196,68,204
EX 122
Mvav 144,200,72
s_nop 0
TR 188,12,64
TR 190,12,576
Mvavc 148,196,84,208
s_add_u32 s98,s71,65536
v_lshl_add_u32 v8,v2,1,s98
CV 156,116,117
Mvav 148,200,88
buffer_load_dword v8,v8,s[60:63],0 offen
Mvavc 152,196,100,212
EX 123
Mvav 152,200,104
WL 6
EX 124
Mvav 136,204,44
M0 78,132800
TR 192,12,96
TR 194,12,608
Mvav 136,208,48
DN 19,36,5,0
Mvav 140,204,60
EX 125
Mvav 140,208,64
EX 126
Mvav 144,204,76
EX 127
Mvav 144,208,80
s_nop 0
TR 196,12,128
TR 198,12,640
Mvav 148,204,92
EX 128
Mvav 148,208,96
EX 129
Mvav 152,204,108
M0 78,137024
EX 130
Mvav 152,208,112
WL 6
DN 33,36,5,0
Maav 0,180,176
s_nop 0
TR 200,12,160
TR 202,12,672
Maav 4,180,180
EX 131
Maav 8,180,184
EX 132
Maav 12,180,188
EX 133
Maav 16,180,192
EX 134
Maav 20,184,176
s_nop 0
TR 204,12,192
TR 206,12,704
Maav 24,184,180
EX 135
Maav 28,184,184
EX 136
Maav 32,184,188
EX 137
Maav 36,184,192
WL 6
EX 138
Maav 40,188,176
s_nop 0
TR 208,12,224
TR 210,12,736
Maav 44,188,180
EX 139
Maav 48,188,184
EX 140
Maav 52,188,188
EX 141
Maav 56,188,192
EX 142
Maav 60,192,176
EX 143
Maav 64,192,180
EX 144
Maav 68,192,184
EX 145
Maav 72,192,188
EX 146
Maav 76,192,192
WL 4
EX 147
Maav 80,196,176
s_memrealtime s[94:95]
s_waitcnt vmcnt(17) lgkmcnt(0)
s_barrier
s_sub_u32 s95,s94,s96
s_cmp_lt_i32 s95,0
s_cbranch_scc1 LB_pc46
LB_pb46:
s_add_u32 s96,s94,90
Maav 84,196,180
s_add_u32 s4,s80,98304
RA 180,10,17312
CV 157,118,119
Maav 88,196,184
s_and_b32 s4,s4,0x1e000
RA 184,10,17376
CV 160,120,121
Maav 92,196,188
s_xor_b32 s5,s4,0x30000
EX 148
Maav 96,196,192
M0 78,141664
EX 149
Maav 100,200,176
RA 188,10,17440
CV 161,122,123
Maav 104,200,180
DN 19,32,4,0
Maav 108,200,184
EX 150
Maav 112,200,188
EX 151
Maav 116,200,192
EX 152
Maav 120,204,176
RA 192,10,17504
CV 164,124,125
Maav 124,204,180
EX 153
Maav 128,204,184
EX 154
Maav 132,204,188
M0 78,145888
EX 155
Maav 136,204,192
DN 33,32,4,0
Maav 140,208,176
RA 196,10,17824
CV 165,126,127
Maav 144,208,180
CV 168,128,129
CV 169,130,131
Maav 148,208,184
CV 172,132,133
CV 173,134,135
Maav 152,208,188
CV 158,136,137
CV 159,138,139
Maav 156,208,192
CV 162,140,141
CV 163,142,143
Maav 160,212,176
CV 166,144,145
CV 167,146,147
Maav 164,212,180
M0 78,150112
CV 170,148,149
CV 171,150,151
Maav 168,212,184
DN 19,32,5,0
Maav 172,212,188
CV 174,152,153
CV 175,154,155
Maav 176,212,192
WL 3
Mvavc 116,180,36,196
RA 200,10,17888
Mvav 116,184,40
Mvavc 120,180,52,200
Mvav 120,184,56
Mvavc 124,180,68,204
M0 78,154336
Mvav 124,184,72
RA 204,10,17952
Mvavc 128,180,84,208
DN 33,32,5,0
Mvav 128,184,88
Mvavc 132,180,100,212
Mvav 132,184,104
WL 3
Mvav 116,188,44
RA 208,10,18016
Mvav 116,192,48
Mvav 120,188,60
Mvav 120,192,64
M0 78,20480
Mvav 124,188,76
DN 19,36,6,0
Mvav 124,192,80
s_nop 0
TR 180,12,34624
TR 182,12,35136
Mvav 128,188,92
EX 116
Mvav 128,192,96
EX 117
Mvav 132,188,108
EX 118
Mvav 132,192,112
WL 4
EX 119
Mvavc 136,196,36,196
s_nop 0
TR 184,12,34656
TR 186,12,35168
Mvav 136,200,40
M0 78,24704
EX 120
Mvavc 140,196,52,200
DN 33,36,6,0
Mvav 140,200,56
EX 121
Mvavc 144,196,68,204
EX 122
Mvav 144,200,72
s_nop 0
TR 188,12,34688
TR 190,12,35200
Mvavc 148,196,84,208
EX 123
Mvav 148,200,88
EX 124
Mvavc 152,196,100,212
EX 125
Mvav 152,200,104
WL 6
EX 126
Mvav 136,204,44
M0 78,28928
TR 192,12,34720
TR 194,12,35232
Mvav 136,208,48
DN 19,36,7,0
Mvav 140,204,60
EX 127
Mvav 140,208,64
EX 128
Mvav 144,204,76
EX 129
Mvav 144,208,80
s_nop 0
TR 196,12,34752
TR 198,12,35264
Mvav 148,204,92
EX 130
Mvav 148,208,96
EX 131
Mvav 152,204,108
M0 78,33152
EX 132
Mvav 152,208,112
WL 6
DN 33,36,7,0
Maav 0,180,156
s_nop 0
TR 200,12,34784
TR 202,12,35296
Maav 4,180,160
EX 133
Maav 8,180,164
EX 134
Maav 12,180,168
EX 135
Maav 16,180,172
EX 136
Maav 20,184,156
s_nop 0
TR 204,12,34816
TR 206,12,35328
Maav 24,184,160
EX 137
Maav 28,184,164
EX 138
Maav 32,184,168
EX 139
Maav 36,184,172
WL 6
EX 140
Maav 40,188,156
s_nop 0
TR 208,12,34848
TR 210,12,35360
Maav 44,188,160
EX 141
Maav 48,188,164
EX 142
Maav 52,188,168
EX 143
Maav 56,188,172
EX 144
Maav 60,192,156
EX 145
Maav 64,192,160
EX 146
Maav 68,192,164
EX 147
Maav 72,192,168
EX 148
Maav 76,192,172
WL 4
EX 149
Maav 80,196,156
s_memrealtime s[94:95]
s_waitcnt vmcnt(17) lgkmcnt(0)
s_barrier
s_sub_u32 s95,s94,s96
s_cmp_lt_i32 s95,0
s_cbranch_scc1 LB_pc47
LB_pb47:
s_add_u32 s96,s94,90
Maav 84,196,160
s_add_u32 s6,s80,106496
RA 180,10,51936
CV 176,116,117
Maav 88,196,164
s_and_b32 s6,s6,0x1e000
RA 184,10,52000
CV 177,118,119
Maav 92,196,168
s_xor_b32 s7,s6,0x30000
EX 150
Maav 96,196,172
M0 78,37792
EX 151
Maav 100,200,156
RA 188,10,52064
CV 180,120,121
Maav 104,200,160
DN 19,32,6,0
Maav 108,200,164
EX 152
Maav 112,200,168
EX 153
Maav 116,200,172
EX 154
Maav 120,204,156
RA 192,10,52128
CV 181,122,123
Maav 124,204,160
EX 155
Maav 128,204,164
CV 184,124,125
CV 185,126,127
Maav 132,204,168
M0 78,42016
CV 188,128,129
CV 189,130,131
Maav 136,204,172
DN 33,32,6,0
Maav 140,208,156
RA 196,10,52448
CV 192,132,133
Maav 144,208,160
CV 193,134,135
CV 178,136,137
Maav 148,208,164
CV 179,138,139
CV 182,140,141
Maav 152,208,168
CV 183,142,143
CV 186,144,145
Maav 156,208,172
CV 187,146,147
CV 190,148,149
Maav 160,212,156
CV 191,150,151
CV 194,152,153
Maav 164,212,160
M0 78,46240
CV 195,154,155
Maav 168,212,164
DN 19,32,7,0
Maav 172,212,168
Maav 176,212,172
WL 3
Mvavc 116,180,36,196
RA 200,10,52512
Mvav 116,184,40
Mvavc 120,180,52,200
Mvav 120,184,56
Mvavc 124,180,68,204
M0 78,50464
Mvav 124,184,72
RA 204,10,52576
Mvavc 128,180,84,208
DN 33,32,7,0
Mvav 128,184,88
Mvavc 132,180,100,212
Mvav 132,184,104
WL 3
Mvav 116,188,44
RA 208,10,52640
Mvav 116,192,48
Mvav 120,188,60
Mvav 120,192,64
M0 78,55104
Mvav 124,188,76
DN 19,36,4,0
Mvav 124,192,80
s_nop 0
TR 180,13,0
TR 182,13,512
Mvav 128,188,92
EX 116
Mvav 128,192,96
EX 117
Mvav 132,188,108
EX 118
Mvav 132,192,112
WL 4
EX 119
Mvavc 136,196,36,196
s_nop 0
TR 184,13,32
TR 186,13,544
Mvav 136,200,40
M0 78,59328
EX 120
Mvavc 140,196,52,200
DN 33,36,4,0
Mvav 140,200,56
EX 121
Mvavc 144,196,68,204
EX 122
Mvav 144,200,72
s_nop 0
TR 188,13,64
TR 190,13,576
Mvavc 148,196,84,208
EX 123
Mvav 148,200,88
EX 124
Mvavc 152,196,100,212
EX 125
Mvav 152,200,104
WL 6
EX 126
Mvav 136,204,44
M0 78,63552
TR 192,13,96
TR 194,13,608
Mvav 136,208,48
DN 19,36,5,0
Mvav 140,204,60
EX 127
Mvav 140,208,64
EX 128
Mvav 144,204,76
EX 129
Mvav 144,208,80
s_nop 0
TR 196,13,128
TR 198,13,640
Mvav 148,204,92
EX 130
Mvav 148,208,96
EX 131
Mvav 152,204,108
M0 78,67776
EX 132
Mvav 152,208,112
WL 6
DN 33,36,5,0
Maav 0,180,176
s_nop 0
TR 200,13,160
TR 202,13,672
Maav 4,180,180
EX 133
Maav 8,180,184
EX 134
Maav 12,180,188
EX 135
Maav 16,180,192
EX 136
Maav 20,184,176
s_nop 0
TR 204,13,192
TR 206,13,704
Maav 24,184,180
EX 137
Maav 28,184,184
EX 138
Maav 32,184,188
EX 139
Maav 36,184,192
WL 6
EX 140
Maav 40,188,176
s_nop 0
TR 208,13,224
TR 210,13,736
Maav 44,188,180
EX 141
Maav 48,188,184
EX 142
Maav 52,188,188
EX 143
Maav 56,188,192
EX 144
Maav 60,192,176
EX 145
Maav 64,192,180
EX 146
Maav 68,192,184
EX 147
Maav 72,192,188
EX 148
Maav 76,192,192
WL 4
EX 149
Maav 80,196,176
s_memrealtime s[94:95]
s_waitcnt vmcnt(16) lgkmcnt(0)
s_barrier
s_sub_u32 s95,s94,s96
s_cmp_lt_i32 s95,0
s_cbranch_scc1 LB_pc48
LB_pb48:
s_add_u32 s96,s94,90
Maav 84,196,180
s_add_u32 s4,s80,114688
RA 180,11,17312
CV 156,116,117
Maav 88,196,184
s_and_b32 s4,s4,0x1e000
RA 184,11,17376
CV 157,118,119
Maav 92,196,188
s_xor_b32 s5,s4,0x30000
EX 150
Maav 96,196,192
M0 78,72416
EX 151
Maav 100,200,176
RA 188,11,17440
CV 160,120,121
Maav 104,200,180
DN 19,32,4,0
Maav 108,200,184
EX 152
Maav 112,200,188
EX 153
Maav 116,200,192
EX 154
Maav 120,204,176
RA 192,11,17504
CV 161,122,123
Maav 124,204,180
EX 155
Maav 128,204,184
CV 164,124,125
CV 165,126,127
Maav 132,204,188
M0 78,76640
CV 168,128,129
CV 169,130,131
Maav 136,204,192
DN 33,32,4,0
Maav 140,208,176
RA 196,11,17824
CV 172,132,133
Maav 144,208,180
CV 173,134,135
CV 158,136,137
Maav 148,208,184
CV 159,138,139
CV 162,140,141
Maav 152,208,188
CV 163,142,143
CV 166,144,145
Maav 156,208,192
CV 167,146,147
CV 170,148,149
Maav 160,212,176
CV 171,150,151
CV 174,152,153
Maav 164,212,180
M0 78,80864
CV 175,154,155
Maav 168,212,184
DN 19,32,5,0
Maav 172,212,188
Maav 176,212,192
WL 3
Mvavc 116,180,36,196
RA 200,11,17888
Mvav 116,184,40
Mvavc 120,180,52,200
Mvav 120,184,56
Mvavc 124,180,68,204
M0 78,85088
Mvav 124,184,72
RA 204,11,17952
Mvavc 128,180,84,208
DN 33,32,5,0
Mvav 128,184,88
Mvavc 132,180,100,212
Mvav 132,184,104
WL 3
Mvav 116,188,44
RA 208,11,18016
Mvav 116,192,48
Mvav 120,188,60
Mvav 120,192,64
M0 78,89728
Mvav 124,188,76
DN 19,36,6,0
Mvav 124,192,80
s_nop 0
TR 180,13,34624
TR 182,13,35136
Mvav 128,188,92
EX 116
Mvav 128,192,96
EX 117
Mvav 132,188,108
EX 118
Mvav 132,192,112
WL 4
EX 119
Mvavc 136,196,36,196
s_nop 0
TR 184,13,34656
TR 186,13,35168
Mvav 136,200,40
M0 78,93952
EX 120
Mvavc 140,196,52,200
DN 33,36,6,0
Mvav 140,200,56
EX 121
Mvavc 144,196,68,204
EX 122
Mvav 144,200,72
s_nop 0
TR 188,13,34688
TR 190,13,35200
Mvavc 148,196,84,208
EX 123
Mvav 148,200,88
EX 124
Mvavc 152,196,100,212
EX 125
Mvav 152,200,104
WL 6
EX 126
Mvav 136,204,44
M0 78,98176
TR 192,13,34720
TR 194,13,35232
Mvav 136,208,48
DN 19,36,7,0
Mvav 140,204,60
EX 127
Mvav 140,208,64
EX 128
Mvav 144,204,76
EX 129
Mvav 144,208,80
s_nop 0
TR 196,13,34752
TR 198,13,35264
Mvav 148,204,92
EX 130
Mvav 148,208,96
EX 131
Mvav 152,204,108
M0 78,102400
EX 132
Mvav 152,208,112
WL 6
DN 33,36,7,0
Maav 0,180,156
s_nop 0
TR 200,13,34784
TR 202,13,35296
Maav 4,180,160
EX 133
Maav 8,180,164
EX 134
Maav 12,180,168
EX 135
Maav 16,180,172
EX 136
Maav 20,184,156
s_nop 0
TR 204,13,34816
TR 206,13,35328
Maav 24,184,160
EX 137
Maav 28,184,164
EX 138
Maav 32,184,168
EX 139
Maav 36,184,172
WL 6
EX 140
Maav 40,188,156
s_nop 0
TR 208,13,34848
TR 210,13,35360
Maav 44,188,160
EX 141
Maav 48,188,164
EX 142
Maav 52,188,168
EX 143
Maav 56,188,172
EX 144
Maav 60,192,156
EX 145
Maav 64,192,160
EX 146
Maav 68,192,164
EX 147
Maav 72,192,168
EX 148
Maav 76,192,172
WL 4
EX 149
Maav 80,196,156
s_memrealtime s[94:95]
s_waitcnt vmcnt(16) lgkmcnt(0)
s_barrier
s_sub_u32 s95,s94,s96
s_cmp_lt_i32 s95,0
s_cbranch_scc1 LB_pc49
LB_pb49:
s_add_u32 s96,s94,90
Maav 84,196,160
s_add_u32 s6,s80,122880
RA 180,11,51936
CV 176,116,117
Maav 88,196,164
s_and_b32 s6,s6,0x1e000
RA 184,11,52000
CV 177,118,119
Maav 92,196,168
s_xor_b32 s7,s6,0x30000
EX 150
Maav 96,196,172
M0 78,107040
EX 151
Maav 100,200,156
RA 188,11,52064
CV 180,120,121
Maav 104,200,160
DN 19,32,6,0
Maav 108,200,164
EX 152
Maav 112,200,168
EX 153
Maav 116,200,172
EX 154
Maav 120,204,156
RA 192,11,52128
CV 181,122,123
Maav 124,204,160
EX 155
Maav 128,204,164
CV 184,124,125
CV 185,126,127
Maav 132,204,168
M0 78,111264
CV 188,128,129
CV 189,130,131
Maav 136,204,172
DN 33,32,6,0
Maav 140,208,156
RA 196,11,52448
CV 192,132,133
Maav 144,208,160
CV 193,134,135
CV 178,136,137
Maav 148,208,164
CV 179,138,139
CV 182,140,141
Maav 152,208,168
CV 183,142,143
CV 186,144,145
Maav 156,208,172
CV 187,146,147
CV 190,148,149
Maav 160,212,156
CV 191,150,151
CV 194,152,153
Maav 164,212,160
M0 78,115488
CV 195,154,155
Maav 168,212,164
DN 19,32,7,0
Maav 172,212,168
Maav 176,212,172
WL 3
Mvavc 116,180,36,196
RA 200,11,52512
Mvav 116,184,40
Mvavc 120,180,52,200
Mvav 120,184,56
Mvavc 124,180,68,204
M0 78,119712
Mvav 124,184,72
RA 204,11,52576
Mvavc 128,180,84,208
DN 33,32,7,0
Mvav 128,184,88
Mvavc 132,180,100,212
Mvav 132,184,104
WL 3
Mvav 116,188,44
RA 208,11,52640
Mvav 116,192,48
Mvav 120,188,60
Mvav 120,192,64
M0 78,124352
Mvav 124,188,76
DN 19,36,4,0
Mvav 124,192,80
s_nop 0
TR 180,12,0
TR 182,12,512
Mvav 128,188,92
EX 116
Mvav 128,192,96
EX 117
Mvav 132,188,108
EX 118
Mvav 132,192,112
WL 4
EX 119
Mvavc 136,196,36,196
s_nop 0
TR 184,12,32
TR 186,12,544
Mvav 136,200,40
M0 78,128576
EX 120
Mvavc 140,196,52,200
DN 33,36,4,0
Mvav 140,200,56
EX 121
Mvavc 144,196,68,204
EX 122
Mvav 144,200,72
s_nop 0
TR 188,12,64
TR 190,12,576
Mvavc 148,196,84,208
s_add_u32 s98,s71,73728
v_lshl_add_u32 v8,v2,1,s98
CV 156,116,117
Mvav 148,200,88
buffer_load_dword v8,v8,s[60:63],0 offen
Mvavc 152,196,100,212
EX 123
Mvav 152,200,104
WL 6
EX 124
Mvav 136,204,44
M0 78,132800
TR 192,12,96
TR 194,12,608
Mvav 136,208,48
DN 19,36,5,0
Mvav 140,204,60
EX 125
Mvav 140,208,64
EX 126
Mvav 144,204,76
EX 127
Mvav 144,208,80
s_nop 0
TR 196,12,128
TR 198,12,640
Mvav 148,204,92
EX 128
Mvav 148,208,96
EX 129
Mvav 152,204,108
M0 78,137024
EX 130
Mvav 152,208,112
WL 6
DN 33,36,5,0
Maav 0,180,176
s_nop 0
TR 200,12,160
TR 202,12,672
Maav 4,180,180
EX 131
Maav 8,180,184
EX 132
Maav 12,180,188
EX 133
Maav 16,180,192
EX 134
Maav 20,184,176
s_nop 0
TR 204,12,192
TR 206,12,704
Maav 24,184,180
EX 135
Maav 28,184,184
EX 136
Maav 32,184,188
EX 137
Maav 36,184,192
WL 6
EX 138
Maav 40,188,176
s_nop 0
TR 208,12,224
TR 210,12,736
Maav 44,188,180
EX 139
Maav 48,188,184
EX 140
Maav 52,188,188
EX 141
Maav 56,188,192
EX 142
Maav 60,192,176
EX 143
Maav 64,192,180
EX 144
Maav 68,192,184
EX 145
Maav 72,192,188
EX 146
Maav 76,192,192
WL 4
EX 147
Maav 80,196,176
s_memrealtime s[94:95]
s_waitcnt vmcnt(17) lgkmcnt(0)
s_barrier
s_sub_u32 s95,s94,s96
s_cmp_lt_i32 s95,0
s_cbranch_scc1 LB_pc50
LB_pb50:
s_add_u32 s96,s94,90
Maav 84,196,180
M0 78,141664
RA 180,10,17312
CV 157,118,119
Maav 88,196,184
RA 184,10,17376
CV 160,120,121
Maav 92,196,188
DN 19,36,6,0
Maav 96,196,192
EX 148
Maav 100,200,176
RA 188,10,17440
CV 161,122,123
Maav 104,200,180
EX 149
Maav 108,200,184
EX 150
Maav 112,200,188
EX 151
Maav 116,200,192
EX 152
Maav 120,204,176
M0 78,145888
RA 192,10,17504
CV 164,124,125
Maav 124,204,180
DN 33,36,6,0
Maav 128,204,184
EX 153
Maav 132,204,188
EX 154
Maav 136,204,192
EX 155
Maav 140,208,176
RA 196,10,17824
CV 165,126,127
Maav 144,208,180
CV 168,128,129
CV 169,130,131
Maav 148,208,184
CV 172,132,133
CV 173,134,135
Maav 152,208,188
M0 78,150112
CV 158,136,137
CV 159,138,139
Maav 156,208,192
DN 19,36,7,0
Maav 160,212,176
CV 162,140,141
CV 163,142,143
Maav 164,212,180
CV 166,144,145
CV 167,146,147
Maav 168,212,184
CV 170,148,149
CV 171,150,151
Maav 172,212,188
CV 175,154,155
Maav 176,212,192
WL 3
Mvavc 116,180,36,196
RA 200,10,17888
CV 174,152,153
Mvav 116,184,40
M0 78,154336
Mvavc 120,180,52,200
DN 33,36,7,0
Mvav 120,184,56
Mvavc 124,180,68,204
Mvav 124,184,72
RA 204,10,17952
Mvavc 128,180,84,208
Mvav 128,184,88
Mvavc 132,180,100,212
Mvav 132,184,104
WL 3
Mvav 116,188,44
s_add_u32 s4,s80,0
RA 208,10,18016
Mvav 116,192,48
s_and_b32 s4,s4,0x1e000
Mvav 120,188,60
s_or_b32 s4,s4,0x40000
Mvav 120,192,64
s_xor_b32 s5,s4,0x30000
M0 78,20480
Mvav 124,188,76
DN 19,32,4,0
Mvav 124,192,80
s_nop 0
TR 180,12,34624
TR 182,12,35136
Mvav 128,188,92
EX 116
Mvav 128,192,96
EX 117
Mvav 132,188,108
EX 118
Mvav 132,192,112
WL 4
EX 119
Mvavc 136,196,36,196
s_nop 0
TR 184,12,34656
TR 186,12,35168
Mvav 136,200,40
M0 78,24704
EX 120
Mvavc 140,196,52,200
DN 33,32,4,0
Mvav 140,200,56
EX 121
Mvavc 144,196,68,204
EX 122
Mvav 144,200,72
s_nop 0
TR 188,12,34688
TR 190,12,35200
Mvavc 148,196,84,208
EX 123
Mvav 148,200,88
EX 124
Mvavc 152,196,100,212
EX 125
Mvav 152,200,104
WL 6
EX 126
Mvav 136,204,44
M0 78,28928
TR 192,12,34720
TR 194,12,35232
Mvav 136,208,48
DN 19,32,5,0
Mvav 140,204,60
EX 127
Mvav 140,208,64
EX 128
Mvav 144,204,76
EX 129
Mvav 144,208,80
s_nop 0
TR 196,12,34752
TR 198,12,35264
Mvav 148,204,92
EX 130
Mvav 148,208,96
EX 131
Mvav 152,204,108
M0 78,33152
EX 132
Mvav 152,208,112
WL 6
DN 33,32,5,0
Maav 0,180,156
s_nop 0
TR 200,12,34784
TR 202,12,35296
Maav 4,180,160
EX 133
Maav 8,180,164
EX 134
Maav 12,180,168
EX 135
Maav 16,180,172
EX 136
Maav 20,184,156
s_nop 0
TR 204,12,34816
TR 206,12,35328
Maav 24,184,160
EX 137
Maav 28,184,164
EX 138
Maav 32,184,168
EX 139
Maav 36,184,172
WL 6
EX 140
Maav 40,188,156
s_nop 0
TR 208,12,34848
TR 210,12,35360
Maav 44,188,160
EX 141
Maav 48,188,164
EX 142
Maav 52,188,168
EX 143
Maav 56,188,172
EX 144
Maav 60,192,156
EX 145
Maav 64,192,160
EX 146
Maav 68,192,164
EX 147
Maav 72,192,168
EX 148
Maav 76,192,172
WL 4
EX 149
Maav 80,196,156
s_memrealtime s[94:95]
s_waitcnt vmcnt(17) lgkmcnt(0)
s_barrier
s_sub_u32 s95,s94,s96
s_cmp_lt_i32 s95,0
s_cbranch_scc1 LB_pc51
LB_pb51:
s_add_u32 s96,s94,90
Maav 84,196,160
s_add_u32 s6,s80,8192
RA 180,10,51936
CV 176,116,117
Maav 88,196,164
s_and_b32 s6,s6,0x1e000
RA 184,10,52000
CV 177,118,119
Maav 92,196,168
s_or_b32 s6,s6,0x40000
EX 150
Maav 96,196,172
s_xor_b32 s7,s6,0x30000
EX 151
Maav 100,200,156
M0 78,37792
RA 188,10,52064
CV 180,120,121
Maav 104,200,160
DN 19,32,6,0
Maav 108,200,164
EX 152
Maav 112,200,168
EX 153
Maav 116,200,172
EX 154
Maav 120,204,156
RA 192,10,52128
CV 181,122,123
Maav 124,204,160
EX 155
Maav 128,204,164
CV 184,124,125
CV 185,126,127
Maav 132,204,168
M0 78,42016
CV 188,128,129
CV 189,130,131
Maav 136,204,172
DN 33,32,6,0
Maav 140,208,156
RA 196,10,52448
CV 192,132,133
Maav 144,208,160
CV 193,134,135
CV 178,136,137
Maav 148,208,164
CV 179,138,139
CV 182,140,141
Maav 152,208,168
CV 183,142,143
CV 186,144,145
Maav 156,208,172
CV 187,146,147
CV 190,148,149
Maav 160,212,156
CV 191,150,151
CV 194,152,153
Maav 164,212,160
M0 78,46240
CV 195,154,155
Maav 168,212,164
DN 19,32,7,0
Maav 172,212,168
Maav 176,212,172
WL 3
Mvavc 116,180,36,196
RA 200,10,52512
Mvav 116,184,40
Mvavc 120,180,52,200
Mvav 120,184,56
Mvavc 124,180,68,204
M0 78,50464
Mvav 124,184,72
RA 204,10,52576
Mvavc 128,180,84,208
DN 33,32,7,0
Mvav 128,184,88
Mvavc 132,180,100,212
Mvav 132,184,104
WL 3
Mvav 116,188,44
RA 208,10,52640
Mvav 116,192,48
Mvav 120,188,60
Mvav 120,192,64
M0 78,55104
Mvav 124,188,76
DN 19,36,4,0
Mvav 124,192,80
s_nop 0
TR 180,13,0
TR 182,13,512
Mvav 128,188,92
EX 116
Mvav 128,192,96
EX 117
Mvav 132,188,108
EX 118
Mvav 132,192,112
WL 4
EX 119
Mvavc 136,196,36,196
s_nop 0
TR 184,13,32
TR 186,13,544
Mvav 136,200,40
M0 78,59328
EX 120
Mvavc 140,196,52,200
DN 33,36,4,0
Mvav 140,200,56
EX 121
Mvavc 144,196,68,204
EX 122
Mvav 144,200,72
s_nop 0
TR 188,13,64
TR 190,13,576
Mvavc 148,196,84,208
EX 123
Mvav 148,200,88
EX 124
Mvavc 152,196,100,212
EX 125
Mvav 152,200,104
WL 6
EX 126
Mvav 136,204,44
M0 78,63552
TR 192,13,96
TR 194,13,608
Mvav 136,208,48
DN 19,36,5,0
Mvav 140,204,60
EX 127
Mvav 140,208,64
EX 128
Mvav 144,204,76
EX 129
Mvav 144,208,80
s_nop 0
TR 196,13,128
TR 198,13,640
Mvav 148,204,92
EX 130
Mvav 148,208,96
EX 131
Mvav 152,204,108
M0 78,67776
EX 132
Mvav 152,208,112
WL 6
DN 33,36,5,0
Maav 0,180,176
s_nop 0
TR 200,13,160
TR 202,13,672
Maav 4,180,180
EX 133
Maav 8,180,184
EX 134
Maav 12,180,188
EX 135
Maav 16,180,192
EX 136
Maav 20,184,176
s_nop 0
TR 204,13,192
TR 206,13,704
Maav 24,184,180
EX 137
Maav 28,184,184
EX 138
Maav 32,184,188
EX 139
Maav 36,184,192
WL 6
EX 140
Maav 40,188,176
s_nop 0
TR 208,13,224
TR 210,13,736
Maav 44,188,180
EX 141
Maav 48,188,184
EX 142
Maav 52,188,188
EX 143
Maav 56,188,192
EX 144
Maav 60,192,176
EX 145
Maav 64,192,180
EX 146
Maav 68,192,184
EX 147
Maav 72,192,188
EX 148
Maav 76,192,192
WL 4
EX 149
Maav 80,196,176
s_memrealtime s[94:95]
s_waitcnt vmcnt(16) lgkmcnt(0)
s_barrier
s_sub_u32 s95,s94,s96
s_cmp_lt_i32 s95,0
s_cbranch_scc1 LB_pc52
LB_pb52:
s_add_u32 s96,s94,90
Maav 84,196,180
s_add_u32 s4,s80,16384
RA 180,11,17312
CV 156,116,117
Maav 88,196,184
s_and_b32 s4,s4,0x1e000
RA 184,11,17376
CV 157,118,119
Maav 92,196,188
s_or_b32 s4,s4,0x40000
EX 150
Maav 96,196,192
s_xor_b32 s5,s4,0x30000
EX 151
Maav 100,200,176
M0 78,72416
RA 188,11,17440
CV 160,120,121
Maav 104,200,180
DN 19,32,4,0
Maav 108,200,184
EX 152
Maav 112,200,188
EX 153
Maav 116,200,192
EX 154
Maav 120,204,176
RA 192,11,17504
CV 161,122,123
Maav 124,204,180
EX 155
Maav 128,204,184
CV 164,124,125
CV 165,126,127
Maav 132,204,188
M0 78,76640
CV 168,128,129
CV 169,130,131
Maav 136,204,192
DN 33,32,4,0
Maav 140,208,176
RA 196,11,17824
CV 172,132,133
Maav 144,208,180
CV 173,134,135
CV 158,136,137
Maav 148,208,184
CV 159,138,139
CV 162,140,141
Maav 152,208,188
CV 163,142,143
CV 166,144,145
Maav 156,208,192
CV 167,146,147
CV 170,148,149
Maav 160,212,176
CV 171,150,151
CV 174,152,153
Maav 164,212,180
M0 78,80864
CV 175,154,155
Maav 168,212,184
DN 19,32,5,0
Maav 172,212,188
Maav 176,212,192
WL 3
Mvavc 116,180,36,196
RA 200,11,17888
Mvav 116,184,40
Mvavc 120,180,52,200
Mvav 120,184,56
Mvavc 124,180,68,204
M0 78,85088
Mvav 124,184,72
RA 204,11,17952
Mvavc 128,180,84,208
DN 33,32,5,0
Mvav 128,184,88
Mvavc 132,180,100,212
Mvav 132,184,104
WL 3
Mvav 116,188,44
RA 208,11,18016
Mvav 116,192,48
Mvav 120,188,60
Mvav 120,192,64
M0 78,89728
Mvav 124,188,76
DN 19,36,6,0
Mvav 124,192,80
s_nop 0
TR 180,13,34624
TR 182,13,35136
Mvav 128,188,92
EX 116
Mvav 128,192,96
EX 117
Mvav 132,188,108
EX 118
Mvav 132,192,112
WL 4
EX 119
Mvavc 136,196,36,196
s_nop 0
TR 184,13,34656
TR 186,13,35168
Mvav 136,200,40
M0 78,93952
EX 120
Mvavc 140,196,52,200
DN 33,36,6,0
Mvav 140,200,56
EX 121
Mvavc 144,196,68,204
EX 122
Mvav 144,200,72
s_nop 0
TR 188,13,34688
TR 190,13,35200
Mvavc 148,196,84,208
EX 123
Mvav 148,200,88
EX 124
Mvavc 152,196,100,212
EX 125
Mvav 152,200,104
WL 6
EX 126
Mvav 136,204,44
M0 78,98176
TR 192,13,34720
TR 194,13,35232
Mvav 136,208,48
DN 19,36,7,0
Mvav 140,204,60
EX 127
Mvav 140,208,64
EX 128
Mvav 144,204,76
EX 129
Mvav 144,208,80
s_nop 0
TR 196,13,34752
TR 198,13,35264
Mvav 148,204,92
EX 130
Mvav 148,208,96
EX 131
Mvav 152,204,108
M0 78,102400
EX 132
Mvav 152,208,112
WL 6
DN 33,36,7,0
Maav 0,180,156
s_nop 0
TR 200,13,34784
TR 202,13,35296
Maav 4,180,160
EX 133
Maav 8,180,164
EX 134
Maav 12,180,168
EX 135
Maav 16,180,172
EX 136
Maav 20,184,156
s_nop 0
TR 204,13,34816
TR 206,13,35328
Maav 24,184,160
EX 137
Maav 28,184,164
EX 138
Maav 32,184,168
EX 139
Maav 36,184,172
WL 6
EX 140
Maav 40,188,156
s_nop 0
TR 208,13,34848
TR 210,13,35360
Maav 44,188,160
EX 141
Maav 48,188,164
EX 142
Maav 52,188,168
EX 143
Maav 56,188,172
EX 144
Maav 60,192,156
EX 145
Maav 64,192,160
EX 146
Maav 68,192,164
EX 147
Maav 72,192,168
EX 148
Maav 76,192,172
WL 4
EX 149
Maav 80,196,156
s_memrealtime s[94:95]
s_waitcnt vmcnt(20) lgkmcnt(0)
s_barrier
s_sub_u32 s95,s94,s96
s_cmp_lt_i32 s95,0
s_cbranch_scc1 LB_pc53
LB_pb53:
s_add_u32 s96,s94,90
Maav 84,196,160
s_add_u32 s6,s80,24576
TR 180,13,51936
TR 182,13,52448
Maav 88,196,164
s_and_b32 s6,s6,0x1e000
TR 184,13,51968
TR 186,13,52480
Maav 92,196,168
s_or_b32 s6,s6,0x40000
EX 150
Maav 96,196,172
s_xor_b32 s7,s6,0x30000
EX 151
Maav 100,200,156
M0 78,107040
TR 188,13,52000
TR 190,13,52512
Maav 104,200,160
DN 19,32,6,0
Maav 108,200,164
EX 152
Maav 112,200,168
EX 153
Maav 116,200,172
EX 154
CV 194,152,153
Maav 120,204,156
s_nop 0
TR 192,13,52032
TR 194,13,52544
Maav 124,204,160
EX 155
Maav 128,204,164
CV 176,116,117
CV 177,118,119
CV 195,154,155
Maav 132,204,168
M0 78,111264
CV 180,120,121
CV 181,122,123
Maav 136,204,172
DN 33,32,6,0
Maav 140,208,156
s_nop 0
TR 196,13,52064
TR 198,13,52576
Maav 144,208,160
CV 184,124,125
CV 185,126,127
Maav 148,208,164
CV 188,128,129
CV 189,130,131
Maav 152,208,168
CV 192,132,133
CV 193,134,135
Maav 156,208,172
CV 178,136,137
CV 179,138,139
Maav 160,212,156
CV 182,140,141
CV 183,142,143
Maav 164,212,160
M0 78,115488
CV 186,144,145
CV 187,146,147
Maav 168,212,164
DN 19,32,7,0
Maav 172,212,168
CV 190,148,149
CV 191,150,151
Maav 176,212,172
v_mov_b32 v36,v216
v_mov_b32 v37,v217
Maav 160,212,176
v_mov_b32 v38,v218
v_mov_b32 v39,v219
Maav 164,212,180
v_mov_b32 v52,v220
v_mov_b32 v53,v221
Maav 168,212,184
v_mov_b32 v54,v222
v_mov_b32 v55,v223
Maav 172,212,188
v_mov_b32 v68,v224
v_mov_b32 v69,v225
Maav 176,212,192
WL 6
v_mov_b32 v70,v226
v_mov_b32 v71,v227
Maav 0,180,176
M0 78,119712
TR 200,13,52096
TR 202,13,52608
Maav 4,180,180
DN 33,32,7,0
Maav 8,180,184
v_mov_b32 v84,v228
v_mov_b32 v85,v229
Maav 12,180,188
v_mov_b32 v86,v230
v_mov_b32 v87,v231
AR 28,160
Maav 16,180,192
v_mov_b32 v100,v232
v_mov_b32 v101,v233
AR 29,164
Maav 20,184,176
s_nop 0
TR 204,13,52128
TR 206,13,52640
AR 30,168
Maav 24,184,180
v_mov_b32 v102,v234
v_mov_b32 v103,v235
AR 31,172
Maav 28,184,184
v_mov_b32 v40,v236
v_mov_b32 v41,v237
AR 32,176
Maav 32,184,188
M0 78,124352
v_mov_b32 v42,v238
v_mov_b32 v43,v239
v_cmp_nle_f32 vcc,v28,s75
s_nop 0
s_or_b64 s[86:87],s[86:87],vcc
Maav 36,184,192
WL 6
DN 19,36,4,0
Maav 40,188,176
s_nop 0
TR 208,13,52160
TR 210,13,52672
v_cmp_nle_f32 vcc,v29,s75
s_nop 0
s_or_b64 s[86:87],s[86:87],vcc
Maav 44,188,180
v_mov_b32 v56,v240
v_mov_b32 v57,v241
v_cmp_nle_f32 vcc,v30,s75
s_nop 0
s_or_b64 s[86:87],s[86:87],vcc
Maav 48,188,184
v_mov_b32 v58,v242
v_mov_b32 v59,v243
v_cmp_nle_f32 vcc,v31,s75
s_nop 0
s_or_b64 s[86:87],s[86:87],vcc
Maav 52,188,188
v_mov_b32 v72,v244
v_mov_b32 v73,v245
v_cmp_nle_f32 vcc,v32,s75
s_nop 0
s_or_b64 s[86:87],s[86:87],vcc
Maav 56,188,192
v_mov_b32 v74,v246
v_mov_b32 v75,v247
v_rcp_f32 v28,v28
Maav 60,192,176
v_mov_b32 v88,v248
v_mov_b32 v89,v249
v_rcp_f32 v29,v29
Maav 64,192,180
M0 78,128576
v_mov_b32 v90,v250
v_mov_b32 v91,v251
v_rcp_f32 v30,v30
Maav 68,192,184
DN 33,36,4,0
v_rcp_f32 v31,v31
Maav 72,192,188
v_mov_b32 v104,v252
v_mov_b32 v105,v253
v_rcp_f32 v32,v32
Maav 76,192,192
WL 4
v_mov_b32 v106,v254
v_mov_b32 v107,v255
Maav 80,196,176
s_memrealtime s[94:95]
s_waitcnt vmcnt(22) lgkmcnt(0)
s_barrier
s_sub_u32 s95,s94,s96
s_cmp_lt_i32 s95,0
s_cbranch_scc1 LB_pc54
LB_pb54:
s_add_u32 s96,s94,90
Maav 84,196,180
s_add_u32 s98,s71,81920
RA 180,10,0
v_lshl_add_u32 v8,v2,1,s98
Maav 88,196,184
RA 184,10,64
AR 44,216
Maav 92,196,188
buffer_load_dword v8,v8,s[60:63],0 offen
Maav 96,196,192
M0 78,132800
AR 45,217
AR 46,218
Maav 100,200,176
RA 188,10,128
AR 47,219
Maav 104,200,180
DN 19,36,5,0
Maav 108,200,184
AR 60,220
AR 61,221
Maav 112,200,188
AR 62,222
AR 63,223
Maav 116,200,192
AR 76,224
AR 77,225
Maav 120,204,176
RA 192,10,192
AR 78,226
Maav 124,204,180
AR 79,227
AR 92,228
Maav 128,204,184
AR 93,229
AR 94,230
Maav 132,204,188
M0 78,137024
AR 95,231
AR 108,232
Maav 136,204,192
DN 33,36,5,0
Maav 140,208,176
RA 196,10,512
AR 109,233
Maav 144,208,180
AR 110,234
AR 111,235
Maav 148,208,184
AR 48,236
AR 49,237
Maav 152,208,188
AR 50,238
AR 51,239
Maav 156,208,192
WL 3
AR 64,240
AR 65,241
AR 66,242
AR 67,243
AR 80,244
AR 81,245
AR 82,246
AR 83,247
AR 96,248
AR 97,249
AR 98,250
AR 99,251
AR 112,252
AR 113,253
AR 114,254
AR 115,255
AR 116,0
AR 117,1
AR 118,2
AR 119,3
AR 120,20
AR 121,21
AR 122,22
AR 123,23
v_pk_mul_f32 v[116:117],v[116:117],v[28:29] op_sel_hi:[1,0]
v_pk_mul_f32 v[118:119],v[118:119],v[28:29] op_sel_hi:[1,0]
v_pk_mul_f32 v[120:121],v[120:121],v[28:29] op_sel_hi:[1,0]
v_pk_mul_f32 v[122:123],v[122:123],v[28:29] op_sel_hi:[1,0]
CV 216,116,117
CV 217,118,119
CV 218,120,121
CV 219,122,123
AR 124,4
AR 125,5
AR 126,6
AR 127,7
AR 128,24
AR 129,25
AR 130,26
AR 131,27
v_pk_mul_f32 v[124:125],v[124:125],v[28:29] op_sel:[0,1] op_sel_hi:[1,1]
v_pk_mul_f32 v[126:127],v[126:127],v[28:29] op_sel:[0,1] op_sel_hi:[1,1]
v_pk_mul_f32 v[128:129],v[128:129],v[28:29] op_sel:[0,1] op_sel_hi:[1,1]
v_pk_mul_f32 v[130:131],v[130:131],v[28:29] op_sel:[0,1] op_sel_hi:[1,1]
CV 220,124,125
CV 221,126,127
CV 222,128,129
CV 223,130,131
AR 132,8
AR 133,9
AR 134,10
AR 135,11
AR 136,28
AR 137,29
AR 138,30
AR 139,31
v_pk_mul_f32 v[132:133],v[132:133],v[30:31] op_sel_hi:[1,0]
v_pk_mul_f32 v[134:135],v[134:135],v[30:31] op_sel_hi:[1,0]
v_pk_mul_f32 v[136:137],v[136:137],v[30:31] op_sel_hi:[1,0]
v_pk_mul_f32 v[138:139],v[138:139],v[30:31] op_sel_hi:[1,0]
CV 224,132,133
CV 225,134,135
CV 226,136,137
CV 227,138,139
AR 140,12
AR 141,13
AR 142,14
AR 143,15
AR 144,32
AR 145,33
AR 146,34
AR 147,35
v_pk_mul_f32 v[140:141],v[140:141],v[30:31] op_sel:[0,1] op_sel_hi:[1,1]
v_pk_mul_f32 v[142:143],v[142:143],v[30:31] op_sel:[0,1] op_sel_hi:[1,1]
v_pk_mul_f32 v[144:145],v[144:145],v[30:31] op_sel:[0,1] op_sel_hi:[1,1]
v_pk_mul_f32 v[146:147],v[146:147],v[30:31] op_sel:[0,1] op_sel_hi:[1,1]
CV 228,140,141
CV 229,142,143
CV 230,144,145
CV 231,146,147
AR 148,16
AR 149,17
AR 150,18
AR 151,19
AR 152,36
AR 153,37
AR 154,38
AR 155,39
v_pk_mul_f32 v[148:149],v[148:149],v[32:33] op_sel_hi:[1,0]
v_pk_mul_f32 v[150:151],v[150:151],v[32:33] op_sel_hi:[1,0]
v_pk_mul_f32 v[152:153],v[152:153],v[32:33] op_sel_hi:[1,0]
v_pk_mul_f32 v[154:155],v[154:155],v[32:33] op_sel_hi:[1,0]
CV 232,148,149
CV 233,150,151
CV 234,152,153
CV 235,154,155
AR 116,40
AR 117,41
AR 118,42
AR 119,43
AR 120,60
AR 121,61
AR 122,62
AR 123,63
v_pk_mul_f32 v[116:117],v[116:117],v[28:29] op_sel_hi:[1,0]
v_pk_mul_f32 v[118:119],v[118:119],v[28:29] op_sel_hi:[1,0]
v_pk_mul_f32 v[120:121],v[120:121],v[28:29] op_sel_hi:[1,0]
v_pk_mul_f32 v[122:123],v[122:123],v[28:29] op_sel_hi:[1,0]
CV 236,116,117
CV 237,118,119
CV 238,120,121
CV 239,122,123
AR 124,44
AR 125,45
AR 126,46
AR 127,47
AR 128,64
AR 129,65
AR 130,66
AR 131,67
v_pk_mul_f32 v[124:125],v[124:125],v[28:29] op_sel:[0,1] op_sel_hi:[1,1]
v_pk_mul_f32 v[126:127],v[126:127],v[28:29] op_sel:[0,1] op_sel_hi:[1,1]
v_pk_mul_f32 v[128:129],v[128:129],v[28:29] op_sel:[0,1] op_sel_hi:[1,1]
v_pk_mul_f32 v[130:131],v[130:131],v[28:29] op_sel:[0,1] op_sel_hi:[1,1]
CV 240,124,125
CV 241,126,127
CV 242,128,129
CV 243,130,131
AR 132,48
AR 133,49
AR 134,50
AR 135,51
AR 136,68
AR 137,69
AR 138,70
AR 139,71
v_pk_mul_f32 v[132:133],v[132:133],v[30:31] op_sel_hi:[1,0]
v_pk_mul_f32 v[134:135],v[134:135],v[30:31] op_sel_hi:[1,0]
v_pk_mul_f32 v[136:137],v[136:137],v[30:31] op_sel_hi:[1,0]
v_pk_mul_f32 v[138:139],v[138:139],v[30:31] op_sel_hi:[1,0]
CV 244,132,133
CV 245,134,135
CV 246,136,137
CV 247,138,139
AR 140,52
AR 141,53
AR 142,54
AR 143,55
AR 144,72
AR 145,73
AR 146,74
AR 147,75
v_pk_mul_f32 v[140:141],v[140:141],v[30:31] op_sel:[0,1] op_sel_hi:[1,1]
v_pk_mul_f32 v[142:143],v[142:143],v[30:31] op_sel:[0,1] op_sel_hi:[1,1]
v_pk_mul_f32 v[144:145],v[144:145],v[30:31] op_sel:[0,1] op_sel_hi:[1,1]
v_pk_mul_f32 v[146:147],v[146:147],v[30:31] op_sel:[0,1] op_sel_hi:[1,1]
CV 248,140,141
CV 249,142,143
CV 250,144,145
CV 251,146,147
AR 148,56
AR 149,57
AR 150,58
AR 151,59
AR 152,76
AR 153,77
AR 154,78
AR 155,79
v_pk_mul_f32 v[148:149],v[148:149],v[32:33] op_sel_hi:[1,0]
v_pk_mul_f32 v[150:151],v[150:151],v[32:33] op_sel_hi:[1,0]
v_pk_mul_f32 v[152:153],v[152:153],v[32:33] op_sel_hi:[1,0]
v_pk_mul_f32 v[154:155],v[154:155],v[32:33] op_sel_hi:[1,0]
CV 252,148,149
CV 253,150,151
CV 254,152,153
CV 255,154,155
AR 116,80
AR 117,81
AR 118,82
AR 119,83
AR 120,100
AR 121,101
AR 122,102
AR 123,103
v_pk_mul_f32 v[116:117],v[116:117],v[28:29] op_sel_hi:[1,0]
v_pk_mul_f32 v[118:119],v[118:119],v[28:29] op_sel_hi:[1,0]
v_pk_mul_f32 v[120:121],v[120:121],v[28:29] op_sel_hi:[1,0]
v_pk_mul_f32 v[122:123],v[122:123],v[28:29] op_sel_hi:[1,0]
CV 116,116,117
CV 117,118,119
CV 118,120,121
CV 119,122,123
v_accvgpr_write_b32 a216,v116
v_accvgpr_write_b32 a217,v117
v_accvgpr_write_b32 a218,v118
v_accvgpr_write_b32 a219,v119
AR 124,84
AR 125,85
AR 126,86
AR 127,87
AR 128,104
AR 129,105
AR 130,106
AR 131,107
v_pk_mul_f32 v[124:125],v[124:125],v[28:29] op_sel:[0,1] op_sel_hi:[1,1]
v_pk_mul_f32 v[126:127],v[126:127],v[28:29] op_sel:[0,1] op_sel_hi:[1,1]
v_pk_mul_f32 v[128:129],v[128:129],v[28:29] op_sel:[0,1] op_sel_hi:[1,1]
v_pk_mul_f32 v[130:131],v[130:131],v[28:29] op_sel:[0,1] op_sel_hi:[1,1]
CV 124,124,125
CV 125,126,127
CV 126,128,129
CV 127,130,131
v_accvgpr_write_b32 a220,v124
v_accvgpr_write_b32 a221,v125
v_accvgpr_write_b32 a222,v126
v_accvgpr_write_b32 a223,v127
AR 132,88
AR 133,89
AR 134,90
AR 135,91
AR 136,108
AR 137,109
AR 138,110
AR 139,111
v_pk_mul_f32 v[132:133],v[132:133],v[30:31] op_sel_hi:[1,0]
v_pk_mul_f32 v[134:135],v[134:135],v[30:31] op_sel_hi:[1,0]
v_pk_mul_f32 v[136:137],v[136:137],v[30:31] op_sel_hi:[1,0]
v_pk_mul_f32 v[138:139],v[138:139],v[30:31] op_sel_hi:[1,0]
CV 132,132,133
CV 133,134,135
CV 134,136,137
CV 135,138,139
v_accvgpr_write_b32 a224,v132
v_accvgpr_write_b32 a225,v133
v_accvgpr_write_b32 a226,v134
v_accvgpr_write_b32 a227,v135
AR 140,92
AR 141,93
AR 142,94
AR 143,95
AR 144,112
AR 145,113
AR 146,114
AR 147,115
v_pk_mul_f32 v[140:141],v[140:141],v[30:31] op_sel:[0,1] op_sel_hi:[1,1]
v_pk_mul_f32 v[142:143],v[142:143],v[30:31] op_sel:[0,1] op_sel_hi:[1,1]
v_pk_mul_f32 v[144:145],v[144:145],v[30:31] op_sel:[0,1] op_sel_hi:[1,1]
v_pk_mul_f32 v[146:147],v[146:147],v[30:31] op_sel:[0,1] op_sel_hi:[1,1]
CV 140,140,141
CV 141,142,143
CV 142,144,145
CV 143,146,147
v_accvgpr_write_b32 a228,v140
v_accvgpr_write_b32 a229,v141
v_accvgpr_write_b32 a230,v142
v_accvgpr_write_b32 a231,v143
AR 148,96
AR 149,97
AR 150,98
AR 151,99
AR 152,116
AR 153,117
AR 154,118
AR 155,119
v_pk_mul_f32 v[148:149],v[148:149],v[32:33] op_sel_hi:[1,0]
v_pk_mul_f32 v[150:151],v[150:151],v[32:33] op_sel_hi:[1,0]
v_pk_mul_f32 v[152:153],v[152:153],v[32:33] op_sel_hi:[1,0]
v_pk_mul_f32 v[154:155],v[154:155],v[32:33] op_sel_hi:[1,0]
CV 148,148,149
CV 149,150,151
CV 150,152,153
CV 151,154,155
v_accvgpr_write_b32 a232,v148
v_accvgpr_write_b32 a233,v149
v_accvgpr_write_b32 a234,v150
v_accvgpr_write_b32 a235,v151
AR 116,120
AR 117,121
AR 118,122
AR 119,123
AR 120,140
AR 121,141
AR 122,142
AR 123,143
v_pk_mul_f32 v[116:117],v[116:117],v[28:29] op_sel_hi:[1,0]
v_pk_mul_f32 v[118:119],v[118:119],v[28:29] op_sel_hi:[1,0]
v_pk_mul_f32 v[120:121],v[120:121],v[28:29] op_sel_hi:[1,0]
v_pk_mul_f32 v[122:123],v[122:123],v[28:29] op_sel_hi:[1,0]
CV 116,116,117
CV 117,118,119
CV 118,120,121
CV 119,122,123
v_accvgpr_write_b32 a236,v116
v_accvgpr_write_b32 a237,v117
v_accvgpr_write_b32 a238,v118
v_accvgpr_write_b32 a239,v119
AR 124,124
AR 125,125
AR 126,126
AR 127,127
AR 128,144
AR 129,145
AR 130,146
AR 131,147
v_pk_mul_f32 v[124:125],v[124:125],v[28:29] op_sel:[0,1] op_sel_hi:[1,1]
v_pk_mul_f32 v[126:127],v[126:127],v[28:29] op_sel:[0,1] op_sel_hi:[1,1]
v_pk_mul_f32 v[128:129],v[128:129],v[28:29] op_sel:[0,1] op_sel_hi:[1,1]
v_pk_mul_f32 v[130:131],v[130:131],v[28:29] op_sel:[0,1] op_sel_hi:[1,1]
CV 124,124,125
CV 125,126,127
CV 126,128,129
CV 127,130,131
v_accvgpr_write_b32 a240,v124
v_accvgpr_write_b32 a241,v125
v_accvgpr_write_b32 a242,v126
v_accvgpr_write_b32 a243,v127
AR 132,128
AR 133,129
AR 134,130
AR 135,131
AR 136,148
AR 137,149
AR 138,150
AR 139,151
v_pk_mul_f32 v[132:133],v[132:133],v[30:31] op_sel_hi:[1,0]
v_pk_mul_f32 v[134:135],v[134:135],v[30:31] op_sel_hi:[1,0]
v_pk_mul_f32 v[136:137],v[136:137],v[30:31] op_sel_hi:[1,0]
v_pk_mul_f32 v[138:139],v[138:139],v[30:31] op_sel_hi:[1,0]
CV 132,132,133
CV 133,134,135
CV 134,136,137
CV 135,138,139
v_accvgpr_write_b32 a244,v132
v_accvgpr_write_b32 a245,v133
v_accvgpr_write_b32 a246,v134
v_accvgpr_write_b32 a247,v135
AR 140,132
AR 141,133
AR 142,134
AR 143,135
AR 144,152
AR 145,153
AR 146,154
AR 147,155
v_pk_mul_f32 v[140:141],v[140:141],v[30:31] op_sel:[0,1] op_sel_hi:[1,1]
v_pk_mul_f32 v[142:143],v[142:143],v[30:31] op_sel:[0,1] op_sel_hi:[1,1]
v_pk_mul_f32 v[144:145],v[144:145],v[30:31] op_sel:[0,1] op_sel_hi:[1,1]
v_pk_mul_f32 v[146:147],v[146:147],v[30:31] op_sel:[0,1] op_sel_hi:[1,1]
CV 140,140,141
CV 141,142,143
CV 142,144,145
CV 143,146,147
v_accvgpr_write_b32 a248,v140
v_accvgpr_write_b32 a249,v141
v_accvgpr_write_b32 a250,v142
v_accvgpr_write_b32 a251,v143
AR 148,136
AR 149,137
AR 150,138
AR 151,139
AR 152,156
AR 153,157
AR 154,158
AR 155,159
v_pk_mul_f32 v[148:149],v[148:149],v[32:33] op_sel_hi:[1,0]
v_pk_mul_f32 v[150:151],v[150:151],v[32:33] op_sel_hi:[1,0]
v_pk_mul_f32 v[152:153],v[152:153],v[32:33] op_sel_hi:[1,0]
v_pk_mul_f32 v[154:155],v[154:155],v[32:33] op_sel_hi:[1,0]
CV 148,148,149
CV 149,150,151
CV 150,152,153
CV 151,154,155
v_accvgpr_write_b32 a252,v148
v_accvgpr_write_b32 a253,v149
v_accvgpr_write_b32 a254,v150
v_accvgpr_write_b32 a255,v151
Mvavz 116,180,36
RA 200,10,576
Mvavz 120,180,52
s_add_u32 s4,s80,32768
Mvavz 124,180,68
s_and_b32 s4,s4,0x1e000
Mvavz 128,180,84
s_or_b32 s4,s4,0x40000
Mvavz 132,180,100
s_xor_b32 s5,s4,0x30000
Mvav 116,184,40
M0 78,141664
RA 204,10,640
Mvav 120,184,56
DN 19,32,4,0
Mvav 124,184,72
Mvav 128,184,88
Mvav 132,184,104
WL 3
Mvav 116,188,44
RA 208,10,704
Mvav 120,188,60
Mvav 124,188,76
Mvav 128,188,92
M0 78,145888
Mvav 132,188,108
DN 33,32,4,0
Mvav 116,192,48
Mvav 120,192,64
Mvav 124,192,80
Mvav 128,192,96
Mvav 132,192,112
WL 2
s_cmp_lg_u32 s85,0
s_cbranch_scc1 LB_safe1
LB_back1:
s_nop 0
v_max3_f32 v196,v116,v117,v118
v_max3_f32 v200,v120,v121,v122
v_max3_f32 v204,v124,v125,v126
v_max3_f32 v208,v128,v129,v130
v_max3_f32 v212,v132,v133,v134
v_max3_f32 v196,v196,v119,v22
v_max3_f32 v200,v200,v123,v23
v_max3_f32 v204,v204,v127,v24
v_max3_f32 v208,v208,v131,v25
v_max3_f32 v212,v212,v135,v26
v_mov_b32 v197,v196
v_mov_b32 v201,v200
v_mov_b32 v205,v204
v_mov_b32 v209,v208
v_mov_b32 v213,v212
v_permlane16_swap_b32 v196,v197
v_permlane16_swap_b32 v200,v201
v_permlane16_swap_b32 v204,v205
v_permlane16_swap_b32 v208,v209
v_permlane16_swap_b32 v212,v213
v_max_f32 v196,v196,v197
v_max_f32 v200,v200,v201
v_max_f32 v204,v204,v205
v_max_f32 v208,v208,v209
v_max_f32 v212,v212,v213
v_mov_b32 v197,v196
v_mov_b32 v201,v200
v_mov_b32 v205,v204
v_mov_b32 v209,v208
v_mov_b32 v213,v212
v_permlane32_swap_b32 v196,v197
v_permlane32_swap_b32 v200,v201
v_permlane32_swap_b32 v204,v205
v_permlane32_swap_b32 v208,v209
v_permlane32_swap_b32 v212,v213
v_max_f32 v196,v196,v197
v_max_f32 v200,v200,v201
v_max_f32 v204,v204,v205
v_max_f32 v208,v208,v209
v_max_f32 v212,v212,v213
v_sub_f32 v199,0,v196
v_sub_f32 v198,0,v196
v_sub_f32 v197,0,v196
v_sub_f32 v196,0,v196
v_sub_f32 v203,0,v200
v_sub_f32 v202,0,v200
v_sub_f32 v201,0,v200
v_sub_f32 v200,0,v200
v_sub_f32 v207,0,v204
v_sub_f32 v206,0,v204
v_sub_f32 v205,0,v204
v_sub_f32 v204,0,v204
v_sub_f32 v211,0,v208
v_sub_f32 v210,0,v208
v_sub_f32 v209,0,v208
v_sub_f32 v208,0,v208
v_sub_f32 v215,0,v212
v_sub_f32 v214,0,v212
v_sub_f32 v213,0,v212
v_sub_f32 v212,0,v212
Mvavc 136,196,36,196
s_memrealtime s[94:95]
s_waitcnt vmcnt(19) lgkmcnt(0)
s_barrier
s_sub_u32 s95,s94,s96
s_cmp_lt_i32 s95,0
s_cbranch_scc1 LB_pc55
LB_pb55:
s_add_u32 s96,s94,90
Mvav 136,200,40
M0 78,150112
RA 180,10,17312
v_add_f32 v116,v116,v196
Mvavc 140,196,52,200
RA 184,10,17376
v_add_f32 v120,v120,v200
Mvav 140,200,56
DN 19,32,5,0
Mvavc 144,196,68,204
EX 116
Mvav 144,200,72
RA 188,10,17440
v_add_f32 v117,v117,v196
Mvavc 148,196,84,208
EX 117
Mvav 148,200,88
v_add_f32 v118,v118,v196
v_add_f32 v124,v124,v204
CV 156,116,117
Mvavc 152,196,100,212
EX 118
Mvav 152,200,104
EX 120
Mvav 136,204,44
M0 78,154336
RA 192,10,17504
v_add_f32 v119,v119,v196
Mvav 136,208,48
DN 33,32,5,0
Mvav 140,204,60
EX 119
Mvav 140,208,64
v_add_f32 v121,v121,v200
v_add_f32 v128,v128,v208
CV 157,118,119
Mvav 144,204,76
EX 121
Mvav 144,208,80
RA 196,10,17824
v_add_f32 v122,v122,v200
Mvav 148,204,92
EX 122
Mvav 148,208,96
v_add_f32 v123,v123,v200
v_add_f32 v132,v132,v212
Mvav 152,204,108
M0 78,20480
EX 123
Mvav 152,208,112
WL 3
DN 19,36,6,0
CV 161,122,123
Mvavc 116,180,36,196
RA 200,10,17888
CV 160,120,121
Mvav 116,184,40
EX 124
Mvavc 120,180,52,200
v_add_f32 v125,v125,v204
EX 125
v_add_f32 v126,v126,v204
EX 126
v_add_f32 v127,v127,v204
EX 127
Mvav 120,184,56
EX 128
CV 164,124,125
CV 165,126,127
Mvavc 124,180,68,204
v_add_f32 v129,v129,v208
EX 129
v_add_f32 v130,v130,v208
EX 130
v_add_f32 v131,v131,v208
EX 131
Mvav 124,184,72
RA 204,10,17952
CV 168,128,129
CV 169,130,131
Mvavc 128,180,84,208
M0 78,24704
EX 132
v_add_f32 v133,v133,v212
EX 133
v_add_f32 v134,v134,v212
EX 134
v_add_f32 v135,v135,v212
EX 135
Mvav 128,184,88
DN 33,36,6,0
CV 172,132,133
CV 173,134,135
Mvavc 132,180,100,212
EX 136
Mvav 132,184,104
WL 3
EX 137
Mvav 116,188,44
RA 208,10,18016
CV 158,136,137
Mvav 116,192,48
EX 138
Mvav 120,188,60
EX 139
Mvav 120,192,64
EX 140
CV 159,138,139
Mvav 124,188,76
M0 78,28928
EX 141
Mvav 124,192,80
s_nop 0
TR 180,12,34624
TR 182,12,35136
CV 162,140,141
Mvav 128,188,92
DN 19,36,7,0
Mvav 128,192,96
EX 142
Mvav 132,188,108
EX 143
Mvav 132,192,112
WL 4
EX 144
CV 163,142,143
Mvavc 136,196,36,196
s_nop 0
TR 184,12,34656
TR 186,12,35168
Mvav 136,200,40
EX 145
Mvavc 140,196,52,200
EX 146
EX 147
CV 166,144,145
Mvav 140,200,56
M0 78,33152
EX 148
EX 149
CV 167,146,147
Mvavc 144,196,68,204
DN 33,36,7,0
EX 150
EX 151
Mvav 144,200,72
s_nop 0
TR 188,12,34688
TR 190,12,35200
CV 170,148,149
CV 171,150,151
Mvavc 148,196,84,208
EX 152
EX 153
EX 154
EX 155
Mvav 148,200,88
CV 174,152,153
CV 175,154,155
Mvavc 152,196,100,212
EX 116
Mvav 152,200,104
WL 6
EX 117
Mvav 136,204,44
s_nop 0
TR 192,12,34720
TR 194,12,35232
Mvav 136,208,48
EX 118
Mvav 140,204,60
EX 119
Mvav 140,208,64
EX 120
Mvav 144,204,76
EX 121
Mvav 144,208,80
s_nop 0
TR 196,12,34752
TR 198,12,35264
Mvav 148,204,92
EX 122
Mvav 148,208,96
EX 123
Mvav 152,204,108
EX 124
Mvav 152,208,112
WL 6
EX 125
Maavz 0,180,156
s_nop 0
TR 200,12,34784
TR 202,12,35296
Maavz 4,180,160
EX 126
Maavz 8,180,164
EX 127
Maavz 12,180,168
EX 128
Maavz 16,180,172
EX 129
Maavz 20,184,156
s_nop 0
TR 204,12,34816
TR 206,12,35328
Maavz 24,184,160
EX 130
Maavz 28,184,164
EX 131
Maavz 32,184,168
EX 132
Maavz 36,184,172
WL 6
EX 133
Maavz 40,188,156
s_nop 0
TR 208,12,34848
TR 210,12,35360
Maavz 44,188,160
EX 134
Maavz 48,188,164
EX 135
Maavz 52,188,168
EX 136
Maavz 56,188,172
EX 137
Maavz 60,192,156
EX 138
Maavz 64,192,160
EX 139
Maavz 68,192,164
EX 140
Maavz 72,192,168
EX 141
Maavz 76,192,172
WL 4
EX 142
Maavz 80,196,156
s_memrealtime s[94:95]
s_waitcnt vmcnt(17) lgkmcnt(0)
s_barrier
s_sub_u32 s95,s94,s96
s_cmp_lt_i32 s95,0
s_cbranch_scc1 LB_pc56
LB_pb56:
s_add_u32 s96,s94,115
Maavz 84,196,160
s_add_u32 s6,s80,40960
RA 180,10,51936
CV 176,116,117
Maavz 88,196,164
s_and_b32 s6,s6,0x1e000
RA 184,10,52000
CV 177,118,119
Maavz 92,196,168
s_or_b32 s6,s6,0x40000
EX 143
Maavz 96,196,172
s_xor_b32 s7,s6,0x30000
EX 144
Maavz 100,200,156
M0 78,37792
RA 188,10,52064
CV 180,120,121
Maavz 104,200,160
DN 19,32,6,0
Maavz 108,200,164
EX 145
Maavz 112,200,168
EX 146
Maavz 116,200,172
EX 147
Maavz 120,204,156
RA 192,10,52128
CV 181,122,123
Maavz 124,204,160
EX 148
Maavz 128,204,164
EX 149
Maavz 132,204,168
M0 78,42016
EX 150
Maavz 136,204,172
DN 33,32,6,0
Maavz 140,208,156
RA 196,10,52448
CV 184,124,125
Maavz 144,208,160
EX 151
Maavz 148,208,164
EX 152
Maavz 152,208,168
EX 153
Maavz 156,208,172
EX 154
Maavz 160,212,156
EX 155
Maavz 164,212,160
M0 78,46240
CV 185,126,127
CV 188,128,129
Maavz 168,212,164
DN 19,32,7,0
Maavz 172,212,168
CV 189,130,131
CV 192,132,133
Maavz 176,212,172
WL 3
CV 193,134,135
CV 178,136,137
Mvavc 116,180,36,196
RA 200,10,52512
CV 179,138,139
Mvav 116,184,40
CV 182,140,141
CV 183,142,143
Mvavc 120,180,52,200
CV 186,144,145
CV 187,146,147
Mvav 120,184,56
CV 190,148,149
CV 191,150,151
Mvavc 124,180,68,204
M0 78,50464
CV 195,154,155
Mvav 124,184,72
RA 204,10,52576
CV 194,152,153
Mvavc 128,180,84,208
DN 33,32,7,0
Mvav 128,184,88
Mvavc 132,180,100,212
Mvav 132,184,104
WL 3
Mvav 116,188,44
RA 208,10,52640
Mvav 116,192,48
Mvav 120,188,60
Mvav 120,192,64
M0 78,55104
Mvav 124,188,76
DN 19,36,4,0
Mvav 124,192,80
s_nop 0
TR 180,13,0
TR 182,13,512
Mvav 128,188,92
EX 116
Mvav 128,192,96
EX 117
Mvav 132,188,108
EX 118
Mvav 132,192,112
WL 4
EX 119
Mvavc 136,196,36,196
s_nop 0
TR 184,13,32
TR 186,13,544
Mvav 136,200,40
M0 78,59328
EX 120
Mvavc 140,196,52,200
DN 33,36,4,0
Mvav 140,200,56
EX 121
Mvavc 144,196,68,204
EX 122
Mvav 144,200,72
s_nop 0
TR 188,13,64
TR 190,13,576
Mvavc 148,196,84,208
EX 123
Mvav 148,200,88
EX 124
Mvavc 152,196,100,212
EX 125
Mvav 152,200,104
WL 6
EX 126
Mvav 136,204,44
M0 78,63552
TR 192,13,96
TR 194,13,608
Mvav 136,208,48
DN 19,36,5,0
Mvav 140,204,60
EX 127
Mvav 140,208,64
EX 128
Mvav 144,204,76
EX 129
Mvav 144,208,80
s_nop 0
TR 196,13,128
TR 198,13,640
Mvav 148,204,92
EX 130
Mvav 148,208,96
EX 131
Mvav 152,204,108
M0 78,67776
EX 132
Mvav 152,208,112
WL 6
DN 33,36,5,0
Maav 0,180,176
s_nop 0
TR 200,13,160
TR 202,13,672
Maav 4,180,180
EX 133
Maav 8,180,184
EX 134
Maav 12,180,188
EX 135
Maav 16,180,192
EX 136
Maav 20,184,176
s_nop 0
TR 204,13,192
TR 206,13,704
Maav 24,184,180
EX 137
Maav 28,184,184
EX 138
Maav 32,184,188
EX 139
Maav 36,184,192
WL 6
EX 140
Maav 40,188,176
s_nop 0
TR 208,13,224
TR 210,13,736
Maav 44,188,180
EX 141
Maav 48,188,184
EX 142
Maav 52,188,188
EX 143
Maav 56,188,192
EX 144
Maav 60,192,176
EX 145
Maav 64,192,180
EX 146
Maav 68,192,184
EX 147
Maav 72,192,188
EX 148
Maav 76,192,192
WL 4
EX 149
Maav 80,196,176
s_memrealtime s[94:95]
s_waitcnt vmcnt(16) lgkmcnt(0)
s_barrier
s_sub_u32 s95,s94,s96
s_cmp_lt_i32 s95,0
s_cbranch_scc1 LB_pc57
LB_pb57:
s_add_u32 s96,s94,115
Maav 84,196,180
s_add_u32 s4,s80,49152
RA 180,11,17312
CV 156,116,117
Maav 88,196,184
s_and_b32 s4,s4,0x1e000
RA 184,11,17376
CV 157,118,119
Maav 92,196,188
s_or_b32 s4,s4,0x40000
EX 150
Maav 96,196,192
s_xor_b32 s5,s4,0x30000
EX 151
Maav 100,200,176
M0 78,72416
RA 188,11,17440
CV 160,120,121
Maav 104,200,180
DN 19,32,4,0
Maav 108,200,184
EX 152
Maav 112,200,188
EX 153
Maav 116,200,192
EX 154
Maav 120,204,176
RA 192,11,17504
CV 161,122,123
Maav 124,204,180
EX 155
Maav 128,204,184
CV 164,124,125
CV 165,126,127
Maav 132,204,188
M0 78,76640
CV 168,128,129
CV 169,130,131
Maav 136,204,192
DN 33,32,4,0
Maav 140,208,176
RA 196,11,17824
CV 172,132,133
Maav 144,208,180
CV 173,134,135
CV 158,136,137
Maav 148,208,184
CV 159,138,139
CV 162,140,141
Maav 152,208,188
CV 163,142,143
CV 166,144,145
Maav 156,208,192
CV 167,146,147
CV 170,148,149
Maav 160,212,176
CV 171,150,151
CV 174,152,153
Maav 164,212,180
M0 78,80864
CV 175,154,155
Maav 168,212,184
DN 19,32,5,0
Maav 172,212,188
Maav 176,212,192
WL 3
Mvavc 116,180,36,196
RA 200,11,17888
Mvav 116,184,40
Mvavc 120,180,52,200
Mvav 120,184,56
Mvavc 124,180,68,204
M0 78,85088
Mvav 124,184,72
RA 204,11,17952
Mvavc 128,180,84,208
DN 33,32,5,0
Mvav 128,184,88
Mvavc 132,180,100,212
Mvav 132,184,104
WL 3
Mvav 116,188,44
RA 208,11,18016
Mvav 116,192,48
Mvav 120,188,60
Mvav 120,192,64
M0 78,89728
Mvav 124,188,76
DN 19,36,6,0
Mvav 124,192,80
s_nop 0
TR 180,13,34624
TR 182,13,35136
Mvav 128,188,92
EX 116
Mvav 128,192,96
EX 117
Mvav 132,188,108
EX 118
Mvav 132,192,112
WL 4
EX 119
Mvavc 136,196,36,196
s_nop 0
TR 184,13,34656
TR 186,13,35168
Mvav 136,200,40
M0 78,93952
EX 120
Mvavc 140,196,52,200
DN 33,36,6,0
Mvav 140,200,56
EX 121
Mvavc 144,196,68,204
EX 122
Mvav 144,200,72
s_nop 0
TR 188,13,34688
TR 190,13,35200
Mvavc 148,196,84,208
EX 123
Mvav 148,200,88
EX 124
Mvavc 152,196,100,212
EX 125
Mvav 152,200,104
WL 6
EX 126
Mvav 136,204,44
M0 78,98176
TR 192,13,34720
TR 194,13,35232
Mvav 136,208,48
DN 19,36,7,0
Mvav 140,204,60
EX 127
Mvav 140,208,64
EX 128
Mvav 144,204,76
EX 129
Mvav 144,208,80
s_nop 0
TR 196,13,34752
TR 198,13,35264
Mvav 148,204,92
EX 130
Mvav 148,208,96
EX 131
Mvav 152,204,108
M0 78,102400
EX 132
Mvav 152,208,112
WL 6
DN 33,36,7,0
Maav 0,180,156
s_nop 0
TR 200,13,34784
TR 202,13,35296
Maav 4,180,160
EX 133
Maav 8,180,164
EX 134
Maav 12,180,168
EX 135
Maav 16,180,172
EX 136
Maav 20,184,156
s_nop 0
TR 204,13,34816
TR 206,13,35328
Maav 24,184,160
EX 137
Maav 28,184,164
EX 138
Maav 32,184,168
EX 139
Maav 36,184,172
WL 6
EX 140
Maav 40,188,156
s_nop 0
TR 208,13,34848
TR 210,13,35360
Maav 44,188,160
EX 141
Maav 48,188,164
EX 142
Maav 52,188,168
EX 143
Maav 56,188,172
EX 144
Maav 60,192,156
EX 145
Maav 64,192,160
EX 146
Maav 68,192,164
EX 147
Maav 72,192,168
EX 148
Maav 76,192,172
WL 4
EX 149
Maav 80,196,156
s_memrealtime s[94:95]
s_waitcnt vmcnt(16) lgkmcnt(0)
s_barrier
s_sub_u32 s95,s94,s96
s_cmp_lt_i32 s95,0
s_cbranch_scc1 LB_pc58
LB_pb58:
s_add_u32 s96,s94,115
Maav 84,196,160
s_add_u32 s6,s80,57344
RA 180,11,51936
CV 176,116,117
Maav 88,196,164
s_and_b32 s6,s6,0x1e000
RA 184,11,52000
CV 177,118,119
Maav 92,196,168
s_or_b32 s6,s6,0x40000
EX 150
Maav 96,196,172
s_xor_b32 s7,s6,0x30000
EX 151
Maav 100,200,156
M0 78,107040
RA 188,11,52064
CV 180,120,121
Maav 104,200,160
DN 19,32,6,0
Maav 108,200,164
EX 152
Maav 112,200,168
EX 153
Maav 116,200,172
EX 154
Maav 120,204,156
RA 192,11,52128
CV 181,122,123
Maav 124,204,160
EX 155
Maav 128,204,164
CV 184,124,125
CV 185,126,127
Maav 132,204,168
M0 78,111264
CV 188,128,129
CV 189,130,131
Maav 136,204,172
DN 33,32,6,0
Maav 140,208,156
RA 196,11,52448
CV 192,132,133
Maav 144,208,160
CV 193,134,135
CV 178,136,137
Maav 148,208,164
CV 179,138,139
CV 182,140,141
Maav 152,208,168
CV 183,142,143
CV 186,144,145
Maav 156,208,172
CV 187,146,147
CV 190,148,149
Maav 160,212,156
CV 191,150,151
CV 194,152,153
Maav 164,212,160
M0 78,115488
CV 195,154,155
Maav 168,212,164
DN 19,32,7,0
Maav 172,212,168
Maav 176,212,172
WL 3
Mvavc 116,180,36,196
RA 200,11,52512
Mvav 116,184,40
Mvavc 120,180,52,200
Mvav 120,184,56
Mvavc 124,180,68,204
M0 78,119712
Mvav 124,184,72
RA 204,11,52576
Mvavc 128,180,84,208
DN 33,32,7,0
Mvav 128,184,88
Mvavc 132,180,100,212
Mvav 132,184,104
WL 3
Mvav 116,188,44
RA 208,11,52640
Mvav 116,192,48
Mvav 120,188,60
Mvav 120,192,64
M0 78,124352
Mvav 124,188,76
DN 19,36,4,0
Mvav 124,192,80
s_nop 0
TR 180,12,0
TR 182,12,512
Mvav 128,188,92
EX 116
Mvav 128,192,96
EX 117
Mvav 132,188,108
EX 118
Mvav 132,192,112
WL 4
EX 119
Mvavc 136,196,36,196
s_nop 0
TR 184,12,32
TR 186,12,544
Mvav 136,200,40
M0 78,128576
EX 120
Mvavc 140,196,52,200
DN 33,36,4,0
Mvav 140,200,56
EX 121
Mvavc 144,196,68,204
EX 122
Mvav 144,200,72
s_nop 0
TR 188,12,64
TR 190,12,576
Mvavc 148,196,84,208
s_add_u32 s98,s71,90112
v_lshl_add_u32 v8,v2,1,s98
CV 156,116,117
Mvav 148,200,88
buffer_load_dword v8,v8,s[60:63],0 offen
Mvavc 152,196,100,212
EX 123
Mvav 152,200,104
WL 6
EX 124
Mvav 136,204,44
M0 78,132800
TR 192,12,96
TR 194,12,608
Mvav 136,208,48
DN 19,36,5,0
Mvav 140,204,60
EX 125
Mvav 140,208,64
EX 126
Mvav 144,204,76
EX 127
Mvav 144,208,80
s_nop 0
TR 196,12,128
TR 198,12,640
Mvav 148,204,92
EX 128
Mvav 148,208,96
EX 129
Mvav 152,204,108
M0 78,137024
EX 130
Mvav 152,208,112
WL 6
DN 33,36,5,0
Maav 0,180,176
s_nop 0
TR 200,12,160
TR 202,12,672
Maav 4,180,180
EX 131
Maav 8,180,184
EX 132
Maav 12,180,188
EX 133
Maav 16,180,192
EX 134
Maav 20,184,176
s_nop 0
TR 204,12,192
TR 206,12,704
Maav 24,184,180
EX 135
Maav 28,184,184
EX 136
Maav 32,184,188
EX 137
Maav 36,184,192
WL 6
EX 138
Maav 40,188,176
s_nop 0
TR 208,12,224
TR 210,12,736
Maav 44,188,180
EX 139
Maav 48,188,184
EX 140
Maav 52,188,188
EX 141
Maav 56,188,192
EX 142
Maav 60,192,176
EX 143
Maav 64,192,180
EX 144
Maav 68,192,184
EX 145
Maav 72,192,188
EX 146
Maav 76,192,192
WL 4
EX 147
Maav 80,196,176
s_memrealtime s[94:95]
s_waitcnt vmcnt(17) lgkmcnt(0)
s_barrier
s_sub_u32 s95,s94,s96
s_cmp_lt_i32 s95,0
s_cbranch_scc1 LB_pc59
LB_pb59:
s_add_u32 s96,s94,115
Maav 84,196,180
s_add_u32 s4,s80,65536
RA 180,10,17312
CV 157,118,119
Maav 88,196,184
s_and_b32 s4,s4,0x1e000
RA 184,10,17376
CV 160,120,121
Maav 92,196,188
s_or_b32 s4,s4,0x40000
EX 148
Maav 96,196,192
s_xor_b32 s5,s4,0x30000
EX 149
Maav 100,200,176
M0 78,141664
RA 188,10,17440
CV 161,122,123
Maav 104,200,180
DN 19,32,4,0
Maav 108,200,184
EX 150
Maav 112,200,188
EX 151
Maav 116,200,192
EX 152
Maav 120,204,176
RA 192,10,17504
CV 164,124,125
Maav 124,204,180
EX 153
Maav 128,204,184
EX 154
Maav 132,204,188
M0 78,145888
EX 155
Maav 136,204,192
DN 33,32,4,0
Maav 140,208,176
RA 196,10,17824
CV 165,126,127
Maav 144,208,180
CV 168,128,129
CV 169,130,131
Maav 148,208,184
CV 172,132,133
CV 173,134,135
Maav 152,208,188
CV 158,136,137
CV 159,138,139
Maav 156,208,192
CV 162,140,141
CV 163,142,143
Maav 160,212,176
CV 166,144,145
CV 167,146,147
Maav 164,212,180
M0 78,150112
CV 170,148,149
CV 171,150,151
Maav 168,212,184
DN 19,32,5,0
Maav 172,212,188
CV 174,152,153
CV 175,154,155
Maav 176,212,192
WL 3
Mvavc 116,180,36,196
RA 200,10,17888
Mvav 116,184,40
Mvavc 120,180,52,200
Mvav 120,184,56
Mvavc 124,180,68,204
M0 78,154336
Mvav 124,184,72
RA 204,10,17952
Mvavc 128,180,84,208
DN 33,32,5,0
Mvav 128,184,88
Mvavc 132,180,100,212
Mvav 132,184,104
WL 3
Mvav 116,188,44
RA 208,10,18016
Mvav 116,192,48
Mvav 120,188,60
Mvav 120,192,64
M0 78,20480
Mvav 124,188,76
DN 19,36,6,0
Mvav 124,192,80
s_nop 0
TR 180,12,34624
TR 182,12,35136
Mvav 128,188,92
EX 116
Mvav 128,192,96
EX 117
Mvav 132,188,108
EX 118
Mvav 132,192,112
WL 4
EX 119
Mvavc 136,196,36,196
s_nop 0
TR 184,12,34656
TR 186,12,35168
Mvav 136,200,40
M0 78,24704
EX 120
Mvavc 140,196,52,200
DN 33,36,6,0
Mvav 140,200,56
EX 121
Mvavc 144,196,68,204
EX 122
Mvav 144,200,72
s_nop 0
TR 188,12,34688
TR 190,12,35200
Mvavc 148,196,84,208
EX 123
Mvav 148,200,88
EX 124
Mvavc 152,196,100,212
EX 125
Mvav 152,200,104
WL 6
EX 126
Mvav 136,204,44
M0 78,28928
TR 192,12,34720
TR 194,12,35232
Mvav 136,208,48
DN 19,36,7,0
Mvav 140,204,60
EX 127
Mvav 140,208,64
EX 128
Mvav 144,204,76
EX 129
Mvav 144,208,80
s_nop 0
TR 196,12,34752
TR 198,12,35264
Mvav 148,204,92
EX 130
Mvav 148,208,96
EX 131
Mvav 152,204,108
M0 78,33152
EX 132
Mvav 152,208,112
WL 6
DN 33,36,7,0
Maav 0,180,156
s_nop 0
TR 200,12,34784
TR 202,12,35296
Maav 4,180,160
EX 133
Maav 8,180,164
EX 134
Maav 12,180,168
EX 135
Maav 16,180,172
EX 136
Maav 20,184,156
s_nop 0
TR 204,12,34816
TR 206,12,35328
Maav 24,184,160
EX 137
Maav 28,184,164
EX 138
Maav 32,184,168
EX 139
Maav 36,184,172
WL 6
EX 140
Maav 40,188,156
s_nop 0
TR 208,12,34848
TR 210,12,35360
Maav 44,188,160
EX 141
Maav 48,188,164
EX 142
Maav 52,188,168
EX 143
Maav 56,188,172
EX 144
Maav 60,192,156
EX 145
Maav 64,192,160
EX 146
Maav 68,192,164
EX 147
Maav 72,192,168
EX 148
Maav 76,192,172
WL 4
EX 149
Maav 80,196,156
s_memrealtime s[94:95]
s_waitcnt vmcnt(17) lgkmcnt(0)
s_barrier
s_sub_u32 s95,s94,s96
s_cmp_lt_i32 s95,0
s_cbranch_scc1 LB_pc60
LB_pb60:
s_add_u32 s96,s94,115
Maav 84,196,160
s_add_u32 s6,s80,73728
RA 180,10,51936
CV 176,116,117
Maav 88,196,164
s_and_b32 s6,s6,0x1e000
RA 184,10,52000
CV 177,118,119
Maav 92,196,168
s_or_b32 s6,s6,0x40000
EX 150
Maav 96,196,172
s_xor_b32 s7,s6,0x30000
EX 151
Maav 100,200,156
M0 78,37792
RA 188,10,52064
CV 180,120,121
Maav 104,200,160
DN 19,32,6,0
Maav 108,200,164
EX 152
Maav 112,200,168
EX 153
Maav 116,200,172
EX 154
Maav 120,204,156
RA 192,10,52128
CV 181,122,123
Maav 124,204,160
EX 155
Maav 128,204,164
CV 184,124,125
CV 185,126,127
Maav 132,204,168
M0 78,42016
CV 188,128,129
CV 189,130,131
Maav 136,204,172
DN 33,32,6,0
Maav 140,208,156
RA 196,10,52448
CV 192,132,133
Maav 144,208,160
CV 193,134,135
CV 178,136,137
Maav 148,208,164
CV 179,138,139
CV 182,140,141
Maav 152,208,168
CV 183,142,143
CV 186,144,145
Maav 156,208,172
CV 187,146,147
CV 190,148,149
Maav 160,212,156
CV 191,150,151
CV 194,152,153
Maav 164,212,160
M0 78,46240
CV 195,154,155
Maav 168,212,164
DN 19,32,7,0
Maav 172,212,168
Maav 176,212,172
WL 3
Mvavc 116,180,36,196
RA 200,10,52512
Mvav 116,184,40
Mvavc 120,180,52,200
Mvav 120,184,56
Mvavc 124,180,68,204
M0 78,50464
Mvav 124,184,72
RA 204,10,52576
Mvavc 128,180,84,208
DN 33,32,7,0
Mvav 128,184,88
Mvavc 132,180,100,212
Mvav 132,184,104
WL 3
Mvav 116,188,44
RA 208,10,52640
Mvav 116,192,48
Mvav 120,188,60
Mvav 120,192,64
M0 78,55104
Mvav 124,188,76
DN 19,36,4,0
Mvav 124,192,80
s_nop 0
TR 180,13,0
TR 182,13,512
Mvav 128,188,92
EX 116
Mvav 128,192,96
EX 117
Mvav 132,188,108
EX 118
Mvav 132,192,112
WL 4
EX 119
Mvavc 136,196,36,196
s_nop 0
TR 184,13,32
TR 186,13,544
Mvav 136,200,40
M0 78,59328
EX 120
Mvavc 140,196,52,200
DN 33,36,4,0
Mvav 140,200,56
EX 121
Mvavc 144,196,68,204
EX 122
Mvav 144,200,72
s_nop 0
TR 188,13,64
TR 190,13,576
Mvavc 148,196,84,208
EX 123
Mvav 148,200,88
EX 124
Mvavc 152,196,100,212
EX 125
Mvav 152,200,104
WL 6
EX 126
Mvav 136,204,44
M0 78,63552
TR 192,13,96
TR 194,13,608
Mvav 136,208,48
DN 19,36,5,0
Mvav 140,204,60
EX 127
Mvav 140,208,64
EX 128
Mvav 144,204,76
EX 129
Mvav 144,208,80
s_nop 0
TR 196,13,128
TR 198,13,640
Mvav 148,204,92
EX 130
Mvav 148,208,96
EX 131
Mvav 152,204,108
M0 78,67776
EX 132
Mvav 152,208,112
WL 6
DN 33,36,5,0
Maav 0,180,176
s_nop 0
TR 200,13,160
TR 202,13,672
Maav 4,180,180
EX 133
Maav 8,180,184
EX 134
Maav 12,180,188
EX 135
Maav 16,180,192
EX 136
Maav 20,184,176
s_nop 0
TR 204,13,192
TR 206,13,704
Maav 24,184,180
EX 137
Maav 28,184,184
EX 138
Maav 32,184,188
EX 139
Maav 36,184,192
WL 6
EX 140
Maav 40,188,176
s_nop 0
TR 208,13,224
TR 210,13,736
Maav 44,188,180
EX 141
Maav 48,188,184
EX 142
Maav 52,188,188
EX 143
Maav 56,188,192
EX 144
Maav 60,192,176
EX 145
Maav 64,192,180
EX 146
Maav 68,192,184
EX 147
Maav 72,192,188
EX 148
Maav 76,192,192
WL 4
EX 149
Maav 80,196,176
s_memrealtime s[94:95]
s_waitcnt vmcnt(16) lgkmcnt(0)
s_barrier
s_sub_u32 s95,s94,s96
s_cmp_lt_i32 s95,0
s_cbranch_scc1 LB_pc61
LB_pb61:
s_add_u32 s96,s94,115
Maav 84,196,180
s_add_u32 s4,s80,81920
RA 180,11,17312
CV 156,116,117
Maav 88,196,184
s_and_b32 s4,s4,0x1e000
RA 184,11,17376
CV 157,118,119
Maav 92,196,188
s_or_b32 s4,s4,0x40000
EX 150
Maav 96,196,192
s_xor_b32 s5,s4,0x30000
EX 151
Maav 100,200,176
M0 78,72416
RA 188,11,17440
CV 160,120,121
Maav 104,200,180
DN 19,32,4,0
Maav 108,200,184
EX 152
Maav 112,200,188
EX 153
Maav 116,200,192
EX 154
Maav 120,204,176
RA 192,11,17504
CV 161,122,123
Maav 124,204,180
EX 155
Maav 128,204,184
CV 164,124,125
CV 165,126,127
Maav 132,204,188
M0 78,76640
CV 168,128,129
CV 169,130,131
Maav 136,204,192
DN 33,32,4,0
Maav 140,208,176
RA 196,11,17824
CV 172,132,133
Maav 144,208,180
CV 173,134,135
CV 158,136,137
Maav 148,208,184
CV 159,138,139
CV 162,140,141
Maav 152,208,188
CV 163,142,143
CV 166,144,145
Maav 156,208,192
CV 167,146,147
CV 170,148,149
Maav 160,212,176
CV 171,150,151
CV 174,152,153
Maav 164,212,180
M0 78,80864
CV 175,154,155
Maav 168,212,184
DN 19,32,5,0
Maav 172,212,188
Maav 176,212,192
WL 3
Mvavc 116,180,36,196
RA 200,11,17888
Mvav 116,184,40
Mvavc 120,180,52,200
Mvav 120,184,56
Mvavc 124,180,68,204
M0 78,85088
Mvav 124,184,72
RA 204,11,17952
Mvavc 128,180,84,208
DN 33,32,5,0
Mvav 128,184,88
Mvavc 132,180,100,212
Mvav 132,184,104
WL 3
Mvav 116,188,44
RA 208,11,18016
Mvav 116,192,48
Mvav 120,188,60
Mvav 120,192,64
M0 78,89728
Mvav 124,188,76
DN 19,36,6,0
Mvav 124,192,80
s_nop 0
TR 180,13,34624
TR 182,13,35136
Mvav 128,188,92
EX 116
Mvav 128,192,96
EX 117
Mvav 132,188,108
EX 118
Mvav 132,192,112
WL 4
EX 119
Mvavc 136,196,36,196
s_nop 0
TR 184,13,34656
TR 186,13,35168
Mvav 136,200,40
M0 78,93952
EX 120
Mvavc 140,196,52,200
DN 33,36,6,0
Mvav 140,200,56
EX 121
Mvavc 144,196,68,204
EX 122
Mvav 144,200,72
s_nop 0
TR 188,13,34688
TR 190,13,35200
Mvavc 148,196,84,208
EX 123
Mvav 148,200,88
EX 124
Mvavc 152,196,100,212
EX 125
Mvav 152,200,104
WL 6
EX 126
Mvav 136,204,44
M0 78,98176
TR 192,13,34720
TR 194,13,35232
Mvav 136,208,48
DN 19,36,7,0
Mvav 140,204,60
EX 127
Mvav 140,208,64
EX 128
Mvav 144,204,76
EX 129
Mvav 144,208,80
s_nop 0
TR 196,13,34752
TR 198,13,35264
Mvav 148,204,92
EX 130
Mvav 148,208,96
EX 131
Mvav 152,204,108
M0 78,102400
EX 132
Mvav 152,208,112
WL 6
DN 33,36,7,0
Maav 0,180,156
s_nop 0
TR 200,13,34784
TR 202,13,35296
Maav 4,180,160
EX 133
Maav 8,180,164
EX 134
Maav 12,180,168
EX 135
Maav 16,180,172
EX 136
Maav 20,184,156
s_nop 0
TR 204,13,34816
TR 206,13,35328
Maav 24,184,160
EX 137
Maav 28,184,164
EX 138
Maav 32,184,168
EX 139
Maav 36,184,172
WL 6
EX 140
Maav 40,188,156
s_nop 0
TR 208,13,34848
TR 210,13,35360
Maav 44,188,160
EX 141
Maav 48,188,164
EX 142
Maav 52,188,168
EX 143
Maav 56,188,172
EX 144
Maav 60,192,156
EX 145
Maav 64,192,160
EX 146
Maav 68,192,164
EX 147
Maav 72,192,168
EX 148
Maav 76,192,172
WL 4
EX 149
Maav 80,196,156
s_memrealtime s[94:95]
s_waitcnt vmcnt(16) lgkmcnt(0)
s_barrier
s_sub_u32 s95,s94,s96
s_cmp_lt_i32 s95,0
s_cbranch_scc1 LB_pc62
LB_pb62:
s_add_u32 s96,s94,115
Maav 84,196,160
s_add_u32 s6,s80,90112
RA 180,11,51936
CV 176,116,117
Maav 88,196,164
s_and_b32 s6,s6,0x1e000
RA 184,11,52000
CV 177,118,119
Maav 92,196,168
s_or_b32 s6,s6,0x40000
EX 150
Maav 96,196,172
s_xor_b32 s7,s6,0x30000
EX 151
Maav 100,200,156
M0 78,107040
RA 188,11,52064
CV 180,120,121
Maav 104,200,160
DN 19,32,6,0
Maav 108,200,164
EX 152
Maav 112,200,168
EX 153
Maav 116,200,172
EX 154
Maav 120,204,156
RA 192,11,52128
CV 181,122,123
Maav 124,204,160
EX 155
Maav 128,204,164
CV 184,124,125
CV 185,126,127
Maav 132,204,168
M0 78,111264
CV 188,128,129
CV 189,130,131
Maav 136,204,172
DN 33,32,6,0
Maav 140,208,156
RA 196,11,52448
CV 192,132,133
Maav 144,208,160
CV 193,134,135
CV 178,136,137
Maav 148,208,164
CV 179,138,139
CV 182,140,141
Maav 152,208,168
CV 183,142,143
CV 186,144,145
Maav 156,208,172
CV 187,146,147
CV 190,148,149
Maav 160,212,156
CV 191,150,151
CV 194,152,153
Maav 164,212,160
M0 78,115488
CV 195,154,155
Maav 168,212,164
DN 19,32,7,0
Maav 172,212,168
Maav 176,212,172
WL 3
Mvavc 116,180,36,196
RA 200,11,52512
Mvav 116,184,40
Mvavc 120,180,52,200
Mvav 120,184,56
Mvavc 124,180,68,204
M0 78,119712
Mvav 124,184,72
RA 204,11,52576
Mvavc 128,180,84,208
DN 33,32,7,0
Mvav 128,184,88
Mvavc 132,180,100,212
Mvav 132,184,104
WL 3
Mvav 116,188,44
RA 208,11,52640
Mvav 116,192,48
Mvav 120,188,60
Mvav 120,192,64
M0 78,124352
Mvav 124,188,76
DN 19,36,4,0
Mvav 124,192,80
s_nop 0
TR 180,12,0
TR 182,12,512
Mvav 128,188,92
EX 116
Mvav 128,192,96
EX 117
Mvav 132,188,108
EX 118
Mvav 132,192,112
WL 4
EX 119
Mvavc 136,196,36,196
s_nop 0
TR 184,12,32
TR 186,12,544
Mvav 136,200,40
M0 78,128576
EX 120
Mvavc 140,196,52,200
DN 33,36,4,0
Mvav 140,200,56
EX 121
Mvavc 144,196,68,204
EX 122
Mvav 144,200,72
s_nop 0
TR 188,12,64
TR 190,12,576
Mvavc 148,196,84,208
s_add_u32 s98,s71,98304
v_lshl_add_u32 v8,v2,1,s98
CV 156,116,117
Mvav 148,200,88
buffer_load_dword v8,v8,s[60:63],0 offen
Mvavc 152,196,100,212
EX 123
Mvav 152,200,104
WL 6
EX 124
Mvav 136,204,44
M0 78,132800
TR 192,12,96
TR 194,12,608
Mvav 136,208,48
DN 19,36,5,0
Mvav 140,204,60
EX 125
Mvav 140,208,64
EX 126
Mvav 144,204,76
EX 127
Mvav 144,208,80
s_nop 0
TR 196,12,128
TR 198,12,640
Mvav 148,204,92
EX 128
Mvav 148,208,96
EX 129
Mvav 152,204,108
M0 78,137024
EX 130
Mvav 152,208,112
WL 6
DN 33,36,5,0
Maav 0,180,176
s_nop 0
TR 200,12,160
TR 202,12,672
Maav 4,180,180
EX 131
Maav 8,180,184
EX 132
Maav 12,180,188
EX 133
Maav 16,180,192
EX 134
Maav 20,184,176
s_nop 0
TR 204,12,192
TR 206,12,704
Maav 24,184,180
EX 135
Maav 28,184,184
EX 136
Maav 32,184,188
EX 137
Maav 36,184,192
WL 6
EX 138
Maav 40,188,176
s_nop 0
TR 208,12,224
TR 210,12,736
Maav 44,188,180
EX 139
Maav 48,188,184
EX 140
Maav 52,188,188
EX 141
Maav 56,188,192
EX 142
Maav 60,192,176
EX 143
Maav 64,192,180
EX 144
Maav 68,192,184
EX 145
Maav 72,192,188
EX 146
Maav 76,192,192
WL 4
EX 147
Maav 80,196,176
s_memrealtime s[94:95]
s_waitcnt vmcnt(17) lgkmcnt(0)
s_barrier
s_sub_u32 s95,s94,s96
s_cmp_lt_i32 s95,0
s_cbranch_scc1 LB_pc63
LB_pb63:
s_add_u32 s96,s94,115
Maav 84,196,180
s_add_u32 s4,s80,98304
RA 180,10,17312
CV 157,118,119
Maav 88,196,184
s_and_b32 s4,s4,0x1e000
RA 184,10,17376
CV 160,120,121
Maav 92,196,188
s_or_b32 s4,s4,0x40000
EX 148
Maav 96,196,192
s_xor_b32 s5,s4,0x30000
EX 149
Maav 100,200,176
M0 78,141664
RA 188,10,17440
CV 161,122,123
Maav 104,200,180
DN 19,32,4,0
Maav 108,200,184
EX 150
Maav 112,200,188
EX 151
Maav 116,200,192
EX 152
Maav 120,204,176
RA 192,10,17504
CV 164,124,125
Maav 124,204,180
EX 153
Maav 128,204,184
EX 154
Maav 132,204,188
M0 78,145888
EX 155
Maav 136,204,192
DN 33,32,4,0
Maav 140,208,176
RA 196,10,17824
CV 165,126,127
Maav 144,208,180
CV 168,128,129
CV 169,130,131
Maav 148,208,184
CV 172,132,133
CV 173,134,135
Maav 152,208,188
CV 158,136,137
CV 159,138,139
Maav 156,208,192
CV 162,140,141
CV 163,142,143
Maav 160,212,176
CV 166,144,145
CV 167,146,147
Maav 164,212,180
M0 78,150112
CV 170,148,149
CV 171,150,151
Maav 168,212,184
DN 19,32,5,0
Maav 172,212,188
CV 174,152,153
CV 175,154,155
Maav 176,212,192
WL 3
Mvavc 116,180,36,196
RA 200,10,17888
Mvav 116,184,40
Mvavc 120,180,52,200
Mvav 120,184,56
Mvavc 124,180,68,204
M0 78,154336
Mvav 124,184,72
RA 204,10,17952
Mvavc 128,180,84,208
DN 33,32,5,0
Mvav 128,184,88
Mvavc 132,180,100,212
Mvav 132,184,104
WL 3
Mvav 116,188,44
RA 208,10,18016
Mvav 116,192,48
Mvav 120,188,60
Mvav 120,192,64
M0 78,20480
Mvav 124,188,76
DN 19,36,6,0
Mvav 124,192,80
s_nop 0
TR 180,12,34624
TR 182,12,35136
Mvav 128,188,92
EX 116
Mvav 128,192,96
EX 117
Mvav 132,188,108
EX 118
Mvav 132,192,112
WL 4
EX 119
Mvavc 136,196,36,196
s_nop 0
TR 184,12,34656
TR 186,12,35168
Mvav 136,200,40
M0 78,24704
EX 120
Mvavc 140,196,52,200
DN 33,36,6,0
Mvav 140,200,56
EX 121
Mvavc 144,196,68,204
EX 122
Mvav 144,200,72
s_nop 0
TR 188,12,34688
TR 190,12,35200
Mvavc 148,196,84,208
EX 123
Mvav 148,200,88
EX 124
Mvavc 152,196,100,212
EX 125
Mvav 152,200,104
WL 6
EX 126
Mvav 136,204,44
M0 78,28928
TR 192,12,34720
TR 194,12,35232
Mvav 136,208,48
DN 19,36,7,0
Mvav 140,204,60
EX 127
Mvav 140,208,64
EX 128
Mvav 144,204,76
EX 129
Mvav 144,208,80
s_nop 0
TR 196,12,34752
TR 198,12,35264
Mvav 148,204,92
EX 130
Mvav 148,208,96
EX 131
Mvav 152,204,108
M0 78,33152
EX 132
Mvav 152,208,112
WL 6
DN 33,36,7,0
Maav 0,180,156
s_nop 0
TR 200,12,34784
TR 202,12,35296
Maav 4,180,160
EX 133
Maav 8,180,164
EX 134
Maav 12,180,168
EX 135
Maav 16,180,172
EX 136
Maav 20,184,156
s_nop 0
TR 204,12,34816
TR 206,12,35328
Maav 24,184,160
EX 137
Maav 28,184,164
EX 138
Maav 32,184,168
EX 139
Maav 36,184,172
WL 6
EX 140
Maav 40,188,156
s_nop 0
TR 208,12,34848
TR 210,12,35360
Maav 44,188,160
EX 141
Maav 48,188,164
EX 142
Maav 52,188,168
EX 143
Maav 56,188,172
EX 144
Maav 60,192,156
EX 145
Maav 64,192,160
EX 146
Maav 68,192,164
EX 147
Maav 72,192,168
EX 148
Maav 76,192,172
WL 4
EX 149
Maav 80,196,156
s_memrealtime s[94:95]
s_waitcnt vmcnt(17) lgkmcnt(0)
s_barrier
s_sub_u32 s95,s94,s96
s_cmp_lt_i32 s95,0
s_cbranch_scc1 LB_pc64
LB_pb64:
s_add_u32 s96,s94,115
Maav 84,196,160
s_add_u32 s6,s80,106496
RA 180,10,51936
CV 176,116,117
Maav 88,196,164
s_and_b32 s6,s6,0x1e000
RA 184,10,52000
CV 177,118,119
Maav 92,196,168
s_or_b32 s6,s6,0x40000
EX 150
Maav 96,196,172
s_xor_b32 s7,s6,0x30000
EX 151
Maav 100,200,156
M0 78,37792
RA 188,10,52064
CV 180,120,121
Maav 104,200,160
DN 19,32,6,0
Maav 108,200,164
EX 152
Maav 112,200,168
EX 153
Maav 116,200,172
EX 154
Maav 120,204,156
RA 192,10,52128
CV 181,122,123
Maav 124,204,160
EX 155
Maav 128,204,164
CV 184,124,125
CV 185,126,127
Maav 132,204,168
M0 78,42016
CV 188,128,129
CV 189,130,131
Maav 136,204,172
DN 33,32,6,0
Maav 140,208,156
RA 196,10,52448
CV 192,132,133
Maav 144,208,160
CV 193,134,135
CV 178,136,137
Maav 148,208,164
CV 179,138,139
CV 182,140,141
Maav 152,208,168
CV 183,142,143
CV 186,144,145
Maav 156,208,172
CV 187,146,147
CV 190,148,149
Maav 160,212,156
CV 191,150,151
CV 194,152,153
Maav 164,212,160
M0 78,46240
CV 195,154,155
Maav 168,212,164
DN 19,32,7,0
Maav 172,212,168
Maav 176,212,172
WL 3
Mvavc 116,180,36,196
RA 200,10,52512
Mvav 116,184,40
Mvavc 120,180,52,200
Mvav 120,184,56
Mvavc 124,180,68,204
M0 78,50464
Mvav 124,184,72
RA 204,10,52576
Mvavc 128,180,84,208
DN 33,32,7,0
Mvav 128,184,88
Mvavc 132,180,100,212
Mvav 132,184,104
WL 3
Mvav 116,188,44
RA 208,10,52640
Mvav 116,192,48
Mvav 120,188,60
Mvav 120,192,64
M0 78,55104
Mvav 124,188,76
DN 19,36,4,0
Mvav 124,192,80
s_nop 0
TR 180,13,0
TR 182,13,512
Mvav 128,188,92
EX 116
Mvav 128,192,96
EX 117
Mvav 132,188,108
EX 118
Mvav 132,192,112
WL 4
EX 119
Mvavc 136,196,36,196
s_nop 0
TR 184,13,32
TR 186,13,544
Mvav 136,200,40
M0 78,59328
EX 120
Mvavc 140,196,52,200
DN 33,36,4,0
Mvav 140,200,56
EX 121
Mvavc 144,196,68,204
EX 122
Mvav 144,200,72
s_nop 0
TR 188,13,64
TR 190,13,576
Mvavc 148,196,84,208
EX 123
Mvav 148,200,88
EX 124
Mvavc 152,196,100,212
EX 125
Mvav 152,200,104
WL 6
EX 126
Mvav 136,204,44
M0 78,63552
TR 192,13,96
TR 194,13,608
Mvav 136,208,48
DN 19,36,5,0
Mvav 140,204,60
EX 127
Mvav 140,208,64
EX 128
Mvav 144,204,76
EX 129
Mvav 144,208,80
s_nop 0
TR 196,13,128
TR 198,13,640
Mvav 148,204,92
EX 130
Mvav 148,208,96
EX 131
Mvav 152,204,108
M0 78,67776
EX 132
Mvav 152,208,112
WL 6
DN 33,36,5,0
Maav 0,180,176
s_nop 0
TR 200,13,160
TR 202,13,672
Maav 4,180,180
EX 133
Maav 8,180,184
EX 134
Maav 12,180,188
EX 135
Maav 16,180,192
EX 136
Maav 20,184,176
s_nop 0
TR 204,13,192
TR 206,13,704
Maav 24,184,180
EX 137
Maav 28,184,184
EX 138
Maav 32,184,188
EX 139
Maav 36,184,192
WL 6
EX 140
Maav 40,188,176
s_nop 0
TR 208,13,224
TR 210,13,736
Maav 44,188,180
EX 141
Maav 48,188,184
EX 142
Maav 52,188,188
EX 143
Maav 56,188,192
EX 144
Maav 60,192,176
EX 145
Maav 64,192,180
EX 146
Maav 68,192,184
EX 147
Maav 72,192,188
EX 148
Maav 76,192,192
WL 4
EX 149
Maav 80,196,176
s_memrealtime s[94:95]
s_waitcnt vmcnt(16) lgkmcnt(0)
s_barrier
s_sub_u32 s95,s94,s96
s_cmp_lt_i32 s95,0
s_cbranch_scc1 LB_pc65
LB_pb65:
s_add_u32 s96,s94,115
Maav 84,196,180
s_add_u32 s4,s80,114688
RA 180,11,17312
CV 156,116,117
Maav 88,196,184
s_and_b32 s4,s4,0x1e000
RA 184,11,17376
CV 157,118,119
Maav 92,196,188
s_or_b32 s4,s4,0x40000
EX 150
Maav 96,196,192
s_xor_b32 s5,s4,0x30000
EX 151
Maav 100,200,176
M0 78,72416
RA 188,11,17440
CV 160,120,121
Maav 104,200,180
DN 19,32,4,0
Maav 108,200,184
EX 152
Maav 112,200,188
EX 153
Maav 116,200,192
EX 154
Maav 120,204,176
RA 192,11,17504
CV 161,122,123
Maav 124,204,180
EX 155
Maav 128,204,184
CV 164,124,125
CV 165,126,127
Maav 132,204,188
M0 78,76640
CV 168,128,129
CV 169,130,131
Maav 136,204,192
DN 33,32,4,0
Maav 140,208,176
RA 196,11,17824
CV 172,132,133
Maav 144,208,180
CV 173,134,135
CV 158,136,137
Maav 148,208,184
CV 159,138,139
CV 162,140,141
Maav 152,208,188
CV 163,142,143
CV 166,144,145
Maav 156,208,192
CV 167,146,147
CV 170,148,149
Maav 160,212,176
CV 171,150,151
CV 174,152,153
Maav 164,212,180
M0 78,80864
CV 175,154,155
Maav 168,212,184
DN 19,32,5,0
Maav 172,212,188
Maav 176,212,192
WL 3
Mvavc 116,180,36,196
RA 200,11,17888
Mvav 116,184,40
Mvavc 120,180,52,200
Mvav 120,184,56
Mvavc 124,180,68,204
M0 78,85088
Mvav 124,184,72
RA 204,11,17952
Mvavc 128,180,84,208
DN 33,32,5,0
Mvav 128,184,88
Mvavc 132,180,100,212
Mvav 132,184,104
WL 3
Mvav 116,188,44
RA 208,11,18016
Mvav 116,192,48
Mvav 120,188,60
Mvav 120,192,64
M0 78,89728
Mvav 124,188,76
DN 19,36,6,0
Mvav 124,192,80
s_nop 0
TR 180,13,34624
TR 182,13,35136
Mvav 128,188,92
EX 116
Mvav 128,192,96
EX 117
Mvav 132,188,108
EX 118
Mvav 132,192,112
WL 4
EX 119
Mvavc 136,196,36,196
s_nop 0
TR 184,13,34656
TR 186,13,35168
Mvav 136,200,40
M0 78,93952
EX 120
Mvavc 140,196,52,200
DN 33,36,6,0
Mvav 140,200,56
EX 121
Mvavc 144,196,68,204
EX 122
Mvav 144,200,72
s_nop 0
TR 188,13,34688
TR 190,13,35200
Mvavc 148,196,84,208
EX 123
Mvav 148,200,88
EX 124
Mvavc 152,196,100,212
EX 125
Mvav 152,200,104
WL 6
EX 126
Mvav 136,204,44
M0 78,98176
TR 192,13,34720
TR 194,13,35232
Mvav 136,208,48
DN 19,36,7,0
Mvav 140,204,60
EX 127
Mvav 140,208,64
EX 128
Mvav 144,204,76
EX 129
Mvav 144,208,80
s_nop 0
TR 196,13,34752
TR 198,13,35264
Mvav 148,204,92
EX 130
Mvav 148,208,96
EX 131
Mvav 152,204,108
M0 78,102400
EX 132
Mvav 152,208,112
WL 6
DN 33,36,7,0
Maav 0,180,156
s_nop 0
TR 200,13,34784
TR 202,13,35296
Maav 4,180,160
EX 133
Maav 8,180,164
EX 134
Maav 12,180,168
EX 135
Maav 16,180,172
EX 136
Maav 20,184,156
s_nop 0
TR 204,13,34816
TR 206,13,35328
Maav 24,184,160
EX 137
Maav 28,184,164
EX 138
Maav 32,184,168
EX 139
Maav 36,184,172
WL 6
EX 140
Maav 40,188,156
s_nop 0
TR 208,13,34848
TR 210,13,35360
Maav 44,188,160
EX 141
Maav 48,188,164
EX 142
Maav 52,188,168
EX 143
Maav 56,188,172
EX 144
Maav 60,192,156
EX 145
Maav 64,192,160
EX 146
Maav 68,192,164
EX 147
Maav 72,192,168
EX 148
Maav 76,192,172
WL 4
EX 149
Maav 80,196,156
s_memrealtime s[94:95]
s_waitcnt vmcnt(16) lgkmcnt(0)
s_barrier
s_sub_u32 s95,s94,s96
s_cmp_lt_i32 s95,0
s_cbranch_scc1 LB_pc66
LB_pb66:
s_add_u32 s96,s94,115
Maav 84,196,160
s_add_u32 s6,s80,122880
RA 180,11,51936
CV 176,116,117
Maav 88,196,164
s_and_b32 s6,s6,0x1e000
RA 184,11,52000
CV 177,118,119
Maav 92,196,168
s_or_b32 s6,s6,0x40000
EX 150
Maav 96,196,172
s_xor_b32 s7,s6,0x30000
EX 151
Maav 100,200,156
M0 78,107040
RA 188,11,52064
CV 180,120,121
Maav 104,200,160
DN 19,32,6,0
Maav 108,200,164
EX 152
Maav 112,200,168
EX 153
Maav 116,200,172
EX 154
Maav 120,204,156
RA 192,11,52128
CV 181,122,123
Maav 124,204,160
EX 155
Maav 128,204,164
CV 184,124,125
CV 185,126,127
Maav 132,204,168
M0 78,111264
CV 188,128,129
CV 189,130,131
Maav 136,204,172
DN 33,32,6,0
Maav 140,208,156
RA 196,11,52448
CV 192,132,133
Maav 144,208,160
CV 193,134,135
CV 178,136,137
Maav 148,208,164
CV 179,138,139
CV 182,140,141
Maav 152,208,168
CV 183,142,143
CV 186,144,145
Maav 156,208,172
CV 187,146,147
CV 190,148,149
Maav 160,212,156
CV 191,150,151
CV 194,152,153
Maav 164,212,160
M0 78,115488
CV 195,154,155
Maav 168,212,164
DN 19,32,7,0
Maav 172,212,168
Maav 176,212,172
WL 3
Mvavc 116,180,36,196
RA 200,11,52512
Mvav 116,184,40
Mvavc 120,180,52,200
Mvav 120,184,56
Mvavc 124,180,68,204
M0 78,119712
Mvav 124,184,72
RA 204,11,52576
Mvavc 128,180,84,208
DN 33,32,7,0
Mvav 128,184,88
Mvavc 132,180,100,212
Mvav 132,184,104
WL 3
Mvav 116,188,44
RA 208,11,52640
Mvav 116,192,48
Mvav 120,188,60
Mvav 120,192,64
M0 78,124352
Mvav 124,188,76
DN 19,36,4,0
Mvav 124,192,80
s_nop 0
TR 180,12,0
TR 182,12,512
Mvav 128,188,92
EX 116
Mvav 128,192,96
EX 117
Mvav 132,188,108
EX 118
Mvav 132,192,112
WL 4
EX 119
Mvavc 136,196,36,196
s_nop 0
TR 184,12,32
TR 186,12,544
Mvav 136,200,40
M0 78,128576
EX 120
Mvavc 140,196,52,200
DN 33,36,4,0
Mvav 140,200,56
EX 121
Mvavc 144,196,68,204
EX 122
Mvav 144,200,72
s_nop 0
TR 188,12,64
TR 190,12,576
Mvavc 148,196,84,208
s_add_u32 s98,s71,106496
v_lshl_add_u32 v8,v2,1,s98
CV 156,116,117
Mvav 148,200,88
buffer_load_dword v8,v8,s[60:63],0 offen
Mvavc 152,196,100,212
EX 123
Mvav 152,200,104
WL 6
EX 124
Mvav 136,204,44
M0 78,132800
TR 192,12,96
TR 194,12,608
Mvav 136,208,48
DN 19,36,5,0
Mvav 140,204,60
EX 125
Mvav 140,208,64
EX 126
Mvav 144,204,76
EX 127
Mvav 144,208,80
s_nop 0
TR 196,12,128
TR 198,12,640
Mvav 148,204,92
EX 128
Mvav 148,208,96
EX 129
Mvav 152,204,108
M0 78,137024
EX 130
Mvav 152,208,112
WL 6
DN 33,36,5,0
Maav 0,180,176
s_nop 0
TR 200,12,160
TR 202,12,672
Maav 4,180,180
EX 131
Maav 8,180,184
EX 132
Maav 12,180,188
EX 133
Maav 16,180,192
EX 134
Maav 20,184,176
s_nop 0
TR 204,12,192
TR 206,12,704
Maav 24,184,180
EX 135
Maav 28,184,184
EX 136
Maav 32,184,188
EX 137
Maav 36,184,192
WL 6
EX 138
Maav 40,188,176
s_nop 0
TR 208,12,224
TR 210,12,736
Maav 44,188,180
EX 139
Maav 48,188,184
EX 140
Maav 52,188,188
EX 141
Maav 56,188,192
EX 142
Maav 60,192,176
EX 143
Maav 64,192,180
EX 144
Maav 68,192,184
EX 145
Maav 72,192,188
EX 146
Maav 76,192,192
WL 4
EX 147
Maav 80,196,176
s_memrealtime s[94:95]
s_waitcnt vmcnt(17) lgkmcnt(0)
s_barrier
s_sub_u32 s95,s94,s96
s_cmp_lt_i32 s95,0
s_cbranch_scc1 LB_pc67
LB_pb67:
s_add_u32 s96,s94,115
Maav 84,196,180
M0 78,141664
RA 180,10,17312
CV 157,118,119
Maav 88,196,184
RA 184,10,17376
CV 160,120,121
Maav 92,196,188
DN 19,36,6,0
Maav 96,196,192
EX 148
Maav 100,200,176
RA 188,10,17440
CV 161,122,123
Maav 104,200,180
EX 149
Maav 108,200,184
EX 150
Maav 112,200,188
EX 151
Maav 116,200,192
EX 152
Maav 120,204,176
M0 78,145888
RA 192,10,17504
CV 164,124,125
Maav 124,204,180
DN 33,36,6,0
Maav 128,204,184
EX 153
Maav 132,204,188
EX 154
Maav 136,204,192
EX 155
Maav 140,208,176
RA 196,10,17824
CV 165,126,127
Maav 144,208,180
CV 168,128,129
CV 169,130,131
Maav 148,208,184
CV 172,132,133
CV 173,134,135
Maav 152,208,188
M0 78,150112
CV 158,136,137
CV 159,138,139
Maav 156,208,192
DN 19,36,7,0
Maav 160,212,176
CV 162,140,141
CV 163,142,143
Maav 164,212,180
CV 166,144,145
CV 167,146,147
Maav 168,212,184
CV 170,148,149
CV 171,150,151
Maav 172,212,188
CV 175,154,155
Maav 176,212,192
WL 3
Mvavc 116,180,36,196
RA 200,10,17888
CV 174,152,153
Mvav 116,184,40
M0 78,154336
Mvavc 120,180,52,200
DN 33,36,7,0
Mvav 120,184,56
Mvavc 124,180,68,204
Mvav 124,184,72
RA 204,10,17952
Mvavc 128,180,84,208
Mvav 128,184,88
Mvavc 132,180,100,212
Mvav 132,184,104
WL 3
Mvav 116,188,44
RA 208,10,18016
Mvav 116,192,48
Mvav 120,188,60
Mvav 120,192,64
Mvav 124,188,76
Mvav 124,192,80
s_nop 0
TR 180,12,34624
TR 182,12,35136
Mvav 128,188,92
EX 116
Mvav 128,192,96
EX 117
Mvav 132,188,108
EX 118
Mvav 132,192,112
WL 4
EX 119
Mvavc 136,196,36,196
s_nop 0
TR 184,12,34656
TR 186,12,35168
Mvav 136,200,40
EX 120
Mvavc 140,196,52,200
EX 121
Mvav 140,200,56
EX 122
Mvavc 144,196,68,204
EX 123
Mvav 144,200,72
s_nop 0
TR 188,12,34688
TR 190,12,35200
Mvavc 148,196,84,208
EX 124
Mvav 148,200,88
EX 125
Mvavc 152,196,100,212
EX 126
Mvav 152,200,104
WL 6
EX 127
Mvav 136,204,44
s_nop 0
TR 192,12,34720
TR 194,12,35232
Mvav 136,208,48
EX 128
Mvav 140,204,60
EX 129
Mvav 140,208,64
EX 130
Mvav 144,204,76
EX 131
Mvav 144,208,80
s_nop 0
TR 196,12,34752
TR 198,12,35264
Mvav 148,204,92
EX 132
Mvav 148,208,96
EX 133
Mvav 152,204,108
EX 134
Mvav 152,208,112
WL 6
EX 135
Maav 0,180,156
s_nop 0
TR 200,12,34784
TR 202,12,35296
Maav 4,180,160
EX 136
Maav 8,180,164
EX 137
Maav 12,180,168
EX 138
Maav 16,180,172
EX 139
Maav 20,184,156
s_nop 0
TR 204,12,34816
TR 206,12,35328
Maav 24,184,160
EX 140
Maav 28,184,164
EX 141
Maav 32,184,168
EX 142
Maav 36,184,172
WL 6
EX 143
Maav 40,188,156
s_nop 0
TR 208,12,34848
TR 210,12,35360
Maav 44,188,160
EX 144
Maav 48,188,164
EX 145
Maav 52,188,168
EX 146
Maav 56,188,172
EX 147
Maav 60,192,156
EX 148
Maav 64,192,160
EX 149
Maav 68,192,164
EX 150
Maav 72,192,168
EX 151
Maav 76,192,172
WL 4
EX 152
Maav 80,196,156
s_waitcnt vmcnt(13) lgkmcnt(0)
s_barrier
Maav 84,196,160
RA 180,10,51936
CV 176,116,117
Maav 88,196,164
RA 184,10,52000
CV 177,118,119
Maav 92,196,168
EX 153
Maav 96,196,172
EX 154
Maav 100,200,156
RA 188,10,52064
CV 180,120,121
Maav 104,200,160
EX 155
Maav 108,200,164
CV 181,122,123
CV 184,124,125
Maav 112,200,168
CV 185,126,127
CV 188,128,129
Maav 116,200,172
CV 189,130,131
CV 192,132,133
Maav 120,204,156
RA 192,10,52128
CV 193,134,135
Maav 124,204,160
CV 178,136,137
CV 179,138,139
Maav 128,204,164
CV 182,140,141
CV 183,142,143
Maav 132,204,168
CV 186,144,145
CV 187,146,147
Maav 136,204,172
CV 190,148,149
CV 191,150,151
Maav 140,208,156
RA 196,10,52448
CV 194,152,153
Maav 144,208,160
CV 195,154,155
Maav 148,208,164
Maav 152,208,168
Maav 156,208,172
Maav 160,212,156
Maav 164,212,160
Maav 168,212,164
Maav 172,212,168
Maav 176,212,172
WL 3
Mvavc 116,180,36,196
RA 200,10,52512
Mvav 116,184,40
s_lshr_b32 s91,s22,5
s_mul_i32 s91,s91,73728
s_add_u32 s91,s91,0
s_add_u32 s44,s40,s91
s_addc_u32 s45,s41,0
s_mov_b32 s46,73728
s_mov_b32 s47,0x20000
Mvavc 120,180,52,200
s_lshl_b32 s91,s64,12
v_lshl_add_u32 v28,v2,2,s91
Mvav 120,184,56
buffer_load_dword v29,v28,s[44:47],0 offen
Mvavc 124,180,68,204
v_add_u32 v28,16384,v28
Mvav 124,184,72
RA 204,10,52576
Mvavc 128,180,84,208
Mvav 128,184,88
Mvavc 132,180,100,212
Mvav 132,184,104
WL 3
Mvav 116,188,44
RA 208,10,52640
Mvav 116,192,48
buffer_load_dword v29,v28,s[44:47],0 offen
Mvav 120,188,60
v_add_u32 v28,16384,v28
Mvav 120,192,64
Mvav 124,188,76
Mvav 124,192,80
s_nop 0
TR 180,13,0
TR 182,13,512
Mvav 128,188,92
EX 116
Mvav 128,192,96
EX 117
Mvav 132,188,108
EX 118
Mvav 132,192,112
WL 4
buffer_load_dword v29,v28,s[44:47],0 offen
Mvavc 136,196,36,196
s_nop 0
TR 184,13,32
TR 186,13,544
Mvav 136,200,40
v_add_u32 v28,16384,v28
CV 156,116,117
Mvavc 140,196,52,200
EX 119
Mvav 140,200,56
EX 120
Mvavc 144,196,68,204
EX 121
Mvav 144,200,72
s_nop 0
TR 188,13,64
TR 190,13,576
Mvavc 148,196,84,208
EX 122
Mvav 148,200,88
buffer_load_dword v29,v28,s[44:47],0 offen
Mvavc 152,196,100,212
v_add_u32 v28,16384,v28
CV 157,118,119
Mvav 152,200,104
WL 6
EX 123
Mvav 136,204,44
s_nop 0
TR 192,13,96
TR 194,13,608
Mvav 136,208,48
EX 124
Mvav 140,204,60
EX 125
Mvav 140,208,64
EX 126
Mvav 144,204,76
EX 127
Mvav 144,208,80
s_nop 0
TR 196,13,128
TR 198,13,640
Mvav 148,204,92
buffer_load_dword v29,v28,s[44:47],0 offen
Mvav 148,208,96
v_add_u32 v28,16384,v28
CV 160,120,121
Mvav 152,204,108
EX 128
Mvav 152,208,112
WL 6
EX 129
Maav 0,180,176
s_nop 0
TR 200,13,160
TR 202,13,672
Maav 4,180,180
EX 130
Maav 8,180,184
EX 131
Maav 12,180,188
EX 132
Maav 16,180,192
EX 133
Maav 20,184,176
s_nop 0
TR 204,13,192
TR 206,13,704
Maav 24,184,180
EX 134
Maav 28,184,184
EX 135
Maav 32,184,188
EX 136
Maav 36,184,192
WL 6
EX 137
Maav 40,188,176
s_nop 0
TR 208,13,224
TR 210,13,736
Maav 44,188,180
EX 138
Maav 48,188,184
EX 139
Maav 52,188,188
EX 140
Maav 56,188,192
EX 141
Maav 60,192,176
EX 142
Maav 64,192,180
EX 143
Maav 68,192,184
EX 144
Maav 72,192,188
EX 145
Maav 76,192,192
WL 4
EX 146
Maav 80,196,176
s_waitcnt vmcnt(9) lgkmcnt(0)
s_barrier
Maav 84,196,180
RA 180,11,17312
CV 161,122,123
Maav 88,196,184
RA 184,11,17376
CV 164,124,125
Maav 92,196,188
s_mul_i32 s90,s64,20480
v_add_u32 v34,s90,v2
Maav 96,196,192
s_xor_b32 s90,s64,2
s_mul_i32 s90,s90,20480
v_add_u32 v35,s90,v2
Maav 100,200,176
RA 188,11,17440
CV 165,126,127
Maav 104,200,180
EX 147
Maav 108,200,184
ds_write_b128 v34,v[216:219] offset:0
Maav 112,200,188
EX 148
Maav 116,200,192
EX 149
Maav 120,204,176
RA 192,11,17504
CV 168,128,129
Maav 124,204,180
ds_write_b128 v34,v[220:223] offset:1024
Maav 128,204,184
EX 150
Maav 132,204,188
EX 151
Maav 136,204,192
ds_write_b128 v34,v[224:227] offset:2048
Maav 140,208,176
RA 196,11,17824
CV 169,130,131
Maav 144,208,180
EX 152
Maav 148,208,184
ds_write_b128 v34,v[228:231] offset:3072
Maav 152,208,188
EX 153
Maav 156,208,192
EX 154
Maav 160,212,176
ds_write_b128 v34,v[232:235] offset:4096
Maav 164,212,180
EX 155
Maav 168,212,184
CV 172,132,133
CV 173,134,135
Maav 172,212,188
ds_write_b128 v34,v[236:239] offset:5120
Maav 176,212,192
WL 9
CV 158,136,137
CV 159,138,139
Mvavc 116,180,36,196
RA 200,11,17888
CV 162,140,141
Mvav 116,184,40
ds_write_b128 v34,v[240:243] offset:6144
Mvavc 120,180,52,200
CV 163,142,143
CV 166,144,145
Mvav 120,184,56
CV 167,146,147
CV 170,148,149
Mvavc 124,180,68,204
ds_write_b128 v34,v[244:247] offset:7168
Mvav 124,184,72
RA 204,11,17952
CV 171,150,151
Mvavc 128,180,84,208
CV 174,152,153
CV 175,154,155
Mvav 128,184,88
ds_write_b128 v34,v[248:251] offset:8192
Mvavc 132,180,100,212
Mvav 132,184,104
WL 11
Mvav 116,188,44
RA 208,11,18016
Mvav 116,192,48
ds_write_b128 v34,v[252:255] offset:9216
Mvav 120,188,60
Mvav 120,192,64
Mvav 124,188,76
ds_write_b128 v34,a[216:219] offset:10240
Mvav 124,192,80
s_nop 0
TR 180,13,34624
TR 182,13,35136
Mvav 128,188,92
EX 116
Mvav 128,192,96
ds_write_b128 v34,a[220:223] offset:11264
Mvav 132,188,108
EX 117
Mvav 132,192,112
WL 10
EX 118
Mvavc 136,196,36,196
s_nop 0
TR 184,13,34656
TR 186,13,35168
Mvav 136,200,40
ds_write_b128 v34,a[224:227] offset:12288
Mvavc 140,196,52,200
EX 119
Mvav 140,200,56
EX 120
Mvavc 144,196,68,204
ds_write_b128 v34,a[228:231] offset:13312
Mvav 144,200,72
s_nop 0
TR 188,13,34688
TR 190,13,35200
Mvavc 148,196,84,208
EX 121
Mvav 148,200,88
ds_write_b128 v34,a[232:235] offset:14336
Mvavc 152,196,100,212
EX 122
Mvav 152,200,104
WL 12
EX 123
Mvav 136,204,44
s_nop 0
TR 192,13,34720
TR 194,13,35232
Mvav 136,208,48
ds_write_b128 v34,a[236:239] offset:15360
Mvav 140,204,60
EX 124
Mvav 140,208,64
EX 125
Mvav 144,204,76
ds_write_b128 v34,a[240:243] offset:16384
Mvav 144,208,80
s_nop 0
TR 196,13,34752
TR 198,13,35264
Mvav 148,204,92
EX 126
Mvav 148,208,96
ds_write_b128 v34,a[244:247] offset:17408
Mvav 152,204,108
EX 127
Mvav 152,208,112
WL 12
EX 128
Maav 0,180,156
s_nop 0
TR 200,13,34784
TR 202,13,35296
Maav 4,180,160
ds_write_b128 v34,a[248:251] offset:18432
Maav 8,180,164
EX 129
Maav 12,180,168
EX 130
CV 188,128,129
Maav 16,180,172
ds_write_b128 v34,a[252:255] offset:19456
Maav 20,184,156
s_nop 0
TR 204,13,34816
TR 206,13,35328
Maav 24,184,160
EX 131
Maav 28,184,164
EX 132
CV 189,130,131
Maav 32,184,168
EX 133
Maav 36,184,172
WL 11
EX 134
CV 192,132,133
Maav 40,188,156
s_nop 0
TR 208,13,34848
TR 210,13,35360
Maav 44,188,160
EX 135
Maav 48,188,164
EX 136
CV 193,134,135
Maav 52,188,168
EX 137
Maav 56,188,172
EX 138
CV 178,136,137
Maav 60,192,156
EX 139
Maav 64,192,160
EX 140
CV 179,138,139
Maav 68,192,164
EX 141
Maav 72,192,168
EX 142
CV 182,140,141
Maav 76,192,172
WL 6
EX 143
Maav 80,196,156
s_waitcnt vmcnt(5) lgkmcnt(0)
s_barrier
CV 183,142,143
Maav 84,196,160
s_nop 0
TR 180,13,52064
TR 182,13,52576
Maav 88,196,164
s_nop 0
TR 184,13,52096
TR 186,13,52608
Maav 92,196,168
ds_read_b128 v[36:39],v35 offset:0
EX 144
Maav 96,196,172
ds_read_b128 v[40:43],v35 offset:1024
EX 145
Maav 100,200,156
s_nop 0
TR 188,13,52128
TR 190,13,52640
CV 186,144,145
Maav 104,200,160
ds_read_b128 v[44:47],v35 offset:2048
EX 146
Maav 108,200,164
ds_read_b128 v[48:51],v35 offset:3072
EX 147
Maav 112,200,168
ds_read_b128 v[52:55],v35 offset:4096
EX 148
Maav 116,200,172
ds_read_b128 v[56:59],v35 offset:5120
EX 149
Maav 120,204,156
s_nop 0
TR 192,13,52160
TR 194,13,52672
CV 187,146,147
Maav 124,204,160
ds_read_b128 v[60:63],v35 offset:6144
EX 150
Maav 128,204,164
ds_read_b128 v[64:67],v35 offset:7168
EX 151
Maav 132,204,168
ds_read_b128 v[68:71],v35 offset:8192
EX 152
Maav 136,204,172
ds_read_b128 v[72:75],v35 offset:9216
EX 153
Maav 140,208,156
s_nop 0
TR 196,13,51936
TR 198,13,52448
CV 190,148,149
Maav 144,208,160
ds_read_b128 v[76:79],v35 offset:10240
EX 154
Maav 148,208,164
ds_read_b128 v[80:83],v35 offset:11264
EX 155
Maav 152,208,168
ds_read_b128 v[84:87],v35 offset:12288
CV 176,116,117
CV 191,150,151
Maav 156,208,172
ds_read_b128 v[88:91],v35 offset:13312
CV 177,118,119
CV 194,152,153
Maav 160,212,156
ds_read_b128 v[92:95],v35 offset:14336
CV 180,120,121
CV 195,154,155
Maav 164,212,160
ds_read_b128 v[96:99],v35 offset:15360
CV 181,122,123
Maav 168,212,164
ds_read_b128 v[100:103],v35 offset:16384
CV 184,124,125
Maav 172,212,168
ds_read_b128 v[104:107],v35 offset:17408
CV 185,126,127
Maav 176,212,172
ds_read_b128 v[108:111],v35 offset:18432
Maav 160,212,176
ds_read_b128 v[112:115],v35 offset:19456
Maav 164,212,180
Maav 168,212,184
Maav 172,212,188
Maav 176,212,192
WL 15
Maav 80,180,176
s_nop 0
TR 200,13,51968
TR 202,13,52480
Maav 84,180,180
Maav 88,180,184
Maav 92,180,188
AR 28,160
s_waitcnt vmcnt(0)
AR 29,164
Maav 96,180,192
AR 30,168
AR 31,172
Maav 100,184,176
s_nop 0
TR 204,13,52000
TR 206,13,52512
Maav 104,184,180
AR 32,176
Maav 108,184,184
v_cmp_nle_f32 vcc,v28,s75
s_nop 0
s_or_b64 s[86:87],s[86:87],vcc
Maav 112,184,188
v_cmp_nle_f32 vcc,v29,s75
s_nop 0
s_or_b64 s[86:87],s[86:87],vcc
Maav 116,184,192
v_cmp_nle_f32 vcc,v30,s75
s_nop 0
s_or_b64 s[86:87],s[86:87],vcc
Maav 120,188,176
s_nop 0
TR 208,13,52032
TR 210,13,52544
Maav 124,188,180
v_cmp_nle_f32 vcc,v31,s75
s_nop 0
s_or_b64 s[86:87],s[86:87],vcc
Maav 128,188,184
v_cmp_nle_f32 vcc,v32,s75
s_nop 0
s_or_b64 s[86:87],s[86:87],vcc
Maav 132,188,188
v_rcp_f32 v28,v28
Maav 136,188,192
v_rcp_f32 v29,v29
Maav 140,192,176
v_rcp_f32 v30,v30
Maav 144,192,180
v_rcp_f32 v31,v31
Maav 148,192,184
v_rcp_f32 v32,v32
Maav 152,192,188
AR 116,80
AR 117,81
Maav 156,192,192
WL 4
AR 118,82
AR 119,83
Maav 0,196,176
WL 0
s_barrier
Maav 4,196,180
s_mov_b32 s70,1024
s_add_u32 s98,s71,114688
v_lshl_add_u32 v8,v2,1,s98
Maav 8,196,184
s_add_u32 s83,s70,0
AR 120,100
AR 121,101
Maav 12,196,188
M0 82,20480
AR 122,102
AR 123,103
Maav 16,196,192
DM 7,40,83,0
Maav 20,200,176
M0 82,20384
v_mul_f32 v116,v116,v28
v_mul_f32 v117,v117,v28
Maav 24,200,180
DM 7,40,83,1152
Maav 28,200,184
M0 82,20288
v_mul_f32 v118,v118,v28
v_mul_f32 v119,v119,v28
Maav 32,200,188
DM 7,40,83,2304
Maav 36,200,192
M0 82,20192
v_mul_f32 v120,v120,v28
Maav 40,204,176
DM 7,40,83,3456
Maav 44,204,180
v_mul_f32 v121,v121,v28
v_mul_f32 v122,v122,v28
Maav 48,204,184
v_mul_f32 v123,v123,v28
CV 196,116,117
Maav 52,204,188
buffer_load_dword v8,v8,s[60:63],0 offen
Maav 56,204,192
CV 197,118,119
CV 198,120,121
Maav 60,208,176
CV 199,122,123
Maav 64,208,180
Maav 68,208,184
Maav 72,208,188
Maav 76,208,192
s_add_u32 s83,s70,147456
M0 82,37792
AR 124,84
AR 125,85
s_nop 1
DM 7,40,83,0
AR 126,86
AR 127,87
AR 128,104
AR 129,105
AR 130,106
AR 131,107
v_pk_mul_f32 v[124:125],v[124:125],v[28:29] op_sel:[0,1] op_sel_hi:[1,1]
v_pk_mul_f32 v[126:127],v[126:127],v[28:29] op_sel:[0,1] op_sel_hi:[1,1]
v_pk_mul_f32 v[128:129],v[128:129],v[28:29] op_sel:[0,1] op_sel_hi:[1,1]
v_pk_mul_f32 v[130:131],v[130:131],v[28:29] op_sel:[0,1] op_sel_hi:[1,1]
CV 200,124,125
CV 201,126,127
CV 202,128,129
CV 203,130,131
M0 82,37696
AR 132,88
AR 133,89
s_nop 1
DM 7,40,83,1152
AR 134,90
AR 135,91
AR 136,108
AR 137,109
AR 138,110
AR 139,111
v_pk_mul_f32 v[132:133],v[132:133],v[30:31] op_sel_hi:[1,0]
v_pk_mul_f32 v[134:135],v[134:135],v[30:31] op_sel_hi:[1,0]
v_pk_mul_f32 v[136:137],v[136:137],v[30:31] op_sel_hi:[1,0]
v_pk_mul_f32 v[138:139],v[138:139],v[30:31] op_sel_hi:[1,0]
CV 204,132,133
CV 205,134,135
CV 206,136,137
CV 207,138,139
M0 82,37600
AR 140,92
AR 141,93
s_nop 1
DM 7,40,83,2304
AR 142,94
AR 143,95
AR 144,112
AR 145,113
AR 146,114
AR 147,115
v_pk_mul_f32 v[140:141],v[140:141],v[30:31] op_sel:[0,1] op_sel_hi:[1,1]
v_pk_mul_f32 v[142:143],v[142:143],v[30:31] op_sel:[0,1] op_sel_hi:[1,1]
v_pk_mul_f32 v[144:145],v[144:145],v[30:31] op_sel:[0,1] op_sel_hi:[1,1]
v_pk_mul_f32 v[146:147],v[146:147],v[30:31] op_sel:[0,1] op_sel_hi:[1,1]
CV 208,140,141
CV 209,142,143
CV 210,144,145
CV 211,146,147
M0 82,37504
AR 148,96
AR 149,97
s_nop 1
DM 7,40,83,3456
AR 150,98
AR 151,99
AR 152,116
AR 153,117
AR 154,118
AR 155,119
v_pk_mul_f32 v[148:149],v[148:149],v[32:33] op_sel_hi:[1,0]
v_pk_mul_f32 v[150:151],v[150:151],v[32:33] op_sel_hi:[1,0]
v_pk_mul_f32 v[152:153],v[152:153],v[32:33] op_sel_hi:[1,0]
v_pk_mul_f32 v[154:155],v[154:155],v[32:33] op_sel_hi:[1,0]
CV 212,148,149
CV 213,150,151
CV 214,152,153
CV 215,154,155
s_add_u32 s70,s68,1
s_and_b32 s70,s70,7
s_lshl_b32 s70,s70,7
s_add_u32 s83,s70,0
M0 82,55104
AR 116,120
AR 117,121
s_nop 1
DM 7,40,83,0
AR 118,122
AR 119,123
AR 120,140
AR 121,141
AR 122,142
AR 123,143
v_pk_mul_f32 v[116:117],v[116:117],v[28:29] op_sel_hi:[1,0]
v_pk_mul_f32 v[118:119],v[118:119],v[28:29] op_sel_hi:[1,0]
v_pk_mul_f32 v[120:121],v[120:121],v[28:29] op_sel_hi:[1,0]
v_pk_mul_f32 v[122:123],v[122:123],v[28:29] op_sel_hi:[1,0]
CV 156,116,117
CV 157,118,119
CV 158,120,121
CV 159,122,123
M0 82,55008
AR 124,124
AR 125,125
s_nop 1
DM 7,40,83,1152
AR 126,126
AR 127,127
AR 128,144
AR 129,145
AR 130,146
AR 131,147
v_pk_mul_f32 v[124:125],v[124:125],v[28:29] op_sel:[0,1] op_sel_hi:[1,1]
v_pk_mul_f32 v[126:127],v[126:127],v[28:29] op_sel:[0,1] op_sel_hi:[1,1]
v_pk_mul_f32 v[128:129],v[128:129],v[28:29] op_sel:[0,1] op_sel_hi:[1,1]
v_pk_mul_f32 v[130:131],v[130:131],v[28:29] op_sel:[0,1] op_sel_hi:[1,1]
CV 160,124,125
CV 161,126,127
CV 162,128,129
CV 163,130,131
M0 82,54912
AR 132,128
AR 133,129
s_nop 1
DM 7,40,83,2304
AR 134,130
AR 135,131
AR 136,148
AR 137,149
AR 138,150
AR 139,151
v_pk_mul_f32 v[132:133],v[132:133],v[30:31] op_sel_hi:[1,0]
v_pk_mul_f32 v[134:135],v[134:135],v[30:31] op_sel_hi:[1,0]
v_pk_mul_f32 v[136:137],v[136:137],v[30:31] op_sel_hi:[1,0]
v_pk_mul_f32 v[138:139],v[138:139],v[30:31] op_sel_hi:[1,0]
CV 164,132,133
CV 165,134,135
CV 166,136,137
CV 167,138,139
M0 82,54816
AR 140,132
AR 141,133
s_nop 1
DM 7,40,83,3456
AR 142,134
AR 143,135
AR 144,152
AR 145,153
AR 146,154
AR 147,155
v_pk_mul_f32 v[140:141],v[140:141],v[30:31] op_sel:[0,1] op_sel_hi:[1,1]
v_pk_mul_f32 v[142:143],v[142:143],v[30:31] op_sel:[0,1] op_sel_hi:[1,1]
v_pk_mul_f32 v[144:145],v[144:145],v[30:31] op_sel:[0,1] op_sel_hi:[1,1]
v_pk_mul_f32 v[146:147],v[146:147],v[30:31] op_sel:[0,1] op_sel_hi:[1,1]
CV 168,140,141
CV 169,142,143
CV 170,144,145
CV 171,146,147
s_add_u32 s83,s70,147456
M0 82,72416
AR 148,136
AR 149,137
s_nop 1
DM 7,40,83,0
AR 150,138
AR 151,139
AR 152,156
AR 153,157
AR 154,158
AR 155,159
v_pk_mul_f32 v[148:149],v[148:149],v[32:33] op_sel_hi:[1,0]
v_pk_mul_f32 v[150:151],v[150:151],v[32:33] op_sel_hi:[1,0]
v_pk_mul_f32 v[152:153],v[152:153],v[32:33] op_sel_hi:[1,0]
v_pk_mul_f32 v[154:155],v[154:155],v[32:33] op_sel_hi:[1,0]
CV 172,148,149
CV 173,150,151
CV 174,152,153
CV 175,154,155
M0 82,72320
s_nop 0
DM 7,40,83,1152
M0 82,72224
s_nop 0
DM 7,40,83,2304
M0 82,72128
s_nop 0
DM 7,40,83,3456
s_waitcnt vmcnt(8) lgkmcnt(0)
s_barrier
TR 180,3,0
TR 182,3,512
TR 184,3,4224
TR 186,3,4736
TR 188,3,32
TR 190,3,544
TR 192,3,4256
TR 194,3,4768
TR 196,3,8448
TR 198,3,8960
WL 8
Maavz 80,180,216
s_nop 0
TR 200,3,12672
TR 202,3,13184
WL 8
Maav 80,184,236
s_mul_i32 s90,s64,20480
s_add_u32 s90,s90,69248
s_cmp_eq_u32 s64,0
s_cselect_b32 s90,0,s90
v_add_u32 v34,s90,v2
Maavz 84,180,220
s_xor_b32 s91,s64,2
s_mul_i32 s90,s91,20480
s_add_u32 s90,s90,69248
s_cmp_eq_u32 s91,0
s_cselect_b32 s90,0,s90
v_add_u32 v35,s90,v2
Maav 84,184,240
AR 176,0
AR 177,1
Maavz 88,180,224
AR 178,2
AR 179,3
Maav 88,184,244
s_nop 0
TR 204,3,8480
TR 206,3,8992
Maavz 92,180,228
AR 180,20
AR 181,21
Maav 92,184,248
AR 182,22
AR 183,23
Maavz 96,180,232
v_mul_f32 v176,v176,v28
v_mul_f32 v177,v177,v28
Maav 96,184,252
WL 6
v_mul_f32 v178,v178,v28
v_mul_f32 v179,v179,v28
Maavz 100,188,216
s_nop 0
TR 208,3,12704
TR 210,3,13216
Maav 100,192,236
v_mul_f32 v180,v180,v28
v_mul_f32 v181,v181,v28
Maavz 104,188,220
v_mul_f32 v182,v182,v28
v_mul_f32 v183,v183,v28
Maav 104,192,240
CV 116,176,177
CV 117,178,179
Maavz 108,188,224
CV 118,180,181
CV 119,182,183
Maav 108,192,244
s_nop 0
TR 180,5,0
TR 182,5,512
Maavz 112,188,228
ds_write_b128 v34,v[196:199] offset:10240
Maav 112,192,248
ds_write_b128 v34,v[116:119] offset:0
Maavz 116,188,232
AR 184,4
AR 185,5
Maav 116,192,252
WL 8
AR 186,6
AR 187,7
Maaa 80,196,216
s_nop 0
TR 184,5,4224
TR 186,5,4736
Maaa 80,200,236
AR 188,24
AR 189,25
Maaa 84,196,220
AR 190,26
AR 191,27
Maaa 84,200,240
v_mul_f32 v184,v184,v29
v_mul_f32 v185,v185,v29
Maaa 88,196,224
v_mul_f32 v186,v186,v29
v_mul_f32 v187,v187,v29
Maaa 88,200,244
s_nop 0
TR 188,5,32
TR 190,5,544
Maaa 92,196,228
v_mul_f32 v188,v188,v29
v_mul_f32 v189,v189,v29
Maaa 92,200,248
v_mul_f32 v190,v190,v29
v_mul_f32 v191,v191,v29
Maaa 96,196,232
CV 120,184,185
CV 121,186,187
Maaa 96,200,252
WL 8
CV 122,188,189
CV 123,190,191
Maaa 100,204,216
s_nop 0
TR 192,5,4256
TR 194,5,4768
Maaa 100,208,236
ds_write_b128 v34,v[200:203] offset:11264
Maaa 104,204,220
ds_write_b128 v34,v[120:123] offset:1024
Maaa 104,208,240
AR 176,8
AR 177,9
Maaa 108,204,224
AR 178,10
AR 179,11
Maaa 108,208,244
s_nop 0
TR 196,5,8448
TR 198,5,8960
Maaa 112,204,228
AR 180,28
AR 181,29
Maaa 112,208,248
AR 182,30
AR 183,31
Maaa 116,204,232
v_mul_f32 v176,v176,v30
v_mul_f32 v177,v177,v30
Maaa 116,208,252
WL 8
v_mul_f32 v178,v178,v30
v_mul_f32 v179,v179,v30
Maav 80,180,36
s_nop 0
TR 200,5,12672
TR 202,5,13184
Maav 80,184,56
v_mul_f32 v180,v180,v30
v_mul_f32 v181,v181,v30
Maav 84,180,40
v_mul_f32 v182,v182,v30
v_mul_f32 v183,v183,v30
Maav 84,184,60
CV 124,176,177
CV 125,178,179
Maav 88,180,44
CV 126,180,181
CV 127,182,183
Maav 88,184,64
s_nop 0
TR 204,5,8480
TR 206,5,8992
Maav 92,180,48
ds_write_b128 v34,v[204:207] offset:12288
Maav 92,184,68
ds_write_b128 v34,v[124:127] offset:2048
Maav 96,180,52
AR 184,12
AR 185,13
Maav 96,184,72
WL 10
AR 186,14
AR 187,15
Maav 100,188,36
s_nop 0
TR 208,5,12704
TR 210,5,13216
Maav 100,192,56
AR 188,32
AR 189,33
Maav 104,188,40
AR 190,34
AR 191,35
Maav 104,192,60
v_mul_f32 v184,v184,v31
v_mul_f32 v185,v185,v31
Maav 108,188,44
v_mul_f32 v186,v186,v31
v_mul_f32 v187,v187,v31
Maav 108,192,64
v_mul_f32 v188,v188,v31
v_mul_f32 v189,v189,v31
Maav 112,188,48
v_mul_f32 v190,v190,v31
v_mul_f32 v191,v191,v31
Maav 112,192,68
CV 128,184,185
CV 129,186,187
Maav 116,188,52
CV 130,188,189
CV 131,190,191
Maav 116,192,72
WL 6
ds_write_b128 v34,v[208:211] offset:13312
Maav 80,196,76
s_waitcnt vmcnt(0) lgkmcnt(0)
s_barrier
Maav 80,200,96
s_mov_b32 s70,1024
TR 180,3,34624
TR 182,3,35136
Maav 84,196,80
s_add_u32 s83,s70,294912
TR 184,3,38848
TR 186,3,39360
Maav 84,200,100
M0 82,20480
ds_write_b128 v34,v[128:131] offset:3072
Maav 88,196,84
DM 7,40,83,0
Maav 88,200,104
M0 82,20384
TR 188,3,34656
TR 190,3,35168
Maav 92,196,88
DM 7,40,83,1152
Maav 92,200,108
M0 82,20288
AR 176,16
AR 177,17
Maav 96,196,92
DM 7,40,83,2304
Maav 96,200,112
AR 178,18
AR 179,19
Maav 100,204,76
M0 82,20192
TR 192,3,38880
TR 194,3,39392
Maav 100,208,96
DM 7,40,83,3456
Maav 104,204,80
s_add_u32 s83,s70,442368
AR 180,36
AR 181,37
Maav 104,208,100
M0 82,37792
AR 182,38
AR 183,39
Maav 108,204,84
DM 7,40,83,0
Maav 108,208,104
M0 82,37696
TR 196,3,43072
TR 198,3,43584
Maav 112,204,88
DM 7,40,83,1152
Maav 112,208,108
M0 82,37600
v_mul_f32 v176,v176,v32
v_mul_f32 v177,v177,v32
Maav 116,204,92
DM 7,40,83,2304
Maav 116,208,112
WL 7
v_mul_f32 v178,v178,v32
v_mul_f32 v179,v179,v32
Maavz 120,180,216
M0 82,37504
TR 200,3,47296
TR 202,3,47808
Maav 120,184,236
DM 7,40,83,3456
Maavz 124,180,220
v_mul_f32 v180,v180,v32
v_mul_f32 v181,v181,v32
Maav 124,184,240
v_mul_f32 v182,v182,v32
v_mul_f32 v183,v183,v32
Maavz 128,180,224
CV 132,176,177
CV 133,178,179
Maav 128,184,244
s_nop 0
TR 204,3,43104
TR 206,3,43616
Maavz 132,180,228
CV 134,180,181
CV 135,182,183
Maav 132,184,248
ds_write_b128 v34,v[212:215] offset:14336
Maavz 136,180,232
ds_write_b128 v34,v[132:135] offset:4096
Maav 136,184,252
WL 8
AR 184,40
AR 185,41
Maavz 140,188,216
s_nop 0
TR 208,3,47328
TR 210,3,47840
Maav 140,192,236
AR 186,42
AR 187,43
Maavz 144,188,220
AR 188,60
AR 189,61
Maav 144,192,240
AR 190,62
AR 191,63
Maavz 148,188,224
v_mul_f32 v184,v184,v28
v_mul_f32 v185,v185,v28
Maav 148,192,244
s_nop 0
TR 180,5,34624
TR 182,5,35136
Maavz 152,188,228
v_mul_f32 v186,v186,v28
v_mul_f32 v187,v187,v28
Maav 152,192,248
v_mul_f32 v188,v188,v28
v_mul_f32 v189,v189,v28
Maavz 156,188,232
v_mul_f32 v190,v190,v28
v_mul_f32 v191,v191,v28
Maav 156,192,252
WL 8
CV 136,184,185
CV 137,186,187
Maaa 120,196,216
s_nop 0
TR 184,5,38848
TR 186,5,39360
Maaa 120,200,236
CV 138,188,189
CV 139,190,191
Maaa 124,196,220
ds_write_b128 v34,v[156:159] offset:15360
Maaa 124,200,240
ds_write_b128 v34,v[136:139] offset:5120
Maaa 128,196,224
AR 176,44
AR 177,45
Maaa 128,200,244
s_nop 0
TR 188,5,34656
TR 190,5,35168
Maaa 132,196,228
AR 178,46
AR 179,47
Maaa 132,200,248
AR 180,64
AR 181,65
Maaa 136,196,232
AR 182,66
AR 183,67
Maaa 136,200,252
WL 8
v_mul_f32 v176,v176,v29
v_mul_f32 v177,v177,v29
Maaa 140,204,216
s_nop 0
TR 192,5,38880
TR 194,5,39392
Maaa 140,208,236
v_mul_f32 v178,v178,v29
v_mul_f32 v179,v179,v29
Maaa 144,204,220
v_mul_f32 v180,v180,v29
v_mul_f32 v181,v181,v29
Maaa 144,208,240
v_mul_f32 v182,v182,v29
v_mul_f32 v183,v183,v29
Maaa 148,204,224
CV 140,176,177
CV 141,178,179
Maaa 148,208,244
s_nop 0
TR 196,5,43072
TR 198,5,43584
Maaa 152,204,228
CV 142,180,181
CV 143,182,183
Maaa 152,208,248
ds_write_b128 v34,v[160:163] offset:16384
Maaa 156,204,232
ds_write_b128 v34,v[140:143] offset:6144
Maaa 156,208,252
WL 10
AR 184,48
AR 185,49
Maav 120,180,36
s_nop 0
TR 200,5,47296
TR 202,5,47808
Maav 120,184,56
AR 186,50
AR 187,51
Maav 124,180,40
AR 188,68
AR 189,69
Maav 124,184,60
AR 190,70
AR 191,71
Maav 128,180,44
v_mul_f32 v184,v184,v30
v_mul_f32 v185,v185,v30
Maav 128,184,64
s_nop 0
TR 204,5,43104
TR 206,5,43616
Maav 132,180,48
v_mul_f32 v186,v186,v30
v_mul_f32 v187,v187,v30
Maav 132,184,68
v_mul_f32 v188,v188,v30
v_mul_f32 v189,v189,v30
Maav 136,180,52
v_mul_f32 v190,v190,v30
v_mul_f32 v191,v191,v30
Maav 136,184,72
WL 8
CV 144,184,185
CV 145,186,187
Maav 140,188,36
s_nop 0
TR 208,5,47328
TR 210,5,47840
Maav 140,192,56
CV 146,188,189
CV 147,190,191
Maav 144,188,40
ds_write_b128 v34,v[164:167] offset:17408
Maav 144,192,60
ds_write_b128 v34,v[144:147] offset:7168
Maav 148,188,44
AR 176,52
AR 177,53
Maav 148,192,64
AR 178,54
AR 179,55
Maav 152,188,48
AR 180,72
AR 181,73
AR 182,74
AR 183,75
v_mul_f32 v176,v176,v31
v_mul_f32 v177,v177,v31
v_mul_f32 v178,v178,v31
v_mul_f32 v179,v179,v31
v_mul_f32 v180,v180,v31
v_mul_f32 v181,v181,v31
v_mul_f32 v182,v182,v31
v_mul_f32 v183,v183,v31
CV 148,176,177
CV 149,178,179
CV 150,180,181
CV 151,182,183
ds_write_b128 v34,v[168:171] offset:18432
ds_write_b128 v34,v[148:151] offset:8192
AR 184,56
AR 185,57
AR 186,58
AR 187,59
AR 188,76
AR 189,77
AR 190,78
AR 191,79
v_mul_f32 v184,v184,v32
v_mul_f32 v185,v185,v32
v_mul_f32 v186,v186,v32
v_mul_f32 v187,v187,v32
v_mul_f32 v188,v188,v32
v_mul_f32 v189,v189,v32
v_mul_f32 v190,v190,v32
v_mul_f32 v191,v191,v32
CV 152,184,185
CV 153,186,187
CV 154,188,189
CV 155,190,191
ds_write_b128 v34,v[172:175] offset:19456
ds_write_b128 v34,v[152:155] offset:9216
Maav 152,192,68
s_or_b32 s90,s86,s87
v_mov_b32 v28,s90
s_lshl_b32 s91,s64,2
s_add_u32 s91,s91,162144
v_mov_b32 v29,s91
Maav 156,188,52
ds_write_b32 v29,v28 offset:0
Maav 156,192,72
WL 11
v_mov_b32 v27,162144
Maav 120,196,76
s_waitcnt vmcnt(0) lgkmcnt(0)
s_barrier
Maav 120,200,96
s_add_u32 s70,s68,1
TR 180,3,0
TR 182,3,512
Maav 124,196,80
s_and_b32 s70,s70,7
TR 184,3,4224
TR 186,3,4736
Maav 124,200,100
s_lshl_b32 s70,s70,7
ds_read_b128 v[28:31],v27 offset:0
Maav 128,196,84
s_add_u32 s83,s70,294912
RA 0,35,0
Maav 128,200,104
M0 82,55104
TR 188,3,32
TR 190,3,544
Maav 132,196,88
DM 7,40,83,0
Maav 132,200,108
M0 82,55008
RA 4,35,1024
Maav 136,196,92
DM 7,40,83,1152
Maav 136,200,112
RA 8,35,2048
Maav 140,204,76
M0 82,54912
TR 192,3,4256
TR 194,3,4768
Maav 140,208,96
DM 7,40,83,2304
Maav 144,204,80
M0 82,54816
RA 12,35,3072
Maav 144,208,100
DM 7,40,83,3456
Maav 148,204,84
s_add_u32 s83,s70,442368
RA 16,35,4096
Maav 148,208,104
M0 82,72416
TR 196,3,8448
TR 198,3,8960
Maav 152,204,88
DM 7,40,83,0
Maav 152,208,108
M0 82,72320
RA 20,35,5120
Maav 156,204,92
DM 7,40,83,1152
Maav 156,208,112
WL 13
RA 24,35,6144
Maav 80,180,116
M0 82,72224
TR 200,3,12672
TR 202,3,13184
Maav 80,184,136
DM 7,40,83,2304
Maav 84,180,120
M0 82,72128
RA 28,35,7168
Maav 84,184,140
DM 7,40,83,3456
Maav 88,180,124
RA 32,35,8192
Maav 88,184,144
s_nop 0
TR 204,3,8480
TR 206,3,8992
Maav 92,180,128
RA 36,35,9216
Maav 92,184,148
RA 40,35,10240
Maav 96,180,132
RA 44,35,11264
Maav 96,184,152
WL 15
RA 48,35,12288
Maav 100,188,116
s_nop 0
TR 208,3,12704
TR 210,3,13216
Maav 100,192,136
RA 52,35,13312
Maav 104,188,120
RA 56,35,14336
Maav 104,192,140
RA 60,35,15360
Maav 108,188,124
RA 64,35,16384
Maav 108,192,144
s_nop 0
TR 180,5,0
TR 182,5,512
Maav 112,188,128
RA 68,35,17408
Maav 112,192,148
RA 72,35,18432
Maav 116,188,132
RA 76,35,19456
Maav 116,192,152
WL 15
Maav 80,196,196
s_nop 0
TR 184,5,4224
TR 186,5,4736
Maav 80,200,156
Maav 84,196,200
v_or3_b32 v28,v28,v29,v30
v_or_b32 v28,v28,v31
s_nop 0
v_readfirstlane_b32 s90,v28
s_nop 0
s_andn2_b32 s90,s90,s85
s_cbranch_scc1 LB_redo
Maav 84,200,160
v_bfe_u32 v28,v1,2,2
v_lshrrev_b32 v29,4,v1
Maav 88,196,204
v_and_b32 v30,1,v1
v_lshlrev_b32 v30,4,v30
Maav 88,200,164
s_nop 0
TR 188,5,32
TR 190,5,544
Maav 92,196,208
v_mov_b32 v31,0x3f80
v_lshlrev_b32 v30,v30,v31
Maav 92,200,168
v_cmp_eq_u32 vcc,v28,v29
s_nop 1
v_cndmask_b32 v30,0,v30,vcc
Maav 96,196,212
v_and_b32 v28,2,v1
Maav 96,200,172
WL 13
v_cmp_eq_u32 vcc,0,v28
s_nop 1
v_cndmask_b32 v12,0,v30,vcc
v_sub_u32 v13,v30,v12
Maav 100,204,196
s_nop 0
TR 192,5,4256
TR 194,5,4768
Maav 100,208,156
v_mov_b32 v10,0
v_mov_b32 v11,0
Maav 104,204,200
v_mov_b32 v14,0
v_mov_b32 v15,0
Maav 104,208,160
v_and_b32 v192,15,v1
v_mul_u32_u24 v192,0x480,v192
Maav 108,204,204
v_bfe_u32 v28,v1,4,1
v_lshl_add_u32 v192,v28,5,v192
Maav 108,208,164
s_nop 0
TR 196,5,8448
TR 198,5,8960
Maav 112,204,208
v_lshrrev_b32 v28,5,v1
v_lshl_add_u32 v192,v28,4,v192
Maav 112,208,168
v_mov_b32 v195,s89
Maav 116,204,212
s_mov_b32 s17,s89
Maav 116,208,172
WL 6
Maaa 80,180,0
s_mov_b32 s19,s89
TR 200,5,12672
TR 202,5,13184
Maaa 80,184,20
s_add_u32 s18,s88,31
Maaa 84,180,4
s_lshl_b32 s90,s64,6
Maaa 84,184,24
s_lshl_b32 s91,s68,3
Maaa 88,180,8
s_add_u32 s81,s90,s91
Maaa 88,184,28
s_add_u32 s91,s68,2
TR 204,5,8480
TR 206,5,8992
Maaa 92,180,12
s_and_b32 s91,s91,7
Maaa 92,184,32
s_mul_i32 s72,s91,163840
Maaa 96,180,16
s_mul_i32 s92,s64,5120
Maaa 96,184,36
WL 6
Maaa 100,188,0
s_add_u32 s72,s72,s92
TR 208,5,12704
TR 210,5,13216
Maaa 100,192,20
s_lshl_b32 s91,s91,3
Maaa 104,188,4
s_add_u32 s44,s90,s91
Maaa 104,192,24
s_add_u32 s91,s68,1
Maaa 108,188,8
s_and_b32 s91,s91,7
Maaa 108,192,28
s_mul_i32 s73,s91,163840
Maaa 112,188,12
s_mul_i32 s92,s64,5120
Maaa 112,192,32
s_add_u32 s73,s73,s92
Maaa 116,188,16
s_lshl_b32 s91,s91,3
s_add_u32 s45,s90,s91
s_lshl_b32 s92,s64,3
Maaa 116,192,36
WL 4
s_add_u32 s92,s92,s68
s_lshl_b32 s101,s92,10
Maaa 80,196,40
s_waitcnt vmcnt(0) lgkmcnt(0)
s_barrier
Maaa 80,200,60
s_add_u32 s70,s68,2
TR 180,3,34624
TR 182,3,35136
Maaa 84,196,44
s_and_b32 s70,s70,7
TR 184,3,38848
TR 186,3,39360
Maaa 84,200,64
s_lshl_b32 s70,s70,7
s_mul_i32 s90,s92,52
s_lshr_b32 s90,s90,8
Maaa 88,196,48
s_add_u32 s83,s70,0
s_min_u32 s90,s90,3
s_lshl_b32 s46,s90,6
Maaa 88,200,68
M0 82,89728
TR 188,3,34656
TR 190,3,35168
Maaa 92,196,52
DM 7,40,83,0
Maaa 92,200,72
M0 82,89632
s_mul_i32 s91,s90,5
s_sub_u32 s91,s92,s91
Maaa 96,196,56
DM 7,40,83,1152
Maaa 96,200,76
s_mul_i32 s100,s91,18432
s_and_b32 s91,s90,1
Maaa 100,204,40
M0 82,89536
TR 192,3,38880
TR 194,3,39392
Maaa 100,208,60
DM 7,40,83,2304
Maaa 104,204,44
M0 82,89440
s_mul_i32 s91,s91,92160
s_add_u32 s100,s100,s91
Maaa 104,208,64
DM 7,40,83,3456
Maaa 108,204,48
s_add_u32 s83,s70,147456
s_lshr_b32 s90,s90,1
s_lshl_b32 s90,s90,6
Maaa 108,208,68
M0 82,107040
TR 196,3,43072
TR 198,3,43584
Maaa 112,204,52
DM 7,40,83,0
Maaa 112,208,72
M0 82,106944
s_add_u32 s100,s100,s90
s_add_u32 s100,s100,1024
Maaa 116,204,56
DM 7,40,83,1152
Maaa 116,208,76
WL 6
s_mov_b32 s8,s48
s_mov_b32 s9,s49
Maav 120,180,116
M0 82,106848
TR 200,3,47296
TR 202,3,47808
Maav 120,184,136
DM 7,40,83,2304
Maav 124,180,120
M0 82,106752
s_mov_b32 s11,0x20000
Maav 124,184,140
DM 7,40,83,3456
Maav 128,180,124
s_add_u32 s83,s70,294912
s_cmp_lt_u32 s92,20
s_cselect_b32 s10,s50,0
Maav 128,184,144
M0 82,124352
TR 204,3,43104
TR 206,3,43616
Maav 132,180,128
DM 7,40,83,0
Maav 132,184,148
M0 82,124256
AR 176,80
AR 177,81
Maav 136,180,132
s_mov_b32 s12,s52
AR 178,82
AR 179,83
Maav 136,184,152
WL 6
DM 7,40,83,1152
Maav 140,188,116
M0 82,124160
TR 208,3,47328
TR 210,3,47840
Maav 140,192,136
s_mov_b32 s13,s53
AR 180,100
AR 181,101
Maav 144,188,120
DM 7,40,83,2304
Maav 144,192,140
M0 82,124064
AR 182,102
AR 183,103
Maav 148,188,124
s_mov_b32 s15,0x20000
s_cmp_lt_u32 s92,20
s_cselect_b32 s14,s54,0
Maav 148,192,144
s_mov_b32 s99,131072
TR 180,5,34624
TR 182,5,35136
Maav 152,188,128
DM 7,40,83,3456
Maav 152,192,148
s_add_u32 s83,s70,442368
s_mul_i32 s47,s65,92160
CV 176,176,177
Maav 156,188,132
M0 82,141664
CV 177,178,179
CV 178,180,181
Maav 156,192,152
WL 6
DM 7,40,83,0
Maav 120,196,196
M0 82,141568
TR 184,5,38848
TR 186,5,39360
Maav 120,200,156
s_lshl_b32 s90,s68,7
CV 179,182,183
s_add_u32 s23,s74,0
Maav 124,196,200
DM 7,40,83,1152
Maav 124,200,160
M0 82,141472
Maav 128,196,204
s_add_u32 s47,s47,s90
Maav 128,200,164
s_lshl_b32 s90,s66,6
TR 188,5,34656
TR 190,5,35168
Maav 132,196,208
DM 7,40,83,2304
Maav 132,200,168
M0 82,141376
Maav 136,196,212
s_add_u32 s47,s47,s90
Maav 136,200,172
WL 6
DM 7,40,83,3456
Maav 140,204,196
s_nop 0
TR 192,5,38880
TR 194,5,39392
Maav 140,208,156
Maav 144,204,200
buffer_store_dwordx4 v[176:179],v2,s[48:51],s23 offen sc1
Maav 144,208,160
AR 184,84
AR 185,85
Maav 148,204,204
s_add_u32 s70,s68,3
AR 186,86
AR 187,87
Maav 148,208,164
s_and_b32 s70,s70,7
TR 196,5,43072
TR 198,5,43584
Maav 152,204,208
s_lshl_b32 s70,s70,7
AR 188,104
AR 189,105
Maav 152,208,168
s_add_u32 s83,s70,0
AR 190,106
AR 191,107
Maav 156,204,212
M0 82,20480
CV 184,184,185
CV 185,186,187
Maav 156,208,172
WL 6
CV 186,188,189
CV 187,190,191
Maaa 120,180,0
s_add_u32 s23,s74,1024
TR 200,5,47296
TR 202,5,47808
Maaa 120,184,20
DM 7,40,83,0
Maaa 124,180,4
buffer_store_dwordx4 v[184:187],v2,s[48:51],s23 offen sc1
Maaa 124,184,24
AR 176,88
AR 177,89
Maaa 128,180,8
AR 178,90
AR 179,91
Maaa 128,184,28
s_nop 0
TR 204,5,43104
TR 206,5,43616
Maaa 132,180,12
AR 180,108
AR 181,109
Maaa 132,184,32
AR 182,110
AR 183,111
Maaa 136,180,16
M0 82,20384
CV 176,176,177
CV 177,178,179
Maaa 136,184,36
WL 6
CV 178,180,181
CV 179,182,183
Maaa 140,188,0
s_add_u32 s23,s74,2048
TR 208,5,47328
TR 210,5,47840
Maaa 140,192,20
DM 7,40,83,1152
Maaa 144,188,4
buffer_store_dwordx4 v[176:179],v2,s[48:51],s23 offen sc1
Maaa 144,192,24
AR 184,92
AR 185,93
Maaa 148,188,8
AR 186,94
AR 187,95
Maaa 148,192,28
AR 188,112
AR 189,113
Maaa 152,188,12
M0 82,20288
AR 190,114
AR 191,115
Maaa 152,192,32
CV 184,184,185
CV 185,186,187
Maaa 156,188,16
CV 186,188,189
CV 187,190,191
Maaa 156,192,36
WL 4
DM 7,40,83,2304
Maaa 120,196,40
s_waitcnt vmcnt(14) lgkmcnt(0)
s_barrier
Maaa 120,200,60
s_add_u32 s23,s74,3072
TR 180,4,0
TR 182,4,512
Maaa 124,196,44
s_nop 0
TR 184,4,4224
TR 186,4,4736
Maaa 124,200,64
buffer_store_dwordx4 v[184:187],v2,s[48:51],s23 offen sc1
Maaa 128,196,48
AR 176,96
AR 177,97
Maaa 128,200,68
s_nop 0
TR 188,4,32
TR 190,4,544
Maaa 132,196,52
AR 178,98
AR 179,99
Maaa 132,200,72
AR 180,116
AR 181,117
Maaa 136,196,56
AR 182,118
AR 183,119
Maaa 136,200,76
CV 176,176,177
CV 177,178,179
Maaa 140,204,40
s_nop 0
TR 192,4,4256
TR 194,4,4768
Maaa 140,208,60
CV 178,180,181
CV 179,182,183
Maaa 144,204,44
s_add_u32 s23,s74,4096
Maaa 144,208,64
buffer_store_dwordx4 v[176:179],v2,s[48:51],s23 offen sc1
Maaa 148,204,48
Maaa 148,208,68
s_nop 0
TR 196,4,8448
TR 198,4,8960
Maaa 152,204,52
Maaa 152,208,72
Maaa 156,204,56
Maaa 156,208,76
WL 6
Maavz 80,180,216
s_nop 0
TR 200,4,12672
TR 202,4,13184
Maav 80,184,236
s_add_u32 s98,s71,122880
v_lshl_add_u32 v8,v2,1,s98
Maavz 84,180,220
buffer_load_dword v8,v8,s[60:63],0 offen
Maav 84,184,240
AR 184,120
AR 185,121
Maavz 88,180,224
AR 186,122
AR 187,123
Maav 88,184,244
s_nop 0
TR 204,4,8480
TR 206,4,8992
Maavz 92,180,228
AR 188,140
AR 189,141
Maav 92,184,248
AR 190,142
AR 191,143
Maavz 96,180,232
CV 184,184,185
CV 185,186,187
Maav 96,184,252
WL 6
CV 186,188,189
CV 187,190,191
Maavz 100,188,216
s_add_u32 s23,s74,20480
TR 208,4,12704
TR 210,4,13216
Maav 100,192,236
buffer_store_dwordx4 v[184:187],v2,s[48:51],s23 offen sc1
Maavz 104,188,220
AR 176,124
AR 177,125
Maav 104,192,240
AR 178,126
AR 179,127
Maavz 108,188,224
AR 180,144
AR 181,145
Maav 108,192,244
s_nop 0
TR 180,6,0
TR 182,6,512
Maavz 112,188,228
AR 182,146
AR 183,147
Maav 112,192,248
CV 176,176,177
CV 177,178,179
Maavz 116,188,232
CV 178,180,181
CV 179,182,183
Maav 116,192,252
WL 6
v_add_u32_e64 v194,s88,1
Maaa 80,196,216
s_add_u32 s23,s74,21504
TR 184,6,4224
TR 186,6,4736
Maaa 80,200,236
Maaa 84,196,220
Maaa 84,200,240
s_waitcnt vmcnt(2)
s_mov_b64 exec,1
buffer_store_dwordx2 v[194:195],off,s[56:59],s81 sc1
s_mov_b64 exec,-1
Maaa 88,196,224
buffer_store_dwordx4 v[176:179],v2,s[48:51],s23 offen sc1
Maaa 88,200,244
s_nop 0
TR 188,6,32
TR 190,6,544
Maaa 92,196,228
AR 184,128
AR 185,129
Maaa 92,200,248
AR 186,130
AR 187,131
Maaa 96,196,232
AR 188,148
AR 189,149
Maaa 96,200,252
WL 6
AR 190,150
AR 191,151
Maaa 100,204,216
s_nop 0
TR 192,6,4256
TR 194,6,4768
Maaa 100,208,236
CV 184,184,185
CV 185,186,187
Maaa 104,204,220
CV 186,188,189
CV 187,190,191
Maaa 104,208,240
s_add_u32 s23,s74,22528
Maaa 108,204,224
buffer_store_dwordx4 v[184:187],v2,s[48:51],s23 offen sc1
Maaa 108,208,244
s_nop 0
TR 196,6,8448
TR 198,6,8960
Maaa 112,204,228
AR 176,132
AR 177,133
Maaa 112,208,248
AR 178,134
AR 179,135
Maaa 116,204,232
AR 180,152
AR 181,153
Maaa 116,208,252
WL 6
AR 182,154
AR 183,155
Maav 80,180,36
s_nop 0
TR 200,6,12672
TR 202,6,13184
Maav 80,184,56
CV 176,176,177
CV 177,178,179
Maav 84,180,40
CV 178,180,181
CV 179,182,183
Maav 84,184,60
s_add_u32 s23,s74,23552
Maav 88,180,44
buffer_store_dwordx4 v[176:179],v2,s[48:51],s23 offen sc1
Maav 88,184,64
s_nop 0
TR 204,6,8480
TR 206,6,8992
Maav 92,180,48
AR 184,136
AR 185,137
Maav 92,184,68
AR 186,138
AR 187,139
Maav 96,180,52
AR 188,156
AR 189,157
Maav 96,184,72
WL 6
AR 190,158
AR 191,159
Maav 100,188,36
s_nop 0
TR 208,6,12704
TR 210,6,13216
Maav 100,192,56
CV 184,184,185
CV 185,186,187
Maav 104,188,40
CV 186,188,189
CV 187,190,191
Maav 104,192,60
s_add_u32 s23,s74,24576
Maav 108,188,44
buffer_store_dwordx4 v[184:187],v2,s[48:51],s23 offen sc1
Maav 108,192,64
Maav 112,188,48
Maav 112,192,68
Maav 116,188,52
Maav 116,192,72
WL 4
Maav 80,196,76
WL 0
s_barrier
Maav 80,200,96
M0 82,20192
TR 180,4,34624
TR 182,4,35136
Maav 84,196,80
s_nop 0
TR 184,4,38848
TR 186,4,39360
Maav 84,200,100
Maav 88,196,84
DM 7,40,83,3456
Maav 88,200,104
s_add_u32 s83,s70,147456
TR 188,4,34656
TR 190,4,35168
Maav 92,196,88
M0 82,37792
Maav 92,200,108
DM 7,40,83,0
Maav 96,196,92
M0 82,37696
Maav 96,200,112
Maav 100,204,76
s_nop 0
TR 192,4,38880
TR 194,4,39392
Maav 100,208,96
DM 7,40,83,1152
Maav 104,204,80
M0 82,37600
Maav 104,208,100
Maav 108,204,84
DM 7,40,83,2304
Maav 108,208,104
M0 82,37504
TR 196,4,43072
TR 198,4,43584
Maav 112,204,88
Maav 112,208,108
DM 7,40,83,3456
Maav 116,204,92
Maav 116,208,112
WL 6
Maav 80,180,116
s_add_u32 s83,s70,294912
TR 200,4,47296
TR 202,4,47808
Maav 80,184,136
M0 82,55104
v_and_b32 v26,7,v1
v_lshlrev_b32 v26,3,v26
Maav 84,180,120
buffer_load_dwordx2 v[24:25],v26,s[56:59],s46 offen sc1
Maav 84,184,140
DM 7,40,83,0
Maav 88,180,124
M0 82,55008
Maav 88,184,144
s_nop 0
TR 204,4,43104
TR 206,4,43616
Maav 92,180,128
v_add_u32_e64 v194,s88,2
Maav 92,184,148
DM 7,40,83,1152
Maav 96,180,132
Maav 96,184,152
WL 6
Maav 100,188,116
M0 82,54912
TR 208,4,47328
TR 210,4,47840
Maav 100,192,136
s_waitcnt vmcnt(8)
s_mov_b64 exec,1
buffer_store_dwordx2 v[194:195],off,s[56:59],s81 sc1
s_mov_b64 exec,-1
Maav 104,188,120
DM 7,40,83,2304
Maav 104,192,140
M0 82,54816
Maav 108,188,124
Maav 108,192,144
s_nop 0
TR 180,6,34624
TR 182,6,35136
Maav 112,188,128
DM 7,40,83,3456
Maav 112,192,148
s_add_u32 s83,s70,442368
Maav 116,188,132
M0 82,72416
Maav 116,192,152
WL 6
DM 7,40,83,0
Maav 80,196,196
M0 82,72320
TR 184,6,38848
TR 186,6,39360
Maav 80,200,156
Maav 84,196,200
DM 7,40,83,1152
Maav 84,200,160
M0 82,72224
Maav 88,196,204
Maav 88,200,164
s_nop 0
TR 188,6,34656
TR 190,6,35168
Maav 92,196,208
DM 7,40,83,2304
Maav 92,200,168
M0 82,72128
Maav 96,196,212
Maav 96,200,172
WL 6
DM 7,40,83,3456
Maav 100,204,196
s_add_u32 s70,s68,4
TR 192,6,38880
TR 194,6,39392
Maav 100,208,156
s_and_b32 s70,s70,7
Maav 104,204,200
s_lshl_b32 s70,s70,7
Maav 104,208,160
s_add_u32 s83,s70,0
Maav 108,204,204
M0 82,89728
Maav 108,208,164
s_nop 0
TR 196,6,43072
TR 198,6,43584
Maav 112,204,208
DM 7,40,83,0
Maav 112,208,168
M0 82,89632
Maav 116,204,212
Maav 116,208,172
WL 6
DM 7,40,83,1152
Maaa 80,180,0
M0 82,89536
TR 200,6,47296
TR 202,6,47808
Maaa 80,184,20
Maaa 84,180,4
DM 7,40,83,2304
Maaa 84,184,24
M0 82,89440
Maaa 88,180,8
Maaa 88,184,28
s_nop 0
TR 204,6,43104
TR 206,6,43616
Maaa 92,180,12
DM 7,40,83,3456
Maaa 92,184,32
s_add_u32 s83,s70,147456
Maaa 96,180,16
M0 82,107040
s_add_u32 s16,s88,1
s_waitcnt vmcnt(13)
v_cmp_lt_u64 vcc,v[24:25],s[16:17]
v_cmp_gt_u64 s[20:21],v[24:25],s[18:19]
s_nop 0
s_or_b64 s[20:21],s[20:21],vcc
s_cbranch_scc1 LB_spin7
LB_sb7:
Maaa 96,184,36
WL 6
DM 7,40,83,0
Maaa 100,188,0
M0 82,106944
TR 208,6,47328
TR 210,6,47840
Maaa 100,192,20
Maaa 104,188,4
DM 7,40,83,1152
Maaa 104,192,24
M0 82,106848
Maaa 108,188,8
Maaa 108,192,28
DM 7,40,83,2304
Maaa 112,188,12
Maaa 112,192,32
s_add_u32 s76,s101,0
Maaa 116,188,16
buffer_load_dwordx4 a[160:163],v2,s[8:11],s76 offen sc1
Maaa 116,192,36
WL 4
Maaa 80,196,40
WL 0
s_barrier
Maaa 80,200,60
s_add_u32 s76,s101,163840
TR 180,3,0
TR 182,3,512
Maaa 84,196,44
s_nop 0
TR 184,3,4224
TR 186,3,4736
Maaa 84,200,64
buffer_load_dwordx4 a[164:167],v2,s[8:11],s76 offen sc1
Maaa 88,196,48
s_add_u32 s76,s101,327680
Maaa 88,200,68
s_nop 0
TR 188,3,32
TR 190,3,544
Maaa 92,196,52
buffer_load_dwordx4 a[168:171],v2,s[8:11],s76 offen sc1
Maaa 92,200,72
s_add_u32 s76,s101,491520
Maaa 96,196,56
Maaa 96,200,76
buffer_load_dwordx4 a[172:175],v2,s[8:11],s76 offen sc1
Maaa 100,204,40
M0 82,106752
TR 192,3,4256
TR 194,3,4768
Maaa 100,208,60
Maaa 104,204,44
DM 7,40,83,3456
Maaa 104,208,64
s_add_u32 s83,s70,294912
Maaa 108,204,48
M0 82,124352
Maaa 108,208,68
s_nop 0
TR 196,3,8448
TR 198,3,8960
Maaa 112,204,52
DM 7,40,83,0
Maaa 112,208,72
Maaa 116,204,56
Maaa 116,208,76
WL 6
Maavz 120,180,216
M0 82,124256
TR 200,3,12672
TR 202,3,13184
Maav 120,184,236
buffer_load_dwordx2 v[24:25],off,s[56:59],s44 sc1
Maavz 124,180,220
DM 7,40,83,1152
Maav 124,184,240
AR 176,80
AR 177,81
Maavz 128,180,224
AR 178,82
AR 179,83
Maav 128,184,244
s_nop 0
TR 204,3,8480
TR 206,3,8992
Maavz 132,180,228
AR 180,100
AR 181,101
Maav 132,184,248
AR 182,102
AR 183,103
Maavz 136,180,232
CV 176,176,177
CV 177,178,179
Maav 136,184,252
WL 6
CV 178,180,181
CV 179,182,183
Maavz 140,188,216
s_add_u32 s23,s74,40960
TR 208,3,12704
TR 210,3,13216
Maav 140,192,236
buffer_store_dwordx4 v[176:179],v2,s[48:51],s23 offen sc1
Maavz 144,188,220
AR 184,84
AR 185,85
Maav 144,192,240
AR 186,86
AR 187,87
Maavz 148,188,224
AR 188,104
AR 189,105
Maav 148,192,244
s_nop 0
TR 180,5,0
TR 182,5,512
Maavz 152,188,228
AR 190,106
AR 191,107
Maav 152,192,248
CV 184,184,185
CV 185,186,187
Maavz 156,188,232
CV 186,188,189
CV 187,190,191
Maav 156,192,252
WL 6
Maaa 120,196,216
s_add_u32 s23,s74,41984
TR 184,5,4224
TR 186,5,4736
Maaa 120,200,236
buffer_store_dwordx4 v[184:187],v2,s[48:51],s23 offen sc1
Maaa 124,196,220
AR 176,88
AR 177,89
Maaa 124,200,240
AR 178,90
AR 179,91
Maaa 128,196,224
AR 180,108
AR 181,109
Maaa 128,200,244
s_nop 0
TR 188,5,32
TR 190,5,544
Maaa 132,196,228
AR 182,110
AR 183,111
Maaa 132,200,248
CV 176,176,177
CV 177,178,179
Maaa 136,196,232
CV 178,180,181
CV 179,182,183
Maaa 136,200,252
WL 6
Maaa 140,204,216
s_add_u32 s23,s74,43008
TR 192,5,4256
TR 194,5,4768
Maaa 140,208,236
buffer_store_dwordx4 v[176:179],v2,s[48:51],s23 offen sc1
Maaa 144,204,220
AR 184,92
AR 185,93
Maaa 144,208,240
AR 186,94
AR 187,95
Maaa 148,204,224
AR 188,112
AR 189,113
Maaa 148,208,244
s_nop 0
TR 196,5,8448
TR 198,5,8960
Maaa 152,204,228
AR 190,114
AR 191,115
Maaa 152,208,248
CV 184,184,185
CV 185,186,187
Maaa 156,204,232
CV 186,188,189
CV 187,190,191
Maaa 156,208,252
WL 6
Maav 120,180,36
s_add_u32 s23,s74,44032
TR 200,5,12672
TR 202,5,13184
Maav 120,184,56
buffer_store_dwordx4 v[184:187],v2,s[48:51],s23 offen sc1
Maav 124,180,40
AR 176,96
AR 177,97
Maav 124,184,60
AR 178,98
AR 179,99
Maav 128,180,44
AR 180,116
AR 181,117
Maav 128,184,64
s_nop 0
TR 204,5,8480
TR 206,5,8992
Maav 132,180,48
AR 182,118
AR 183,119
Maav 132,184,68
CV 176,176,177
CV 177,178,179
Maav 136,180,52
CV 178,180,181
CV 179,182,183
Maav 136,184,72
WL 6
Maav 140,188,36
s_add_u32 s23,s74,45056
TR 208,5,12704
TR 210,5,13216
Maav 140,192,56
buffer_store_dwordx4 v[176:179],v2,s[48:51],s23 offen sc1
Maav 144,188,40
Maav 144,192,60
Maav 148,188,44
Maav 148,192,64
Maav 152,188,48
Maav 152,192,68
Maav 156,188,52
Maav 156,192,72
WL 4
Maav 120,196,76
s_waitcnt vmcnt(20) lgkmcnt(0)
s_barrier
Maav 120,200,96
M0 82,124160
TR 180,3,34624
TR 182,3,35136
Maav 124,196,80
s_nop 0
TR 184,3,38848
TR 186,3,39360
Maav 124,200,100
DM 7,40,83,2304
Maav 128,196,84
M0 82,124064
Maav 128,200,104
s_nop 0
TR 188,3,34656
TR 190,3,35168
Maav 132,196,88
DM 7,40,83,3456
Maav 132,200,108
s_add_u32 s83,s70,442368
Maav 136,196,92
M0 82,141664
Maav 136,200,112
DM 7,40,83,0
Maav 140,204,76
M0 82,141568
TR 192,3,38880
TR 194,3,39392
Maav 140,208,96
Maav 144,204,80
DM 7,40,83,1152
Maav 144,208,100
M0 82,141472
Maav 148,204,84
Maav 148,208,104
s_nop 0
TR 196,3,43072
TR 198,3,43584
Maav 152,204,88
DM 7,40,83,2304
Maav 152,208,108
M0 82,141376
Maav 156,204,92
Maav 156,208,112
DM 7,40,83,3456
s_waitcnt vmcnt(18)
Mvvaz 16,12,160
s_add_u32 s70,s68,5
Mvvaz 20,10,160
s_and_b32 s70,s70,7
s_waitcnt vmcnt(17)
Mvva 16,12,164
s_lshl_b32 s70,s70,7
Mvva 20,10,164
s_add_u32 s83,s70,0
s_waitcnt vmcnt(16)
Mvva 16,12,168
M0 82,20480
Mvva 20,10,168
DM 7,40,83,0
s_waitcnt vmcnt(16)
Mvva 16,12,172
v_add_u32_e64 v194,s88,3
Mvva 20,10,172
WL 6
Maav 120,180,116
s_nop 0
TR 200,3,47296
TR 202,3,47808
Maav 120,184,136
s_add_u32 s16,s88,2
s_waitcnt vmcnt(13)
v_cmp_lt_u64 vcc,v[24:25],s[16:17]
v_cmp_gt_u64 s[20:21],v[24:25],s[18:19]
s_nop 0
s_or_b64 s[20:21],s[20:21],vcc
s_cbranch_scc1 LB_spin0
LB_sb0:
Maav 124,180,120
s_waitcnt vmcnt(7)
s_mov_b64 exec,1
buffer_store_dwordx2 v[194:195],off,s[56:59],s81 sc1
s_mov_b64 exec,-1
Maav 124,184,140
s_add_u32 s76,s72,20480
Maav 128,180,124
buffer_load_dwordx4 a[160:163],v2,s[48:51],s76 offen sc1
Maav 128,184,144
s_nop 0
TR 204,3,43104
TR 206,3,43616
Maav 132,180,128
Maav 132,184,148
buffer_load_dwordx4 a[164:167],v2,s[48:51],s76 offen offset:1024 sc1
Maav 136,180,132
Maav 136,184,152
WL 6
Maav 140,188,116
s_nop 0
TR 208,3,47328
TR 210,3,47840
Maav 140,192,136
buffer_load_dwordx4 a[168:171],v2,s[48:51],s76 offen offset:2048 sc1
Maav 144,188,120
Maav 144,192,140
Maav 148,188,124
buffer_load_dwordx4 a[172:175],v2,s[48:51],s76 offen offset:3072 sc1
Maav 148,192,144
s_add_u32 s76,s72,24576
TR 180,5,34624
TR 182,5,35136
Maav 152,188,128
Maav 152,192,148
buffer_load_dwordx4 a[176:179],v2,s[48:51],s76 offen sc1
Maav 156,188,132
M0 82,20384
Maav 156,192,152
WL 6
Maav 120,196,196
s_nop 0
TR 184,5,38848
TR 186,5,39360
Maav 120,200,156
DM 7,40,83,1152
Maav 124,196,200
M0 82,20288
Maav 124,200,160
Maav 128,196,204
DM 7,40,83,2304
Maav 128,200,164
M0 82,20192
TR 188,5,34656
TR 190,5,35168
Maav 132,196,208
Maav 132,200,168
DM 7,40,83,3456
Maav 136,196,212
s_add_u32 s83,s70,147456
Maav 136,200,172
WL 6
Maav 140,204,196
M0 82,37792
TR 192,5,38880
TR 194,5,39392
Maav 140,208,156
DM 7,40,83,0
Maav 144,204,200
M0 82,37696
Maav 144,208,160
Maav 148,204,204
DM 7,40,83,1152
Maav 148,208,164
M0 82,37600
TR 196,5,43072
TR 198,5,43584
Maav 152,204,208
Maav 152,208,168
DM 7,40,83,2304
Maav 156,204,212
M0 82,37504
Maav 156,208,172
WL 6
Maaa 120,180,0
s_nop 0
TR 200,5,47296
TR 202,5,47808
Maaa 120,184,20
DM 7,40,83,3456
Maaa 124,180,4
Maaa 124,184,24
Maaa 128,180,8
Maaa 128,184,28
s_nop 0
TR 204,5,43104
TR 206,5,43616
Maaa 132,180,12
Maaa 132,184,32
Maaa 136,180,16
Maaa 136,184,36
WL 6
Maaa 140,188,0
s_nop 0
TR 208,5,47328
TR 210,5,47840
Maaa 140,192,20
Maaa 144,188,4
Maaa 144,192,24
Maaa 148,188,8
Maaa 148,192,28
Maaa 152,188,12
Maaa 152,192,32
Maaa 156,188,16
Maaa 156,192,36
WL 4
Maaa 120,196,40
WL 0
s_barrier
Maaa 120,200,60
s_add_u32 s83,s70,294912
TR 180,4,0
TR 182,4,512
Maaa 124,196,44
M0 82,55104
TR 184,4,4224
TR 186,4,4736
Maaa 124,200,64
DM 7,40,83,0
Maaa 128,196,48
M0 82,55008
Maaa 128,200,68
s_nop 0
TR 188,4,32
TR 190,4,544
Maaa 132,196,52
DM 7,40,83,1152
Maaa 132,200,72
M0 82,54912
Maaa 136,196,56
Maaa 136,200,76
DM 7,40,83,2304
Maaa 140,204,40
M0 82,54816
TR 192,4,4256
TR 194,4,4768
Maaa 140,208,60
Maaa 144,204,44
DM 7,40,83,3456
Maaa 144,208,64
s_add_u32 s83,s70,442368
Maaa 148,204,48
M0 82,72416
Maaa 148,208,68
s_nop 0
TR 196,4,8448
TR 198,4,8960
Maaa 152,204,52
DM 7,40,83,0
Maaa 152,208,72
M0 82,72320
Maaa 156,204,56
Maaa 156,208,76
DM 7,40,83,1152
s_waitcnt vmcnt(17)
Mava 120,12,160
M0 82,72224
Mava 140,10,160
s_waitcnt vmcnt(16)
Mava 124,12,164
DM 7,40,83,2304
Mava 144,10,164
M0 82,72128
s_waitcnt vmcnt(16)
Mava 128,12,168
Mava 148,10,168
DM 7,40,83,3456
s_waitcnt vmcnt(16)
Mava 132,12,172
Mava 152,10,172
s_waitcnt vmcnt(15)
Mava 136,12,176
Mava 156,10,176
WL 6
Maavz 80,180,216
s_nop 0
TR 200,4,12672
TR 202,4,13184
Maav 80,184,236
buffer_load_dwordx2 v[24:25],off,s[56:59],s44 sc1
Maavz 84,180,220
s_add_u32 s76,s101,655360
Maav 84,184,240
AR 184,120
AR 185,121
Maavz 88,180,224
AR 186,122
AR 187,123
Maav 88,184,244
s_nop 0
TR 204,4,8480
TR 206,4,8992
buffer_load_dwordx4 a[160:163],v2,s[8:11],s76 offen sc1
Maavz 92,180,228
s_add_u32 s76,s101,819200
AR 188,140
AR 189,141
Maav 92,184,248
AR 190,142
AR 191,143
Maavz 96,180,232
CV 184,184,185
CV 185,186,187
Maav 96,184,252
WL 6
CV 186,188,189
CV 187,190,191
Maavz 100,188,216
s_add_u32 s23,s74,61440
TR 208,4,12704
TR 210,4,13216
Maav 100,192,236
buffer_store_dwordx4 v[184:187],v2,s[48:51],s23 offen sc1
Maavz 104,188,220
AR 176,124
AR 177,125
Maav 104,192,240
AR 178,126
AR 179,127
Maavz 108,188,224
AR 180,144
AR 181,145
Maav 108,192,244
s_nop 0
TR 180,6,0
TR 182,6,512
Maavz 112,188,228
AR 182,146
AR 183,147
Maav 112,192,248
CV 176,176,177
CV 177,178,179
Maavz 116,188,232
CV 178,180,181
CV 179,182,183
Maav 116,192,252
WL 6
buffer_load_dwordx4 a[164:167],v2,s[8:11],s76 offen sc1
Maaa 80,196,216
s_add_u32 s23,s74,62464
TR 184,6,4224
TR 186,6,4736
Maaa 80,200,236
buffer_store_dwordx4 v[176:179],v2,s[48:51],s23 offen sc1
Maaa 84,196,220
s_add_u32 s76,s101,983040
AR 184,128
AR 185,129
Maaa 84,200,240
AR 186,130
AR 187,131
Maaa 88,196,224
AR 188,148
AR 189,149
Maaa 88,200,244
s_nop 0
TR 188,6,32
TR 190,6,544
Maaa 92,196,228
AR 190,150
AR 191,151
Maaa 92,200,248
CV 184,184,185
CV 185,186,187
Maaa 96,196,232
CV 186,188,189
CV 187,190,191
Maaa 96,200,252
WL 6
buffer_load_dwordx4 a[168:171],v2,s[8:11],s76 offen sc1
Maaa 100,204,216
s_add_u32 s23,s74,63488
TR 192,6,4256
TR 194,6,4768
Maaa 100,208,236
buffer_store_dwordx4 v[184:187],v2,s[48:51],s23 offen sc1
Maaa 104,204,220
s_add_u32 s76,s101,1146880
AR 176,132
AR 177,133
Maaa 104,208,240
AR 178,134
AR 179,135
Maaa 108,204,224
AR 180,152
AR 181,153
Maaa 108,208,244
s_nop 0
TR 196,6,8448
TR 198,6,8960
Maaa 112,204,228
AR 182,154
AR 183,155
Maaa 112,208,248
CV 176,176,177
CV 177,178,179
Maaa 116,204,232
CV 178,180,181
CV 179,182,183
Maaa 116,208,252
WL 6
buffer_load_dwordx4 a[172:175],v2,s[8:11],s76 offen sc1
Maav 80,180,36
s_add_u32 s23,s74,64512
TR 200,6,12672
TR 202,6,13184
Maav 80,184,56
buffer_store_dwordx4 v[176:179],v2,s[48:51],s23 offen sc1
Maav 84,180,40
AR 184,136
AR 185,137
Maav 84,184,60
AR 186,138
AR 187,139
Maav 88,180,44
AR 188,156
AR 189,157
Maav 88,184,64
s_nop 0
TR 204,6,8480
TR 206,6,8992
Maav 92,180,48
AR 190,158
AR 191,159
Maav 92,184,68
CV 184,184,185
CV 185,186,187
Maav 96,180,52
CV 186,188,189
CV 187,190,191
Maav 96,184,72
WL 6
Maav 100,188,36
s_add_u32 s23,s74,65536
TR 208,6,12704
TR 210,6,13216
Maav 100,192,56
buffer_store_dwordx4 v[184:187],v2,s[48:51],s23 offen sc1
Maav 104,188,40
Maav 104,192,60
Maav 108,188,44
Maav 108,192,64
Maav 112,188,48
Maav 112,192,68
Maav 116,188,52
Maav 116,192,72
WL 4
Maav 80,196,76
WL 0
s_barrier
Maav 80,200,96
s_add_u32 s70,s68,6
TR 180,4,34624
TR 182,4,35136
Maav 84,196,80
s_and_b32 s70,s70,7
TR 184,4,38848
TR 186,4,39360
Maav 84,200,100
s_lshl_b32 s70,s70,7
Maav 88,196,84
s_add_u32 s83,s70,0
Maav 88,200,104
M0 82,89728
TR 188,4,34656
TR 190,4,35168
Maav 92,196,88
DM 7,40,83,0
Maav 92,200,108
M0 82,89632
Maav 96,196,92
Maav 96,200,112
DM 7,40,83,1152
Maav 100,204,76
M0 82,89536
TR 192,4,38880
TR 194,4,39392
Maav 100,208,96
Maav 104,204,80
DM 7,40,83,2304
Maav 104,208,100
M0 82,89440
Maav 108,204,84
Maav 108,208,104
s_nop 0
TR 196,4,43072
TR 198,4,43584
Maav 112,204,88
DM 7,40,83,3456
Maav 112,208,108
s_add_u32 s83,s70,147456
Maav 116,204,92
M0 82,107040
Maav 116,208,112
DM 7,40,83,0
s_waitcnt vmcnt(13)
Mvva 16,12,160
M0 82,106944
Mvva 20,10,160
s_waitcnt vmcnt(11)
Mvva 16,12,164
DM 7,40,83,1152
Mvva 20,10,164
M0 82,106848
s_waitcnt vmcnt(10)
Mvva 16,12,168
Mvva 20,10,168
DM 7,40,83,2304
s_waitcnt vmcnt(9)
Mvva 16,12,172
v_add_u32_e64 v194,s88,4
Mvva 20,10,172
WL 6
Maav 80,180,116
s_nop 0
TR 200,4,47296
TR 202,4,47808
Maav 80,184,136
s_add_u32 s16,s88,3
v_cmp_lt_u64 vcc,v[24:25],s[16:17]
v_cmp_gt_u64 s[20:21],v[24:25],s[18:19]
s_nop 0
s_or_b64 s[20:21],s[20:21],vcc
s_cbranch_scc1 LB_spin1
LB_sb1:
Maav 84,180,120
s_waitcnt vmcnt(7)
s_mov_b64 exec,1
buffer_store_dwordx2 v[194:195],off,s[56:59],s81 sc1
s_mov_b64 exec,-1
Maav 84,184,140
s_add_u32 s76,s72,40960
Maav 88,180,124
buffer_load_dwordx4 a[160:163],v2,s[48:51],s76 offen sc1
Maav 88,184,144
s_nop 0
TR 204,4,43104
TR 206,4,43616
Maav 92,180,128
Maav 92,184,148
buffer_load_dwordx4 a[164:167],v2,s[48:51],s76 offen offset:1024 sc1
Maav 96,180,132
Maav 96,184,152
WL 6
Maav 100,188,116
s_nop 0
TR 208,4,47328
TR 210,4,47840
Maav 100,192,136
buffer_load_dwordx4 a[168:171],v2,s[48:51],s76 offen offset:2048 sc1
Maav 104,188,120
Maav 104,192,140
Maav 108,188,124
buffer_load_dwordx4 a[172:175],v2,s[48:51],s76 offen offset:3072 sc1
Maav 108,192,144
s_add_u32 s76,s72,45056
TR 180,6,34624
TR 182,6,35136
Maav 112,188,128
Maav 112,192,148
buffer_load_dwordx4 a[176:179],v2,s[48:51],s76 offen sc1
Maav 116,188,132
M0 82,106752
Maav 116,192,152
WL 6
Maav 80,196,196
s_nop 0
TR 184,6,38848
TR 186,6,39360
Maav 80,200,156
DM 7,40,83,3456
Maav 84,196,200
Maav 84,200,160
Maav 88,196,204
Maav 88,200,164
s_nop 0
TR 188,6,34656
TR 190,6,35168
Maav 92,196,208
Maav 92,200,168
Maav 96,196,212
Maav 96,200,172
WL 6
Maav 100,204,196
s_nop 0
TR 192,6,38880
TR 194,6,39392
Maav 100,208,156
Maav 104,204,200
Maav 104,208,160
Maav 108,204,204
Maav 108,208,164
s_nop 0
TR 196,6,43072
TR 198,6,43584
Maav 112,204,208
Maav 112,208,168
Maav 116,204,212
Maav 116,208,172
WL 6
Maaa 80,180,0
s_nop 0
TR 200,6,47296
TR 202,6,47808
Maaa 80,184,20
Maaa 84,180,4
Maaa 84,184,24
Maaa 88,180,8
Maaa 88,184,28
s_nop 0
TR 204,6,43104
TR 206,6,43616
Maaa 92,180,12
Maaa 92,184,32
Maaa 96,180,16
Maaa 96,184,36
WL 6
Maaa 100,188,0
s_nop 0
TR 208,6,47328
TR 210,6,47840
Maaa 100,192,20
Maaa 104,188,4
Maaa 104,192,24
Maaa 108,188,8
Maaa 108,192,28
Maaa 112,188,12
Maaa 112,192,32
Maaa 116,188,16
Maaa 116,192,36
WL 4
Maaa 80,196,40
WL 0
s_barrier
Maaa 80,200,60
s_add_u32 s83,s70,294912
TR 180,3,0
TR 182,3,512
Maaa 84,196,44
M0 82,124352
TR 184,3,4224
TR 186,3,4736
Maaa 84,200,64
DM 7,40,83,0
Maaa 88,196,48
M0 82,124256
Maaa 88,200,68
s_nop 0
TR 188,3,32
TR 190,3,544
Maaa 92,196,52
DM 7,40,83,1152
Maaa 92,200,72
M0 82,124160
Maaa 96,196,56
Maaa 96,200,76
DM 7,40,83,2304
Maaa 100,204,40
M0 82,124064
TR 192,3,4256
TR 194,3,4768
Maaa 100,208,60
Maaa 104,204,44
DM 7,40,83,3456
Maaa 104,208,64
s_add_u32 s83,s70,442368
Maaa 108,204,48
M0 82,141664
Maaa 108,208,68
s_nop 0
TR 196,3,8448
TR 198,3,8960
Maaa 112,204,52
DM 7,40,83,0
Maaa 112,208,72
M0 82,141568
Maaa 116,204,56
Maaa 116,208,76
DM 7,40,83,1152
s_waitcnt vmcnt(11)
Mava 80,12,160
M0 82,141472
Mava 100,10,160
s_waitcnt vmcnt(10)
Mava 84,12,164
DM 7,40,83,2304
Mava 104,10,164
M0 82,141376
s_waitcnt vmcnt(10)
Mava 88,12,168
Mava 108,10,168
DM 7,40,83,3456
s_waitcnt vmcnt(10)
Mava 92,12,172
Mava 112,10,172
s_waitcnt vmcnt(9)
Mava 96,12,176
Mava 116,10,176
WL 6
Maavz 120,180,216
s_nop 0
TR 200,3,12672
TR 202,3,13184
Maav 120,184,236
buffer_load_dwordx2 v[24:25],off,s[56:59],s44 sc1
Maavz 124,180,220
Maav 124,184,240
AR 176,80
AR 177,81
Maavz 128,180,224
AR 178,82
AR 179,83
Maav 128,184,244
s_nop 0
TR 204,3,8480
TR 206,3,8992
Maavz 132,180,228
AR 180,100
AR 181,101
Maav 132,184,248
AR 182,102
AR 183,103
Maavz 136,180,232
CV 176,176,177
CV 177,178,179
Maav 136,184,252
WL 6
CV 178,180,181
CV 179,182,183
Maavz 140,188,216
s_add_u32 s23,s74,81920
TR 208,3,12704
TR 210,3,13216
Maav 140,192,236
buffer_store_dwordx4 v[176:179],v2,s[48:51],s23 offen sc1
Maavz 144,188,220
AR 184,84
AR 185,85
Maav 144,192,240
AR 186,86
AR 187,87
Maavz 148,188,224
AR 188,104
AR 189,105
Maav 148,192,244
s_nop 0
TR 180,5,0
TR 182,5,512
Maavz 152,188,228
AR 190,106
AR 191,107
Maav 152,192,248
CV 184,184,185
CV 185,186,187
Maavz 156,188,232
CV 186,188,189
CV 187,190,191
Maav 156,192,252
WL 6
Maaa 120,196,216
s_add_u32 s23,s74,82944
TR 184,5,4224
TR 186,5,4736
Maaa 120,200,236
buffer_store_dwordx4 v[184:187],v2,s[48:51],s23 offen sc1
Maaa 124,196,220
AR 176,88
AR 177,89
Maaa 124,200,240
AR 178,90
AR 179,91
Maaa 128,196,224
AR 180,108
AR 181,109
Maaa 128,200,244
s_nop 0
TR 188,5,32
TR 190,5,544
Maaa 132,196,228
AR 182,110
AR 183,111
Maaa 132,200,248
CV 176,176,177
CV 177,178,179
Maaa 136,196,232
CV 178,180,181
CV 179,182,183
Maaa 136,200,252
WL 6
Maaa 140,204,216
s_add_u32 s23,s74,83968
TR 192,5,4256
TR 194,5,4768
Maaa 140,208,236
buffer_store_dwordx4 v[176:179],v2,s[48:51],s23 offen sc1
Maaa 144,204,220
AR 184,92
AR 185,93
Maaa 144,208,240
AR 186,94
AR 187,95
Maaa 148,204,224
AR 188,112
AR 189,113
Maaa 148,208,244
s_nop 0
TR 196,5,8448
TR 198,5,8960
Maaa 152,204,228
AR 190,114
AR 191,115
Maaa 152,208,248
CV 184,184,185
CV 185,186,187
Maaa 156,204,232
CV 186,188,189
CV 187,190,191
Maaa 156,208,252
WL 6
Maav 120,180,36
s_add_u32 s23,s74,84992
TR 200,5,12672
TR 202,5,13184
Maav 120,184,56
buffer_store_dwordx4 v[184:187],v2,s[48:51],s23 offen sc1
Maav 124,180,40
AR 176,96
AR 177,97
Maav 124,184,60
AR 178,98
AR 179,99
Maav 128,180,44
AR 180,116
AR 181,117
Maav 128,184,64
s_nop 0
TR 204,5,8480
TR 206,5,8992
Maav 132,180,48
AR 182,118
AR 183,119
Maav 132,184,68
CV 176,176,177
CV 177,178,179
Maav 136,180,52
CV 178,180,181
CV 179,182,183
Maav 136,184,72
WL 6
Maav 140,188,36
s_add_u32 s23,s74,86016
TR 208,5,12704
TR 210,5,13216
Maav 140,192,56
buffer_store_dwordx4 v[176:179],v2,s[48:51],s23 offen sc1
Maav 144,188,40
Maav 144,192,60
Maav 148,188,44
Maav 148,192,64
Maav 152,188,48
Maav 152,192,68
Maav 156,188,52
Maav 156,192,72
WL 4
Maav 120,196,76
WL 0
s_barrier
Maav 120,200,96
s_nop 0
TR 180,3,34624
TR 182,3,35136
Maav 124,196,80
s_nop 0
TR 184,3,38848
TR 186,3,39360
Maav 124,200,100
s_add_u32 s16,s88,4
s_waitcnt vmcnt(5)
v_cmp_lt_u64 vcc,v[24:25],s[16:17]
v_cmp_gt_u64 s[20:21],v[24:25],s[18:19]
s_nop 0
s_or_b64 s[20:21],s[20:21],vcc
s_cbranch_scc1 LB_spin2
LB_sb2:
Maav 128,196,84
s_add_u32 s76,s72,61440
Maav 128,200,104
s_nop 0
TR 188,3,34656
TR 190,3,35168
Maav 132,196,88
buffer_load_dwordx4 a[160:163],v2,s[48:51],s76 offen sc1
Maav 132,200,108
Maav 136,196,92
Maav 136,200,112
buffer_load_dwordx4 a[164:167],v2,s[48:51],s76 offen offset:1024 sc1
Maav 140,204,76
s_nop 0
TR 192,3,38880
TR 194,3,39392
Maav 140,208,96
Maav 144,204,80
buffer_load_dwordx4 a[168:171],v2,s[48:51],s76 offen offset:2048 sc1
Maav 144,208,100
Maav 148,204,84
Maav 148,208,104
s_add_u32 s70,s68,7
TR 196,3,43072
TR 198,3,43584
Maav 152,204,88
buffer_load_dwordx4 a[172:175],v2,s[48:51],s76 offen offset:3072 sc1
Maav 152,208,108
s_add_u32 s76,s72,65536
Maav 156,204,92
s_and_b32 s70,s70,7
Maav 156,208,112
WL 6
buffer_load_dwordx4 a[176:179],v2,s[48:51],s76 offen sc1
Maav 120,180,116
s_lshl_b32 s70,s70,7
TR 200,3,47296
TR 202,3,47808
Maav 120,184,136
s_add_u32 s83,s70,0
M0 82,20480
Maav 124,180,120
DM 7,40,83,0
Maav 124,184,140
Maav 128,180,124
M0 82,20384
Maav 128,184,144
s_nop 0
TR 204,3,43104
TR 206,3,43616
Maav 132,180,128
v_add_u32_e64 v194,s88,5
Maav 132,184,148
DM 7,40,83,1152
Maav 136,180,132
Maav 136,184,152
WL 6
Maav 140,188,116
M0 82,20288
TR 208,3,47328
TR 210,3,47840
Maav 140,192,136
s_waitcnt vmcnt(7)
s_mov_b64 exec,1
buffer_store_dwordx2 v[194:195],off,s[56:59],s81 sc1
s_mov_b64 exec,-1
Maav 144,188,120
DM 7,40,83,2304
Maav 144,192,140
M0 82,20192
Maav 148,188,124
Maav 148,192,144
s_nop 0
TR 180,5,34624
TR 182,5,35136
Maav 152,188,128
DM 7,40,83,3456
Maav 152,192,148
s_add_u32 s83,s70,147456
Maav 156,188,132
M0 82,37792
Maav 156,192,152
WL 6
DM 7,40,83,0
Maav 120,196,196
M0 82,37696
TR 184,5,38848
TR 186,5,39360
Maav 120,200,156
Maav 124,196,200
DM 7,40,83,1152
Maav 124,200,160
M0 82,37600
Maav 128,196,204
Maav 128,200,164
s_nop 0
TR 188,5,34656
TR 190,5,35168
Maav 132,196,208
DM 7,40,83,2304
Maav 132,200,168
M0 82,37504
Maav 136,196,212
Maav 136,200,172
WL 6
DM 7,40,83,3456
Maav 140,204,196
s_nop 0
TR 192,5,38880
TR 194,5,39392
Maav 140,208,156
CV 16,16,17
CV 17,18,19
Maav 144,204,200
CV 18,20,21
CV 19,22,23
Maav 144,208,160
v_permlane16_swap_b32 v16,v18
v_permlane16_swap_b32 v17,v19
Maav 148,204,204
buffer_store_dwordx4 v[16:19],v192,s[12:15],s100 offen
Maav 148,208,164
s_nop 0
TR 196,5,43072
TR 198,5,43584
Maav 152,204,208
v_mov_b32 v194,s88
s_mov_b64 exec,1
buffer_store_dwordx2 v[194:195],off,s[56:59],0 offset:256 sc1
s_mov_b64 exec,-1
Maav 152,208,168
Maav 156,204,212
Maav 156,208,172
WL 6
Maaa 120,180,0
s_nop 0
TR 200,5,47296
TR 202,5,47808
Maaa 120,184,20
Maaa 124,180,4
Maaa 124,184,24
Maaa 128,180,8
Maaa 128,184,28
s_nop 0
TR 204,5,43104
TR 206,5,43616
Maaa 132,180,12
Maaa 132,184,32
Maaa 136,180,16
Maaa 136,184,36
WL 6
Maaa 140,188,0
s_nop 0
TR 208,5,47328
TR 210,5,47840
Maaa 140,192,20
Maaa 144,188,4
Maaa 144,192,24
Maaa 148,188,8
Maaa 148,192,28
Maaa 152,188,12
Maaa 152,192,32
Maaa 156,188,16
Maaa 156,192,36
WL 4
Maaa 120,196,40
WL 0
s_barrier
Maaa 120,200,60
s_add_u32 s83,s70,294912
TR 180,4,0
TR 182,4,512
Maaa 124,196,44
M0 82,55104
TR 184,4,4224
TR 186,4,4736
Maaa 124,200,64
DM 7,40,83,0
Maaa 128,196,48
M0 82,55008
Maaa 128,200,68
s_nop 0
TR 188,4,32
TR 190,4,544
Maaa 132,196,52
DM 7,40,83,1152
Maaa 132,200,72
M0 82,54912
Maaa 136,196,56
Maaa 136,200,76
DM 7,40,83,2304
Maaa 140,204,40
M0 82,54816
TR 192,4,4256
TR 194,4,4768
Maaa 140,208,60
Maaa 144,204,44
DM 7,40,83,3456
Maaa 144,208,64
s_add_u32 s83,s70,442368
Maaa 148,204,48
M0 82,72416
Maaa 148,208,68
s_nop 0
TR 196,4,8448
TR 198,4,8960
Maaa 152,204,52
DM 7,40,83,0
Maaa 152,208,72
M0 82,72320
Maaa 156,204,56
Maaa 156,208,76
DM 7,40,83,1152
s_waitcnt vmcnt(21)
Mava 120,12,160
M0 82,72224
Mava 140,10,160
s_waitcnt vmcnt(20)
Mava 124,12,164
DM 7,40,83,2304
Mava 144,10,164
M0 82,72128
s_waitcnt vmcnt(20)
Mava 128,12,168
Mava 148,10,168
DM 7,40,83,3456
s_waitcnt vmcnt(20)
Mava 132,12,172
Mava 152,10,172
s_waitcnt vmcnt(19)
Mava 136,12,176
Mava 156,10,176
WL 6
Maavz 80,180,216
s_nop 0
TR 200,4,12672
TR 202,4,13184
Maav 80,184,236
buffer_load_dwordx2 v[24:25],off,s[56:59],s44 sc1
Maavz 84,180,220
Maav 84,184,240
AR 184,120
AR 185,121
Maavz 88,180,224
AR 186,122
AR 187,123
Maav 88,184,244
s_nop 0
TR 204,4,8480
TR 206,4,8992
Maavz 92,180,228
AR 188,140
AR 189,141
Maav 92,184,248
AR 190,142
AR 191,143
Maavz 96,180,232
CV 184,184,185
CV 185,186,187
Maav 96,184,252
WL 6
CV 186,188,189
CV 187,190,191
Maavz 100,188,216
s_add_u32 s23,s74,102400
TR 208,4,12704
TR 210,4,13216
Maav 100,192,236
buffer_store_dwordx4 v[184:187],v2,s[48:51],s23 offen sc1
Maavz 104,188,220
AR 176,124
AR 177,125
Maav 104,192,240
AR 178,126
AR 179,127
Maavz 108,188,224
AR 180,144
AR 181,145
Maav 108,192,244
s_nop 0
TR 180,6,0
TR 182,6,512
Maavz 112,188,228
AR 182,146
AR 183,147
Maav 112,192,248
CV 176,176,177
CV 177,178,179
Maavz 116,188,232
CV 178,180,181
CV 179,182,183
Maav 116,192,252
WL 6
Maaa 80,196,216
s_add_u32 s23,s74,103424
TR 184,6,4224
TR 186,6,4736
Maaa 80,200,236
buffer_store_dwordx4 v[176:179],v2,s[48:51],s23 offen sc1
Maaa 84,196,220
AR 184,128
AR 185,129
Maaa 84,200,240
AR 186,130
AR 187,131
Maaa 88,196,224
AR 188,148
AR 189,149
Maaa 88,200,244
s_nop 0
TR 188,6,32
TR 190,6,544
Maaa 92,196,228
AR 190,150
AR 191,151
Maaa 92,200,248
CV 184,184,185
CV 185,186,187
Maaa 96,196,232
CV 186,188,189
CV 187,190,191
Maaa 96,200,252
WL 6
Maaa 100,204,216
s_add_u32 s23,s74,104448
TR 192,6,4256
TR 194,6,4768
Maaa 100,208,236
buffer_store_dwordx4 v[184:187],v2,s[48:51],s23 offen sc1
Maaa 104,204,220
AR 176,132
AR 177,133
Maaa 104,208,240
AR 178,134
AR 179,135
Maaa 108,204,224
AR 180,152
AR 181,153
Maaa 108,208,244
s_nop 0
TR 196,6,8448
TR 198,6,8960
Maaa 112,204,228
AR 182,154
AR 183,155
Maaa 112,208,248
CV 176,176,177
CV 177,178,179
Maaa 116,204,232
CV 178,180,181
CV 179,182,183
Maaa 116,208,252
WL 6
Maav 80,180,36
s_add_u32 s23,s74,105472
TR 200,6,12672
TR 202,6,13184
Maav 80,184,56
buffer_store_dwordx4 v[176:179],v2,s[48:51],s23 offen sc1
Maav 84,180,40
AR 184,136
AR 185,137
Maav 84,184,60
AR 186,138
AR 187,139
Maav 88,180,44
AR 188,156
AR 189,157
Maav 88,184,64
s_nop 0
TR 204,6,8480
TR 206,6,8992
Maav 92,180,48
AR 190,158
AR 191,159
Maav 92,184,68
CV 184,184,185
CV 185,186,187
Maav 96,180,52
CV 186,188,189
CV 187,190,191
Maav 96,184,72
WL 6
Maav 100,188,36
s_add_u32 s23,s74,106496
TR 208,6,12704
TR 210,6,13216
Maav 100,192,56
buffer_store_dwordx4 v[184:187],v2,s[48:51],s23 offen sc1
Maav 104,188,40
Maav 104,192,60
Maav 108,188,44
Maav 108,192,64
Maav 112,188,48
Maav 112,192,68
Maav 116,188,52
Maav 116,192,72
WL 4
Maav 80,196,76
WL 0
s_barrier
Maav 80,200,96
s_nop 0
TR 180,4,34624
TR 182,4,35136
Maav 84,196,80
s_nop 0
TR 184,4,38848
TR 186,4,39360
Maav 84,200,100
s_add_u32 s16,s88,5
s_waitcnt vmcnt(5)
v_cmp_lt_u64 vcc,v[24:25],s[16:17]
v_cmp_gt_u64 s[20:21],v[24:25],s[18:19]
s_nop 0
s_or_b64 s[20:21],s[20:21],vcc
s_cbranch_scc1 LB_spin3
LB_sb3:
Maav 88,196,84
s_add_u32 s76,s72,81920
Maav 88,200,104
s_nop 0
TR 188,4,34656
TR 190,4,35168
Maav 92,196,88
buffer_load_dwordx4 a[160:163],v2,s[48:51],s76 offen sc1
Maav 92,200,108
Maav 96,196,92
Maav 96,200,112
buffer_load_dwordx4 a[164:167],v2,s[48:51],s76 offen offset:1024 sc1
Maav 100,204,76
s_nop 0
TR 192,4,38880
TR 194,4,39392
Maav 100,208,96
Maav 104,204,80
buffer_load_dwordx4 a[168:171],v2,s[48:51],s76 offen offset:2048 sc1
Maav 104,208,100
Maav 108,204,84
Maav 108,208,104
s_add_u32 s70,s68,8
TR 196,4,43072
TR 198,4,43584
Maav 112,204,88
buffer_load_dwordx4 a[172:175],v2,s[48:51],s76 offen offset:3072 sc1
Maav 112,208,108
s_add_u32 s76,s72,86016
Maav 116,204,92
s_and_b32 s70,s70,7
Maav 116,208,112
WL 6
buffer_load_dwordx4 a[176:179],v2,s[48:51],s76 offen sc1
Maav 80,180,116
s_lshl_b32 s70,s70,7
TR 200,4,47296
TR 202,4,47808
Maav 80,184,136
s_add_u32 s83,s70,0
M0 82,89728
Maav 84,180,120
DM 7,40,83,0
Maav 84,184,140
Maav 88,180,124
M0 82,89632
Maav 88,184,144
s_nop 0
TR 204,4,43104
TR 206,4,43616
Maav 92,180,128
v_add_u32_e64 v194,s88,6
Maav 92,184,148
DM 7,40,83,1152
Maav 96,180,132
Maav 96,184,152
WL 6
Maav 100,188,116
M0 82,89536
TR 208,4,47328
TR 210,4,47840
Maav 100,192,136
s_waitcnt vmcnt(7)
s_mov_b64 exec,1
buffer_store_dwordx2 v[194:195],off,s[56:59],s81 sc1
s_mov_b64 exec,-1
Maav 104,188,120
DM 7,40,83,2304
Maav 104,192,140
M0 82,89440
Maav 108,188,124
Maav 108,192,144
s_nop 0
TR 180,6,34624
TR 182,6,35136
Maav 112,188,128
DM 7,40,83,3456
Maav 112,192,148
s_add_u32 s83,s70,147456
Maav 116,188,132
M0 82,107040
Maav 116,192,152
WL 6
DM 7,40,83,0
Maav 80,196,196
M0 82,106944
TR 184,6,38848
TR 186,6,39360
Maav 80,200,156
Maav 84,196,200
DM 7,40,83,1152
Maav 84,200,160
M0 82,106848
Maav 88,196,204
Maav 88,200,164
s_nop 0
TR 188,6,34656
TR 190,6,35168
Maav 92,196,208
DM 7,40,83,2304
Maav 92,200,168
M0 82,106752
Maav 96,196,212
Maav 96,200,172
WL 6
DM 7,40,83,3456
Maav 100,204,196
s_nop 0
TR 192,6,38880
TR 194,6,39392
Maav 100,208,156
Maav 104,204,200
Maav 104,208,160
Maav 108,204,204
Maav 108,208,164
s_nop 0
TR 196,6,43072
TR 198,6,43584
Maav 112,204,208
Maav 112,208,168
Maav 116,204,212
Maav 116,208,172
WL 6
Maaa 80,180,0
s_nop 0
TR 200,6,47296
TR 202,6,47808
Maaa 80,184,20
Maaa 84,180,4
Maaa 84,184,24
Maaa 88,180,8
Maaa 88,184,28
s_nop 0
TR 204,6,43104
TR 206,6,43616
Maaa 92,180,12
Maaa 92,184,32
Maaa 96,180,16
Maaa 96,184,36
WL 6
Maaa 100,188,0
s_nop 0
TR 208,6,47328
TR 210,6,47840
Maaa 100,192,20
Maaa 104,188,4
Maaa 104,192,24
Maaa 108,188,8
Maaa 108,192,28
Maaa 112,188,12
Maaa 112,192,32
Maaa 116,188,16
Maaa 116,192,36
WL 4
Maaa 80,196,40
WL 0
s_barrier
Maaa 80,200,60
s_add_u32 s83,s70,294912
TR 180,3,0
TR 182,3,512
Maaa 84,196,44
M0 82,124352
TR 184,3,4224
TR 186,3,4736
Maaa 84,200,64
DM 7,40,83,0
Maaa 88,196,48
M0 82,124256
Maaa 88,200,68
s_nop 0
TR 188,3,32
TR 190,3,544
Maaa 92,196,52
DM 7,40,83,1152
Maaa 92,200,72
M0 82,124160
Maaa 96,196,56
Maaa 96,200,76
DM 7,40,83,2304
Maaa 100,204,40
M0 82,124064
TR 192,3,4256
TR 194,3,4768
Maaa 100,208,60
Maaa 104,204,44
DM 7,40,83,3456
Maaa 104,208,64
s_add_u32 s83,s70,442368
Maaa 108,204,48
M0 82,141664
Maaa 108,208,68
s_nop 0
TR 196,3,8448
TR 198,3,8960
Maaa 112,204,52
DM 7,40,83,0
Maaa 112,208,72
M0 82,141568
Maaa 116,204,56
Maaa 116,208,76
DM 7,40,83,1152
s_waitcnt vmcnt(19)
Mava 80,12,160
M0 82,141472
Mava 100,10,160
s_waitcnt vmcnt(18)
Mava 84,12,164
DM 7,40,83,2304
Mava 104,10,164
M0 82,141376
s_waitcnt vmcnt(18)
Mava 88,12,168
Mava 108,10,168
DM 7,40,83,3456
s_waitcnt vmcnt(18)
Mava 92,12,172
Mava 112,10,172
s_waitcnt vmcnt(17)
Mava 96,12,176
Mava 116,10,176
WL 6
Maavz 120,180,216
s_nop 0
TR 200,3,12672
TR 202,3,13184
Maav 120,184,236
buffer_load_dwordx2 v[24:25],off,s[56:59],s44 sc1
Maavz 124,180,220
Maav 124,184,240
AR 176,80
AR 177,81
Maavz 128,180,224
AR 178,82
AR 179,83
Maav 128,184,244
s_nop 0
TR 204,3,8480
TR 206,3,8992
Maavz 132,180,228
AR 180,100
AR 181,101
Maav 132,184,248
AR 182,102
AR 183,103
Maavz 136,180,232
CV 176,176,177
CV 177,178,179
Maav 136,184,252
WL 6
CV 178,180,181
CV 179,182,183
Maavz 140,188,216
s_add_u32 s23,s74,122880
TR 208,3,12704
TR 210,3,13216
Maav 140,192,236
buffer_store_dwordx4 v[176:179],v2,s[48:51],s23 offen sc1
Maavz 144,188,220
AR 184,84
AR 185,85
Maav 144,192,240
AR 186,86
AR 187,87
Maavz 148,188,224
AR 188,104
AR 189,105
Maav 148,192,244
s_nop 0
TR 180,5,0
TR 182,5,512
Maavz 152,188,228
AR 190,106
AR 191,107
Maav 152,192,248
CV 184,184,185
CV 185,186,187
Maavz 156,188,232
CV 186,188,189
CV 187,190,191
Maav 156,192,252
WL 6
Maaa 120,196,216
s_add_u32 s23,s74,123904
TR 184,5,4224
TR 186,5,4736
Maaa 120,200,236
buffer_store_dwordx4 v[184:187],v2,s[48:51],s23 offen sc1
Maaa 124,196,220
AR 176,88
AR 177,89
Maaa 124,200,240
AR 178,90
AR 179,91
Maaa 128,196,224
AR 180,108
AR 181,109
Maaa 128,200,244
s_nop 0
TR 188,5,32
TR 190,5,544
Maaa 132,196,228
AR 182,110
AR 183,111
Maaa 132,200,248
CV 176,176,177
CV 177,178,179
Maaa 136,196,232
CV 178,180,181
CV 179,182,183
Maaa 136,200,252
WL 6
Maaa 140,204,216
s_add_u32 s23,s74,124928
TR 192,5,4256
TR 194,5,4768
Maaa 140,208,236
buffer_store_dwordx4 v[176:179],v2,s[48:51],s23 offen sc1
Maaa 144,204,220
AR 184,92
AR 185,93
Maaa 144,208,240
AR 186,94
AR 187,95
Maaa 148,204,224
AR 188,112
AR 189,113
Maaa 148,208,244
s_nop 0
TR 196,5,8448
TR 198,5,8960
Maaa 152,204,228
AR 190,114
AR 191,115
Maaa 152,208,248
CV 184,184,185
CV 185,186,187
Maaa 156,204,232
CV 186,188,189
CV 187,190,191
Maaa 156,208,252
WL 6
Maav 120,180,36
s_add_u32 s23,s74,125952
TR 200,5,12672
TR 202,5,13184
Maav 120,184,56
buffer_store_dwordx4 v[184:187],v2,s[48:51],s23 offen sc1
Maav 124,180,40
AR 176,96
AR 177,97
Maav 124,184,60
AR 178,98
AR 179,99
Maav 128,180,44
AR 180,116
AR 181,117
Maav 128,184,64
s_nop 0
TR 204,5,8480
TR 206,5,8992
Maav 132,180,48
AR 182,118
AR 183,119
Maav 132,184,68
CV 176,176,177
CV 177,178,179
Maav 136,180,52
CV 178,180,181
CV 179,182,183
Maav 136,184,72
WL 6
Maav 140,188,36
s_add_u32 s23,s74,126976
TR 208,5,12704
TR 210,5,13216
Maav 140,192,56
buffer_store_dwordx4 v[176:179],v2,s[48:51],s23 offen sc1
Maav 144,188,40
Maav 144,192,60
Maav 148,188,44
Maav 148,192,64
Maav 152,188,48
Maav 152,192,68
Maav 156,188,52
Maav 156,192,72
WL 4
Maav 120,196,76
WL 0
s_barrier
Maav 120,200,96
s_nop 0
TR 180,3,34624
TR 182,3,35136
Maav 124,196,80
s_nop 0
TR 184,3,38848
TR 186,3,39360
Maav 124,200,100
s_add_u32 s16,s88,6
s_waitcnt vmcnt(5)
v_cmp_lt_u64 vcc,v[24:25],s[16:17]
v_cmp_gt_u64 s[20:21],v[24:25],s[18:19]
s_nop 0
s_or_b64 s[20:21],s[20:21],vcc
s_cbranch_scc1 LB_spin4
LB_sb4:
Maav 128,196,84
s_add_u32 s76,s72,102400
Maav 128,200,104
s_nop 0
TR 188,3,34656
TR 190,3,35168
Maav 132,196,88
buffer_load_dwordx4 a[160:163],v2,s[48:51],s76 offen sc1
Maav 132,200,108
Maav 136,196,92
Maav 136,200,112
buffer_load_dwordx4 a[164:167],v2,s[48:51],s76 offen offset:1024 sc1
Maav 140,204,76
s_nop 0
TR 192,3,38880
TR 194,3,39392
Maav 140,208,96
Maav 144,204,80
buffer_load_dwordx4 a[168:171],v2,s[48:51],s76 offen offset:2048 sc1
Maav 144,208,100
Maav 148,204,84
Maav 148,208,104
s_nop 0
TR 196,3,43072
TR 198,3,43584
Maav 152,204,88
buffer_load_dwordx4 a[172:175],v2,s[48:51],s76 offen offset:3072 sc1
Maav 152,208,108
s_add_u32 s76,s72,106496
Maav 156,204,92
Maav 156,208,112
WL 6
buffer_load_dwordx4 a[176:179],v2,s[48:51],s76 offen sc1
Maav 120,180,116
s_nop 0
TR 200,3,47296
TR 202,3,47808
Maav 120,184,136
Maav 124,180,120
Maav 124,184,140
Maav 128,180,124
Maav 128,184,144
s_nop 0
TR 204,3,43104
TR 206,3,43616
Maav 132,180,128
v_add_u32_e64 v194,s88,7
Maav 132,184,148
Maav 136,180,132
Maav 136,184,152
WL 6
Maav 140,188,116
s_nop 0
TR 208,3,47328
TR 210,3,47840
Maav 140,192,136
s_waitcnt vmcnt(5)
s_mov_b64 exec,1
buffer_store_dwordx2 v[194:195],off,s[56:59],s81 sc1
s_mov_b64 exec,-1
Maav 144,188,120
Maav 144,192,140
Maav 148,188,124
Maav 148,192,144
s_nop 0
TR 180,5,34624
TR 182,5,35136
Maav 152,188,128
Maav 152,192,148
Maav 156,188,132
Maav 156,192,152
WL 6
Maav 120,196,196
s_nop 0
TR 184,5,38848
TR 186,5,39360
Maav 120,200,156
Maav 124,196,200
Maav 124,200,160
Maav 128,196,204
Maav 128,200,164
s_nop 0
TR 188,5,34656
TR 190,5,35168
Maav 132,196,208
Maav 132,200,168
Maav 136,196,212
Maav 136,200,172
WL 6
Maav 140,204,196
s_nop 0
TR 192,5,38880
TR 194,5,39392
Maav 140,208,156
Maav 144,204,200
Maav 144,208,160
Maav 148,204,204
Maav 148,208,164
s_nop 0
TR 196,5,43072
TR 198,5,43584
Maav 152,204,208
Maav 152,208,168
Maav 156,204,212
Maav 156,208,172
WL 6
Maaa 120,180,0
s_nop 0
TR 200,5,47296
TR 202,5,47808
Maaa 120,184,20
Maaa 124,180,4
Maaa 124,184,24
Maaa 128,180,8
Maaa 128,184,28
s_nop 0
TR 204,5,43104
TR 206,5,43616
Maaa 132,180,12
Maaa 132,184,32
Maaa 136,180,16
Maaa 136,184,36
WL 6
Maaa 140,188,0
s_nop 0
TR 208,5,47328
TR 210,5,47840
Maaa 140,192,20
Maaa 144,188,4
Maaa 144,192,24
Maaa 148,188,8
Maaa 148,192,28
Maaa 152,188,12
Maaa 152,192,32
Maaa 156,188,16
Maaa 156,192,36
WL 4
Maaa 120,196,40
WL 0
s_barrier
Maaa 120,200,60
s_nop 0
TR 180,4,0
TR 182,4,512
Maaa 124,196,44
s_nop 0
TR 184,4,4224
TR 186,4,4736
Maaa 124,200,64
Maaa 128,196,48
Maaa 128,200,68
s_nop 0
TR 188,4,32
TR 190,4,544
Maaa 132,196,52
Maaa 132,200,72
Maaa 136,196,56
Maaa 136,200,76
Maaa 140,204,40
s_nop 0
TR 192,4,4256
TR 194,4,4768
Maaa 140,208,60
Maaa 144,204,44
Maaa 144,208,64
Maaa 148,204,48
Maaa 148,208,68
s_nop 0
TR 196,4,8448
TR 198,4,8960
Maaa 152,204,52
Maaa 152,208,72
Maaa 156,204,56
Maaa 156,208,76
s_waitcnt vmcnt(5)
Mava 120,12,160
Mava 140,10,160
s_waitcnt vmcnt(4)
Mava 124,12,164
Mava 144,10,164
s_waitcnt vmcnt(3)
Mava 128,12,168
Mava 148,10,168
s_waitcnt vmcnt(2)
Mava 132,12,172
Mava 152,10,172
s_waitcnt vmcnt(1)
Mava 136,12,176
Mava 156,10,176
WL 6
Maavz 80,180,216
s_nop 0
TR 200,4,12672
TR 202,4,13184
Maav 80,184,236
buffer_load_dwordx2 v[24:25],off,s[56:59],s44 sc1
Maavz 84,180,220
Maav 84,184,240
AR 184,120
AR 185,121
Maavz 88,180,224
AR 186,122
AR 187,123
Maav 88,184,244
s_nop 0
TR 204,4,8480
TR 206,4,8992
Maavz 92,180,228
AR 188,140
AR 189,141
Maav 92,184,248
AR 190,142
AR 191,143
Maavz 96,180,232
CV 184,184,185
CV 185,186,187
Maav 96,184,252
WL 6
CV 186,188,189
CV 187,190,191
Maavz 100,188,216
s_add_u32 s23,s74,143360
TR 208,4,12704
TR 210,4,13216
Maav 100,192,236
buffer_store_dwordx4 v[184:187],v2,s[48:51],s23 offen sc1
Maavz 104,188,220
AR 176,124
AR 177,125
Maav 104,192,240
AR 178,126
AR 179,127
Maavz 108,188,224
AR 180,144
AR 181,145
Maav 108,192,244
s_nop 0
TR 180,6,0
TR 182,6,512
Maavz 112,188,228
AR 182,146
AR 183,147
Maav 112,192,248
CV 176,176,177
CV 177,178,179
Maavz 116,188,232
CV 178,180,181
CV 179,182,183
Maav 116,192,252
WL 6
Maaa 80,196,216
s_add_u32 s23,s74,144384
TR 184,6,4224
TR 186,6,4736
Maaa 80,200,236
buffer_store_dwordx4 v[176:179],v2,s[48:51],s23 offen sc1
Maaa 84,196,220
AR 184,128
AR 185,129
Maaa 84,200,240
AR 186,130
AR 187,131
Maaa 88,196,224
AR 188,148
AR 189,149
Maaa 88,200,244
s_nop 0
TR 188,6,32
TR 190,6,544
Maaa 92,196,228
AR 190,150
AR 191,151
Maaa 92,200,248
CV 184,184,185
CV 185,186,187
Maaa 96,196,232
CV 186,188,189
CV 187,190,191
Maaa 96,200,252
WL 6
Maaa 100,204,216
s_add_u32 s23,s74,145408
TR 192,6,4256
TR 194,6,4768
Maaa 100,208,236
buffer_store_dwordx4 v[184:187],v2,s[48:51],s23 offen sc1
Maaa 104,204,220
AR 176,132
AR 177,133
Maaa 104,208,240
AR 178,134
AR 179,135
Maaa 108,204,224
AR 180,152
AR 181,153
Maaa 108,208,244
s_nop 0
TR 196,6,8448
TR 198,6,8960
Maaa 112,204,228
AR 182,154
AR 183,155
Maaa 112,208,248
CV 176,176,177
CV 177,178,179
Maaa 116,204,232
CV 178,180,181
CV 179,182,183
Maaa 116,208,252
WL 6
Maav 80,180,36
s_add_u32 s23,s74,146432
TR 200,6,12672
TR 202,6,13184
Maav 80,184,56
buffer_store_dwordx4 v[176:179],v2,s[48:51],s23 offen sc1
Maav 84,180,40
AR 184,136
AR 185,137
Maav 84,184,60
AR 186,138
AR 187,139
Maav 88,180,44
AR 188,156
AR 189,157
Maav 88,184,64
s_nop 0
TR 204,6,8480
TR 206,6,8992
Maav 92,180,48
AR 190,158
AR 191,159
Maav 92,184,68
CV 184,184,185
CV 185,186,187
Maav 96,180,52
CV 186,188,189
CV 187,190,191
Maav 96,184,72
WL 6
Maav 100,188,36
s_add_u32 s23,s74,147456
TR 208,6,12704
TR 210,6,13216
Maav 100,192,56
buffer_store_dwordx4 v[184:187],v2,s[48:51],s23 offen sc1
Maav 104,188,40
Maav 104,192,60
Maav 108,188,44
Maav 108,192,64
Maav 112,188,48
Maav 112,192,68
Maav 116,188,52
Maav 116,192,72
WL 4
Maav 80,196,76
WL 0
s_barrier
Maav 80,200,96
s_nop 0
TR 180,4,34624
TR 182,4,35136
Maav 84,196,80
s_nop 0
TR 184,4,38848
TR 186,4,39360
Maav 84,200,100
s_add_u32 s16,s88,7
s_waitcnt vmcnt(5)
v_cmp_lt_u64 vcc,v[24:25],s[16:17]
v_cmp_gt_u64 s[20:21],v[24:25],s[18:19]
s_nop 0
s_or_b64 s[20:21],s[20:21],vcc
s_cbranch_scc1 LB_spin5
LB_sb5:
Maav 88,196,84
s_add_u32 s76,s72,122880
Maav 88,200,104
s_nop 0
TR 188,4,34656
TR 190,4,35168
Maav 92,196,88
buffer_load_dwordx4 a[160:163],v2,s[48:51],s76 offen sc1
Maav 92,200,108
Maav 96,196,92
Maav 96,200,112
buffer_load_dwordx4 a[164:167],v2,s[48:51],s76 offen offset:1024 sc1
Maav 100,204,76
s_nop 0
TR 192,4,38880
TR 194,4,39392
Maav 100,208,96
Maav 104,204,80
buffer_load_dwordx4 a[168:171],v2,s[48:51],s76 offen offset:2048 sc1
Maav 104,208,100
Maav 108,204,84
Maav 108,208,104
s_nop 0
TR 196,4,43072
TR 198,4,43584
Maav 112,204,88
buffer_load_dwordx4 a[172:175],v2,s[48:51],s76 offen offset:3072 sc1
Maav 112,208,108
s_add_u32 s76,s72,126976
Maav 116,204,92
Maav 116,208,112
WL 6
buffer_load_dwordx4 a[176:179],v2,s[48:51],s76 offen sc1
Maav 80,180,116
s_nop 0
TR 200,4,47296
TR 202,4,47808
Maav 80,184,136
Maav 84,180,120
Maav 84,184,140
Maav 88,180,124
Maav 88,184,144
s_nop 0
TR 204,4,43104
TR 206,4,43616
Maav 92,180,128
v_add_u32_e64 v194,s88,8
Maav 92,184,148
Maav 96,180,132
Maav 96,184,152
WL 6
Maav 100,188,116
s_nop 0
TR 208,4,47328
TR 210,4,47840
Maav 100,192,136
s_waitcnt vmcnt(5)
s_mov_b64 exec,1
buffer_store_dwordx2 v[194:195],off,s[56:59],s81 sc1
s_mov_b64 exec,-1
Maav 104,188,120
Maav 104,192,140
Maav 108,188,124
Maav 108,192,144
s_nop 0
TR 180,6,34624
TR 182,6,35136
Maav 112,188,128
Maav 112,192,148
Maav 116,188,132
Maav 116,192,152
WL 6
Maav 80,196,196
s_nop 0
TR 184,6,38848
TR 186,6,39360
Maav 80,200,156
buffer_load_dwordx2 v[24:25],off,s[56:59],s45 sc1
Maav 84,196,200
Maav 84,200,160
Maav 88,196,204
Maav 88,200,164
s_nop 0
TR 188,6,34656
TR 190,6,35168
Maav 92,196,208
Maav 92,200,168
Maav 96,196,212
Maav 96,200,172
WL 6
Maav 100,204,196
s_nop 0
TR 192,6,38880
TR 194,6,39392
Maav 100,208,156
Maav 104,204,200
Maav 104,208,160
Maav 108,204,204
Maav 108,208,164
s_nop 0
TR 196,6,43072
TR 198,6,43584
Maav 112,204,208
Maav 112,208,168
Maav 116,204,212
Maav 116,208,172
WL 6
Maaa 80,180,0
s_nop 0
TR 200,6,47296
TR 202,6,47808
Maaa 80,184,20
Maaa 84,180,4
Maaa 84,184,24
Maaa 88,180,8
Maaa 88,184,28
s_nop 0
TR 204,6,43104
TR 206,6,43616
Maaa 92,180,12
Maaa 92,184,32
Maaa 96,180,16
Maaa 96,184,36
WL 6
Maaa 100,188,0
s_nop 0
TR 208,6,47328
TR 210,6,47840
Maaa 100,192,20
s_add_u32 s16,s88,8
s_waitcnt vmcnt(0)
v_cmp_lt_u64 vcc,v[24:25],s[16:17]
v_cmp_gt_u64 s[20:21],v[24:25],s[18:19]
s_nop 0
s_or_b64 s[20:21],s[20:21],vcc
s_cbranch_scc1 LB_spin6
LB_sb6:
Maaa 104,188,4
s_add_u32 s76,s73,143360
Maaa 104,192,24
buffer_load_dwordx4 v[216:219],v2,s[48:51],s76 offen sc1
Maaa 108,188,8
Maaa 108,192,28
Maaa 112,188,12
buffer_load_dwordx4 v[220:223],v2,s[48:51],s76 offen offset:1024 sc1
Maaa 112,192,32
Maaa 116,188,16
Maaa 116,192,36
WL 4
buffer_load_dwordx4 v[224:227],v2,s[48:51],s76 offen offset:2048 sc1
Maaa 80,196,40
Maaa 80,200,60
Maaa 84,196,44
buffer_load_dwordx4 v[228:231],v2,s[48:51],s76 offen offset:3072 sc1
Maaa 84,200,64
s_add_u32 s76,s73,147456
Maaa 88,196,48
Maaa 88,200,68
buffer_load_dwordx4 v[232:235],v2,s[48:51],s76 offen sc1
Maaa 92,196,52
Maaa 92,200,72
Maaa 96,196,56
Maaa 96,200,76
WL 0
Maaa 100,204,40
Maaa 100,208,60
Maaa 104,204,44
Maaa 104,208,64
Maaa 108,204,48
Maaa 108,208,68
Maaa 112,204,52
Maaa 112,208,72
Maaa 116,204,56
Maaa 116,208,76
Mava 80,12,160
Mava 100,10,160
Mava 84,12,164
Mava 104,10,164
Mava 88,12,168
Mava 108,10,168
Mava 92,12,172
Mava 112,10,172
Mava 96,12,176
Mava 116,10,176
s_waitcnt vmcnt(4)
Mavv 80,12,216
Mavv 100,10,216
s_waitcnt vmcnt(3)
Mavv 84,12,220
Mavv 104,10,220
s_waitcnt vmcnt(2)
Mavv 88,12,224
Mavv 108,10,224
s_waitcnt vmcnt(1)
Mavv 92,12,228
Mavv 112,10,228
s_waitcnt vmcnt(0)
Mavv 96,12,232
Mavv 116,10,232
AR 116,80
AR 117,81
AR 118,82
AR 119,83
AR 120,100
AR 121,101
AR 122,102
AR 123,103
CV 116,116,117
CV 117,118,119
CV 118,120,121
CV 119,122,123
s_add_u32 s23,s47,0
v_permlane16_swap_b32 v116,v118
v_permlane16_swap_b32 v117,v119
buffer_store_dwordx4 v[116:119],v192,s[52:55],s23 offen
AR 124,84
AR 125,85
AR 126,86
AR 127,87
AR 128,104
AR 129,105
AR 130,106
AR 131,107
CV 124,124,125
CV 125,126,127
CV 126,128,129
CV 127,130,131
s_add_u32 s23,s47,18432
v_permlane16_swap_b32 v124,v126
v_permlane16_swap_b32 v125,v127
buffer_store_dwordx4 v[124:127],v192,s[52:55],s23 offen
AR 132,88
AR 133,89
AR 134,90
AR 135,91
AR 136,108
AR 137,109
AR 138,110
AR 139,111
CV 132,132,133
CV 133,134,135
CV 134,136,137
CV 135,138,139
s_add_u32 s23,s47,36864
v_permlane16_swap_b32 v132,v134
v_permlane16_swap_b32 v133,v135
buffer_store_dwordx4 v[132:135],v192,s[52:55],s23 offen
AR 140,92
AR 141,93
AR 142,94
AR 143,95
AR 144,112
AR 145,113
AR 146,114
AR 147,115
CV 140,140,141
CV 141,142,143
CV 142,144,145
CV 143,146,147
s_add_u32 s23,s47,55296
v_permlane16_swap_b32 v140,v142
v_permlane16_swap_b32 v141,v143
buffer_store_dwordx4 v[140:143],v192,s[52:55],s23 offen
AR 148,96
AR 149,97
AR 150,98
AR 151,99
AR 152,116
AR 153,117
AR 154,118
AR 155,119
CV 148,148,149
CV 149,150,151
CV 150,152,153
CV 151,154,155
s_add_u32 s23,s47,73728
v_permlane16_swap_b32 v148,v150
v_permlane16_swap_b32 v149,v151
buffer_store_dwordx4 v[148:151],v192,s[52:55],s23 offen
s_endpgm
LB_safe0:
s_nop 7
s_mov_b32 s93,m0
s_waitcnt vmcnt(0) lgkmcnt(0)
v_mov_b32 v22,0xff800000
v_mov_b32 v23,0xff800000
v_mov_b32 v24,0xff800000
v_mov_b32 v25,0xff800000
v_mov_b32 v26,0xff800000
v_subrev_u32 v27,20480,v10
s_mov_b32 s90,0
s_mov_b32 s91,16
LB_sloop0:
s_barrier
s_add_u32 s92,s90,131072
M0 78,0
s_nop 0
DM 19,32,90,0
M0 78,4224
s_nop 0
DM 33,32,90,0
M0 78,8448
s_nop 0
DM 19,32,92,0
M0 78,12672
s_nop 0
DM 33,32,92,0
s_waitcnt vmcnt(0)
s_barrier
RA 40,27,0
RA 44,27,64
RA 48,27,128
RA 52,27,192
RA 56,27,512
RA 60,27,576
RA 64,27,640
RA 68,27,704
WL 0
Mvavz 156,40,36
Mvavz 160,40,52
Mvavz 164,40,68
Mvavz 168,40,84
Mvavz 172,40,100
Mvav 156,44,40
Mvav 160,44,56
Mvav 164,44,72
Mvav 168,44,88
Mvav 172,44,104
Mvav 156,48,44
Mvav 160,48,60
Mvav 164,48,76
Mvav 168,48,92
Mvav 172,48,108
Mvav 156,52,48
Mvav 160,52,64
Mvav 164,52,80
Mvav 168,52,96
Mvav 172,52,112
Mvavz 176,56,36
Mvavz 180,56,52
Mvavz 184,56,68
Mvavz 188,56,84
Mvavz 192,56,100
Mvav 176,60,40
Mvav 180,60,56
Mvav 184,60,72
Mvav 188,60,88
Mvav 192,60,104
Mvav 176,64,44
Mvav 180,64,60
Mvav 184,64,76
Mvav 188,64,92
Mvav 192,64,108
Mvav 176,68,48
Mvav 180,68,64
Mvav 184,68,80
Mvav 188,68,96
Mvav 192,68,112
s_nop 7
v_max3_f32 v22,v22,v156,v157
v_max3_f32 v22,v22,v158,v159
v_max3_f32 v23,v23,v160,v161
v_max3_f32 v23,v23,v162,v163
v_max3_f32 v24,v24,v164,v165
v_max3_f32 v24,v24,v166,v167
v_max3_f32 v25,v25,v168,v169
v_max3_f32 v25,v25,v170,v171
v_max3_f32 v26,v26,v172,v173
v_max3_f32 v26,v26,v174,v175
v_max3_f32 v22,v22,v176,v177
v_max3_f32 v22,v22,v178,v179
v_max3_f32 v23,v23,v180,v181
v_max3_f32 v23,v23,v182,v183
v_max3_f32 v24,v24,v184,v185
v_max3_f32 v24,v24,v186,v187
v_max3_f32 v25,v25,v188,v189
v_max3_f32 v25,v25,v190,v191
v_max3_f32 v26,v26,v192,v193
v_max3_f32 v26,v26,v194,v195
s_add_u32 s90,s90,8192
s_sub_u32 s91,s91,1
s_cmp_lg_u32 s91,0
s_cbranch_scc1 LB_sloop0
s_barrier
s_mov_b32 m0,s93
s_nop 3
s_branch LB_back0
LB_safe1:
s_nop 7
s_mov_b32 s93,m0
s_waitcnt vmcnt(0) lgkmcnt(0)
v_mov_b32 v22,0xff800000
v_mov_b32 v23,0xff800000
v_mov_b32 v24,0xff800000
v_mov_b32 v25,0xff800000
v_mov_b32 v26,0xff800000
v_subrev_u32 v27,20480,v10
s_mov_b32 s90,262144
s_mov_b32 s91,16
LB_sloop1:
s_barrier
s_add_u32 s92,s90,131072
M0 78,0
s_nop 0
DM 19,32,90,0
M0 78,4224
s_nop 0
DM 33,32,90,0
M0 78,8448
s_nop 0
DM 19,32,92,0
M0 78,12672
s_nop 0
DM 33,32,92,0
s_waitcnt vmcnt(0)
s_barrier
RA 40,27,0
RA 44,27,64
RA 48,27,128
RA 52,27,192
RA 56,27,512
RA 60,27,576
RA 64,27,640
RA 68,27,704
WL 0
Mvavz 156,40,36
Mvavz 160,40,52
Mvavz 164,40,68
Mvavz 168,40,84
Mvavz 172,40,100
Mvav 156,44,40
Mvav 160,44,56
Mvav 164,44,72
Mvav 168,44,88
Mvav 172,44,104
Mvav 156,48,44
Mvav 160,48,60
Mvav 164,48,76
Mvav 168,48,92
Mvav 172,48,108
Mvav 156,52,48
Mvav 160,52,64
Mvav 164,52,80
Mvav 168,52,96
Mvav 172,52,112
Mvavz 176,56,36
Mvavz 180,56,52
Mvavz 184,56,68
Mvavz 188,56,84
Mvavz 192,56,100
Mvav 176,60,40
Mvav 180,60,56
Mvav 184,60,72
Mvav 188,60,88
Mvav 192,60,104
Mvav 176,64,44
Mvav 180,64,60
Mvav 184,64,76
Mvav 188,64,92
Mvav 192,64,108
Mvav 176,68,48
Mvav 180,68,64
Mvav 184,68,80
Mvav 188,68,96
Mvav 192,68,112
s_nop 7
v_max3_f32 v22,v22,v156,v157
v_max3_f32 v22,v22,v158,v159
v_max3_f32 v23,v23,v160,v161
v_max3_f32 v23,v23,v162,v163
v_max3_f32 v24,v24,v164,v165
v_max3_f32 v24,v24,v166,v167
v_max3_f32 v25,v25,v168,v169
v_max3_f32 v25,v25,v170,v171
v_max3_f32 v26,v26,v172,v173
v_max3_f32 v26,v26,v174,v175
v_max3_f32 v22,v22,v176,v177
v_max3_f32 v22,v22,v178,v179
v_max3_f32 v23,v23,v180,v181
v_max3_f32 v23,v23,v182,v183
v_max3_f32 v24,v24,v184,v185
v_max3_f32 v24,v24,v186,v187
v_max3_f32 v25,v25,v188,v189
v_max3_f32 v25,v25,v190,v191
v_max3_f32 v26,v26,v192,v193
v_max3_f32 v26,v26,v194,v195
s_add_u32 s90,s90,8192
s_sub_u32 s91,s91,1
s_cmp_lg_u32 s91,0
s_cbranch_scc1 LB_sloop1
s_barrier
s_mov_b32 m0,s93
s_nop 3
s_branch LB_back1
LB_redo:
s_waitcnt vmcnt(0) lgkmcnt(0)
s_barrier
s_mov_b32 s85,-1
s_getpc_b64 s[90:91]
LB_rp:
s_sub_u32 s90,s90,LB_rp-L_restart
s_subb_u32 s91,s91,0
s_setpc_b64 s[90:91]
LB_spin0:
s_sub_u32 s99,s99,1
s_cbranch_scc1 LB_to0
s_sleep 1
buffer_load_dwordx2 v[24:25],off,s[56:59],s44 sc1
s_waitcnt vmcnt(0)
v_cmp_lt_u64 vcc,v[24:25],s[16:17]
v_cmp_gt_u64 s[20:21],v[24:25],s[18:19]
s_nop 0
s_or_b64 s[20:21],s[20:21],vcc
s_cbranch_scc1 LB_spin0
s_branch LB_sb0
LB_to0:
s_mov_b32 s99,0
s_branch LB_sb0
LB_spin1:
s_sub_u32 s99,s99,1
s_cbranch_scc1 LB_to1
s_sleep 1
buffer_load_dwordx2 v[24:25],off,s[56:59],s44 sc1
s_waitcnt vmcnt(0)
v_cmp_lt_u64 vcc,v[24:25],s[16:17]
v_cmp_gt_u64 s[20:21],v[24:25],s[18:19]
s_nop 0
s_or_b64 s[20:21],s[20:21],vcc
s_cbranch_scc1 LB_spin1
s_branch LB_sb1
LB_to1:
s_mov_b32 s99,0
s_branch LB_sb1
LB_spin2:
s_sub_u32 s99,s99,1
s_cbranch_scc1 LB_to2
s_sleep 1
buffer_load_dwordx2 v[24:25],off,s[56:59],s44 sc1
s_waitcnt vmcnt(0)
v_cmp_lt_u64 vcc,v[24:25],s[16:17]
v_cmp_gt_u64 s[20:21],v[24:25],s[18:19]
s_nop 0
s_or_b64 s[20:21],s[20:21],vcc
s_cbranch_scc1 LB_spin2
s_branch LB_sb2
LB_to2:
s_mov_b32 s99,0
s_branch LB_sb2
LB_spin3:
s_sub_u32 s99,s99,1
s_cbranch_scc1 LB_to3
s_sleep 1
buffer_load_dwordx2 v[24:25],off,s[56:59],s44 sc1
s_waitcnt vmcnt(0)
v_cmp_lt_u64 vcc,v[24:25],s[16:17]
v_cmp_gt_u64 s[20:21],v[24:25],s[18:19]
s_nop 0
s_or_b64 s[20:21],s[20:21],vcc
s_cbranch_scc1 LB_spin3
s_branch LB_sb3
LB_to3:
s_mov_b32 s99,0
s_branch LB_sb3
LB_spin4:
s_sub_u32 s99,s99,1
s_cbranch_scc1 LB_to4
s_sleep 1
buffer_load_dwordx2 v[24:25],off,s[56:59],s44 sc1
s_waitcnt vmcnt(0)
v_cmp_lt_u64 vcc,v[24:25],s[16:17]
v_cmp_gt_u64 s[20:21],v[24:25],s[18:19]
s_nop 0
s_or_b64 s[20:21],s[20:21],vcc
s_cbranch_scc1 LB_spin4
s_branch LB_sb4
LB_to4:
s_mov_b32 s99,0
s_branch LB_sb4
LB_spin5:
s_sub_u32 s99,s99,1
s_cbranch_scc1 LB_to5
s_sleep 1
buffer_load_dwordx2 v[24:25],off,s[56:59],s44 sc1
s_waitcnt vmcnt(0)
v_cmp_lt_u64 vcc,v[24:25],s[16:17]
v_cmp_gt_u64 s[20:21],v[24:25],s[18:19]
s_nop 0
s_or_b64 s[20:21],s[20:21],vcc
s_cbranch_scc1 LB_spin5
s_branch LB_sb5
LB_to5:
s_mov_b32 s99,0
s_branch LB_sb5
LB_spin6:
s_sub_u32 s99,s99,1
s_cbranch_scc1 LB_to6
s_sleep 1
buffer_load_dwordx2 v[24:25],off,s[56:59],s45 sc1
s_waitcnt vmcnt(0)
v_cmp_lt_u64 vcc,v[24:25],s[16:17]
v_cmp_gt_u64 s[20:21],v[24:25],s[18:19]
s_nop 0
s_or_b64 s[20:21],s[20:21],vcc
s_cbranch_scc1 LB_spin6
s_branch LB_sb6
LB_to6:
s_mov_b32 s99,0
s_branch LB_sb6
LB_spin7:
s_sub_u32 s99,s99,1
s_cbranch_scc1 LB_to7
s_sleep 1
buffer_load_dwordx2 v[24:25],v26,s[56:59],s46 offen sc1
s_waitcnt vmcnt(0)
v_cmp_lt_u64 vcc,v[24:25],s[16:17]
v_cmp_gt_u64 s[20:21],v[24:25],s[18:19]
s_nop 0
s_or_b64 s[20:21],s[20:21],vcc
s_cbranch_scc1 LB_spin7
s_branch LB_sb7
LB_to7:
s_mov_b32 s99,0
s_branch LB_sb7
LB_pc39:
s_memrealtime s[94:95]
WL 0
s_sub_u32 s95,s94,s96
s_cmp_lt_i32 s95,0
s_cbranch_scc1 LB_pc39
s_branch LB_pb39
LB_pc40:
s_memrealtime s[94:95]
WL 0
s_sub_u32 s95,s94,s96
s_cmp_lt_i32 s95,0
s_cbranch_scc1 LB_pc40
s_branch LB_pb40
LB_pc41:
s_memrealtime s[94:95]
WL 0
s_sub_u32 s95,s94,s96
s_cmp_lt_i32 s95,0
s_cbranch_scc1 LB_pc41
s_branch LB_pb41
LB_pc42:
s_memrealtime s[94:95]
WL 0
s_sub_u32 s95,s94,s96
s_cmp_lt_i32 s95,0
s_cbranch_scc1 LB_pc42
s_branch LB_pb42
LB_pc43:
s_memrealtime s[94:95]
WL 0
s_sub_u32 s95,s94,s96
s_cmp_lt_i32 s95,0
s_cbranch_scc1 LB_pc43
s_branch LB_pb43
LB_pc44:
s_memrealtime s[94:95]
WL 0
s_sub_u32 s95,s94,s96
s_cmp_lt_i32 s95,0
s_cbranch_scc1 LB_pc44
s_branch LB_pb44
LB_pc45:
s_memrealtime s[94:95]
WL 0
s_sub_u32 s95,s94,s96
s_cmp_lt_i32 s95,0
s_cbranch_scc1 LB_pc45
s_branch LB_pb45
LB_pc46:
s_memrealtime s[94:95]
WL 0
s_sub_u32 s95,s94,s96
s_cmp_lt_i32 s95,0
s_cbranch_scc1 LB_pc46
s_branch LB_pb46
LB_pc47:
s_memrealtime s[94:95]
WL 0
s_sub_u32 s95,s94,s96
s_cmp_lt_i32 s95,0
s_cbranch_scc1 LB_pc47
s_branch LB_pb47
LB_pc48:
s_memrealtime s[94:95]
WL 0
s_sub_u32 s95,s94,s96
s_cmp_lt_i32 s95,0
s_cbranch_scc1 LB_pc48
s_branch LB_pb48
LB_pc49:
s_memrealtime s[94:95]
WL 0
s_sub_u32 s95,s94,s96
s_cmp_lt_i32 s95,0
s_cbranch_scc1 LB_pc49
s_branch LB_pb49
LB_pc50:
s_memrealtime s[94:95]
WL 0
s_sub_u32 s95,s94,s96
s_cmp_lt_i32 s95,0
s_cbranch_scc1 LB_pc50
s_branch LB_pb50
LB_pc51:
s_memrealtime s[94:95]
WL 0
s_sub_u32 s95,s94,s96
s_cmp_lt_i32 s95,0
s_cbranch_scc1 LB_pc51
s_branch LB_pb51
LB_pc52:
s_memrealtime s[94:95]
WL 0
s_sub_u32 s95,s94,s96
s_cmp_lt_i32 s95,0
s_cbranch_scc1 LB_pc52
s_branch LB_pb52
LB_pc53:
s_memrealtime s[94:95]
WL 0
s_sub_u32 s95,s94,s96
s_cmp_lt_i32 s95,0
s_cbranch_scc1 LB_pc53
s_branch LB_pb53
LB_pc54:
s_memrealtime s[94:95]
WL 0
s_sub_u32 s95,s94,s96
s_cmp_lt_i32 s95,0
s_cbranch_scc1 LB_pc54
s_branch LB_pb54
LB_pc55:
s_memrealtime s[94:95]
WL 0
s_sub_u32 s95,s94,s96
s_cmp_lt_i32 s95,0
s_cbranch_scc1 LB_pc55
s_branch LB_pb55
LB_pc56:
s_memrealtime s[94:95]
WL 0
s_sub_u32 s95,s94,s96
s_cmp_lt_i32 s95,0
s_cbranch_scc1 LB_pc56
s_branch LB_pb56
LB_pc57:
s_memrealtime s[94:95]
WL 0
s_sub_u32 s95,s94,s96
s_cmp_lt_i32 s95,0
s_cbranch_scc1 LB_pc57
s_branch LB_pb57
LB_pc58:
s_memrealtime s[94:95]
WL 0
s_sub_u32 s95,s94,s96
s_cmp_lt_i32 s95,0
s_cbranch_scc1 LB_pc58
s_branch LB_pb58
LB_pc59:
s_memrealtime s[94:95]
WL 0
s_sub_u32 s95,s94,s96
s_cmp_lt_i32 s95,0
s_cbranch_scc1 LB_pc59
s_branch LB_pb59
LB_pc60:
s_memrealtime s[94:95]
WL 0
s_sub_u32 s95,s94,s96
s_cmp_lt_i32 s95,0
s_cbranch_scc1 LB_pc60
s_branch LB_pb60
LB_pc61:
s_memrealtime s[94:95]
WL 0
s_sub_u32 s95,s94,s96
s_cmp_lt_i32 s95,0
s_cbranch_scc1 LB_pc61
s_branch LB_pb61
LB_pc62:
s_memrealtime s[94:95]
WL 0
s_sub_u32 s95,s94,s96
s_cmp_lt_i32 s95,0
s_cbranch_scc1 LB_pc62
s_branch LB_pb62
LB_pc63:
s_memrealtime s[94:95]
WL 0
s_sub_u32 s95,s94,s96
s_cmp_lt_i32 s95,0
s_cbranch_scc1 LB_pc63
s_branch LB_pb63
LB_pc64:
s_memrealtime s[94:95]
WL 0
s_sub_u32 s95,s94,s96
s_cmp_lt_i32 s95,0
s_cbranch_scc1 LB_pc64
s_branch LB_pb64
LB_pc65:
s_memrealtime s[94:95]
WL 0
s_sub_u32 s95,s94,s96
s_cmp_lt_i32 s95,0
s_cbranch_scc1 LB_pc65
s_branch LB_pb65
LB_pc66:
s_memrealtime s[94:95]
WL 0
s_sub_u32 s95,s94,s96
s_cmp_lt_i32 s95,0
s_cbranch_scc1 LB_pc66
s_branch LB_pb66
LB_pc67:
s_memrealtime s[94:95]
WL 0
s_sub_u32 s95,s94,s96
s_cmp_lt_i32 s95,0
s_cbranch_scc1 LB_pc67
s_branch LB_pb67
L_end:
)ASM"
    :: "s"(__builtin_amdgcn_kernarg_segment_ptr()), "s"(blockIdx.x), "v"(threadIdx.x), "s"((unsigned)(size_t)lds)
    : "v1","v2","v3","v4","v5","v6","v7","v8","v9","v10","v11","v12","v13","v14","v15","v16","v17","v18","v19","v20","v21","v22","v23","v24","v25","v26","v27","v28","v29","v30","v31","v32","v33","v34","v35","v36","v37","v38","v39","v40","v41","v42","v43","v44","v45","v46","v47","v48","v49","v50","v51","v52","v53","v54","v55","v56","v57","v58","v59","v60","v61","v62","v63","v64","v65","v66","v67","v68","v69","v70","v71","v72","v73","v74","v75","v76","v77","v78","v79","v80","v81","v82","v83","v84","v85","v86","v87","v88","v89","v90","v91","v92","v93","v94","v95","v96","v97","v98","v99","v100","v101","v102","v103","v104","v105","v106","v107","v108","v109","v110","v111","v112","v113","v114","v115","v116","v117","v118","v119","v120","v121","v122","v123","v124","v125","v126","v127","v128","v129","v130","v131","v132","v133","v134","v135","v136","v137","v138","v139","v140","v141","v142","v143","v144","v145","v146","v147","v148","v149","v150","v151","v152","v153","v154","v155","v156","v157","v158","v159","v160","v161","v162","v163","v164","v165","v166","v167","v168","v169","v170","v171","v172","v173","v174","v175","v176","v177","v178","v179","v180","v181","v182","v183","v184","v185","v186","v187","v188","v189","v190","v191","v192","v193","v194","v195","v196","v197","v198","v199","v200","v201","v202","v203","v204","v205","v206","v207","v208","v209","v210","v211","v212","v213","v214","v215","v216","v217","v218","v219","v220","v221","v222","v223","v224","v225","v226","v227","v228","v229","v230","v231","v232","v233","v234","v235","v236","v237","v238","v239","v240","v241","v242","v243","v244","v245","v246","v247","v248","v249","v250","v251","v252","v253","v254","v255","a0","a1","a2","a3","a4","a5","a6","a7","a8","a9","a10","a11","a12","a13","a14","a15","a16","a17","a18","a19","a20","a21","a22","a23","a24","a25","a26","a27","a28","a29","a30","a31","a32","a33","a34","a35","a36","a37","a38","a39","a40","a41","a42","a43","a44","a45","a46","a47","a48","a49","a50","a51","a52","a53","a54","a55","a56","a57","a58","a59","a60","a61","a62","a63","a64","a65","a66","a67","a68","a69","a70","a71","a72","a73","a74","a75","a76","a77","a78","a79","a80","a81","a82","a83","a84","a85","a86","a87","a88","a89","a90","a91","a92","a93","a94","a95","a96","a97","a98","a99","a100","a101","a102","a103","a104","a105","a106","a107","a108","a109","a110","a111","a112","a113","a114","a115","a116","a117","a118","a119","a120","a121","a122","a123","a124","a125","a126","a127","a128","a129","a130","a131","a132","a133","a134","a135","a136","a137","a138","a139","a140","a141","a142","a143","a144","a145","a146","a147","a148","a149","a150","a151","a152","a153","a154","a155","a156","a157","a158","a159","a160","a161","a162","a163","a164","a165","a166","a167","a168","a169","a170","a171","a172","a173","a174","a175","a176","a177","a178","a179","a180","a181","a182","a183","a184","a185","a186","a187","a188","a189","a190","a191","a192","a193","a194","a195","a196","a197","a198","a199","a200","a201","a202","a203","a204","a205","a206","a207","a208","a209","a210","a211","a212","a213","a214","a215","a216","a217","a218","a219","a220","a221","a222","a223","a224","a225","a226","a227","a228","a229","a230","a231","a232","a233","a234","a235","a236","a237","a238","a239","a240","a241","a242","a243","a244","a245","a246","a247","a248","a249","a250","a251","a252","a253","a254","a255","s4","s5","s6","s7","s8","s9","s10","s11","s12","s13","s14","s15","s16","s17","s18","s19","s20","s21","s22","s23","s24","s25","s26","s27","s28","s29","s30","s31","s32","s33","s34","s35","s36","s37","s38","s39","s40","s41","s42","s43","s44","s45","s46","s47","s48","s49","s50","s51","s52","s53","s54","s55","s56","s57","s58","s59","s60","s61","s62","s63","s64","s65","s66","s67","s68","s69","s70","s71","s72","s73","s74","s75","s76","s77","s78","s79","s80","s81","s82","s83","s84","s85","s86","s87","s88","s89","s90","s91","s92","s93","s94","s95","s96","s97","s98","s99","s100","s101","vcc","memory");
}

extern "C" void kernel_launch(void* const* d_in, const int* in_sizes, int n_in, void* d_out, int out_size, void* d_ws,
                              size_t ws_size, hipStream_t stream) {
  mha_block_fused<<<256, 256, 0, stream>>>(d_in[0], d_in[1], d_in[2], d_in[3], d_in[4], d_out, d_ws);
}
```
